```python
import torch

def _softmax(x, axis=-1):
    return torch.softmax(x, dim=axis)
def _gelu(x):
    return torch.nn.functional.gelu(x, approximate="tanh")
def _silu(x):
    return torch.nn.functional.silu(x)
def _sigmoid(x):
    return torch.sigmoid(x)
def _layernorm(x, axis=-1, eps=1e-5):
    m = x.mean(dim=axis, keepdim=True)
    v = x.var(dim=axis, keepdim=True, unbiased=False)
    return (x - m) / torch.sqrt(v + eps)
def _scatter_add(tgt, idx, src, axis=0):
    return tgt.clone().index_add_(axis, idx, src)
def _segment_sum(src, seg, n):
    out = torch.zeros((n,) + src.shape[1:], dtype=src.dtype)
    return out.index_add_(0, seg, src)
def _pad(x, pw):
    flat = tuple(v for pair in reversed(pw) for v in pair)
    return torch.nn.functional.pad(x, flat)

kernel_name = 'sequential_preLN__gqa_block_cached__dense_activation_squared_relu__decode'

def setup_inputs():

    _w = (lambda a: (lambda t: t.to(torch.bfloat16) if t.is_floating_point() else t)(torch.as_tensor(a)))
    def _A_setup():
        B, Sq, D = 8, 1, 448
        Hq, Hkv, Sk, Dh = 16, 8, 4096, 256
        assert Hq % Hkv == 0, (Hq, Hkv)
        return {
            "x": _w((lambda *s: torch.randn(*s, dtype=torch.bfloat16) * 0.05)(B, Sq, D)),
            "k_cache": _w((lambda *s: torch.randn(*s, dtype=torch.bfloat16) * 3.2)(B, Hkv, Sk, Dh)),
            "v_cache": _w((lambda *s: torch.randn(*s, dtype=torch.bfloat16) * 0.4)(B, Hkv, Sk, Dh)),
            "wq": _w((lambda *s: torch.randn(*s, dtype=torch.bfloat16) * 3.2)(D, Hq * Dh) * (D ** -0.5)),
            "wo": _w((lambda *s: torch.randn(*s, dtype=torch.bfloat16) * 0.4)(Hq * Dh, D) * ((Hq * Dh) ** -0.5)),
        }
    _A_in = _A_setup()

    def _B_setup():
        B, S, D, F = 8, 1, 448, 1792
        return {
            "x": _w((lambda *s: torch.randn(*s, dtype=torch.bfloat16) * 0.05)(B, S, D)),
            "w1": _w((lambda *s: torch.randn(*s, dtype=torch.bfloat16) * 0.05)(D, F)),
            "b1": _w((lambda *s: torch.randn(*s, dtype=torch.bfloat16) * 0.8)(F)),
            "w2": _w((lambda *s: torch.randn(*s, dtype=torch.bfloat16) * 0.05)(F, D)),
            "b2": _w((lambda *s: torch.randn(*s, dtype=torch.bfloat16) * 3.2)(D)),
        }
    _B_in = _B_setup()

    return {
        "A_x": _A_in["x"],
        "A_k_cache": _A_in["k_cache"],
        "A_v_cache": _A_in["v_cache"],
        "A_wq": _A_in["wq"],
        "A_wo": _A_in["wo"],
        "B_x": _B_in["x"],
        "B_w1": _B_in["w1"],
        "B_b1": _B_in["b1"],
        "B_w2": _B_in["w2"],
        "B_b2": _B_in["b2"],
    }

def reference(A_x, A_k_cache, A_v_cache, A_wq, A_wo, B_x, B_w1, B_b1, B_w2, B_b2):
    def _A_ref(x, k_cache, v_cache, wq, wo):
        B, Sq, D = x.shape
        Hq, Dh = 16, 256
        Hkv = k_cache.shape[1]
        rep = Hq // Hkv
        q = (lambda x, *d: x.permute(*d))((x @ wq).reshape(B, Sq, Hq, Dh), 0, 2, 1, 3)
        k = (lambda x, n, axis: torch.repeat_interleave(x, n, dim=axis))(k_cache, rep, axis=1)
        v = (lambda x, n, axis: torch.repeat_interleave(x, n, dim=axis))(v_cache, rep, axis=1)
        scores = torch.einsum("bhqd,bhkd->bhqk", q, k) * (Dh ** -0.5)
        p = _softmax(scores, axis=-1)
        o = torch.einsum("bhqk,bhkd->bhqd", p, v)
        return (lambda x, *d: x.permute(*d))(o, 0, 2, 1, 3).reshape(B, Sq, Hq * Dh) @ wo

    def _B_ref(x, w1, b1, w2, b2):
        h = torch.einsum("bsd,df->bsf", x, w1) + b1
        zero = torch.zeros_like(h)
        r = torch.where(h > zero, h, zero)
        h = r * r
        out = torch.einsum("bsf,fd->bsd", h, w2) + b2
        return out

    x = A_x
    h = x + _A_ref(_layernorm(x, axis=-1), A_k_cache, A_v_cache, A_wq, A_wo)
    h2 = h + _B_ref(_layernorm(h, axis=-1), B_w1, B_b1, B_w2, B_b2)
    return h2
```

```cpp
#include <hip/hip_runtime.h>
#include <hip/hip_bf16.h>
#include <stdint.h>

typedef unsigned short u16;
typedef unsigned v4u __attribute__((ext_vector_type(4)));
typedef __bf16 bf16x2 __attribute__((ext_vector_type(2)));

constexpr int B = 8, D = 448, HQ = 16, HKV = 8, SK = 4096, DH = 256, F = 1792;
constexpr int NSPLIT = 8;
constexpr int TOK = SK / NSPLIT;

__device__ __forceinline__ float bf2f(u16 v) { return __builtin_bit_cast(float, (unsigned)v << 16); }
__device__ __forceinline__ float rbf(float f) {
  __bf16 b = (__bf16)f;
  return (float)b;
}
__device__ __forceinline__ u16 f2bf(float f) { __bf16 b = (__bf16)f; return __builtin_bit_cast(u16, b); }
__device__ __forceinline__ float dot2(unsigned a, unsigned b, float c) {
  return __builtin_amdgcn_fdot2_f32_bf16(__builtin_bit_cast(bf16x2, a), __builtin_bit_cast(bf16x2, b), c, false);
}
__device__ __forceinline__ float wave_sum(float v) {
#pragma unroll
  for (int m = 32; m >= 1; m >>= 1) v += __shfl_xor(v, m);
  return v;
}
__device__ __forceinline__ float wave_max(float v) {
#pragma unroll
  for (int m = 32; m >= 1; m >>= 1) v = fmaxf(v, __shfl_xor(v, m));
  return v;
}

__device__ __forceinline__ void ln_row(float (&v)[7]) {
  float s = 0.f;
#pragma unroll
  for (int i = 0; i < 7; ++i) s += v[i];
  s = wave_sum(s);
  const float mean = s * (1.0f / 448.0f);
  float q = 0.f;
#pragma unroll
  for (int i = 0; i < 7; ++i) { float d = v[i] - mean; q += d * d; }
  q = wave_sum(q);
  const float var = rbf(q * (1.0f / 448.0f));
  const float mb = rbf(mean);
  const float den = rbf(sqrtf(rbf(var + 1e-5f)));
#pragma unroll
  for (int i = 0; i < 7; ++i) v[i] = rbf(rbf(v[i] - mb) / den);
}

__global__ void __launch_bounds__(512) k_q(const u16* x, const u16* wq, u16* q) {
  __shared__ float ln[B][D];
  __shared__ float red[8][B][64];
  const int tid = threadIdx.x, lane = tid & 63, w = tid >> 6;
  {
    float v[7];
#pragma unroll
    for (int i = 0; i < 7; ++i) v[i] = bf2f(x[w * D + lane + 64 * i]);
    ln_row(v);
#pragma unroll
    for (int i = 0; i < 7; ++i) ln[w][lane + 64 * i] = v[i];
  }
  __syncthreads();
  const int n = blockIdx.x * 64 + lane;
  float acc[B];
#pragma unroll
  for (int b = 0; b < B; ++b) acc[b] = 0.f;
  for (int k = w * 56; k < w * 56 + 56; ++k) {
    const float wv = bf2f(wq[(size_t)k * (HQ * DH) + n]);
#pragma unroll
    for (int b = 0; b < B; ++b) acc[b] = fmaf(ln[b][k], wv, acc[b]);
  }
#pragma unroll
  for (int b = 0; b < B; ++b) red[w][b][lane] = acc[b];
  __syncthreads();
  float s = 0.f;
#pragma unroll
  for (int ww = 0; ww < 8; ++ww) s += red[ww][w][lane];
  q[w * (HQ * DH) + n] = f2bf(s);
}

__global__ void __launch_bounds__(256) k_attn(const u16* q, const u16* kc, const u16* vc, float* pml, float* po) {
  __shared__ float sc[2][TOK];
  __shared__ float red[4][2][DH];
  __shared__ float wm[4][2];
  const int s = blockIdx.x, kvh = blockIdx.y, b = blockIdx.z;
  const int tid = threadIdx.x, lane = tid & 63, w = tid >> 6, c = lane & 31, hf = lane >> 5;
  const v4u q0 = *(const v4u*)(q + (size_t)b * 4096 + (kvh * 2) * DH + 8 * c);
  const v4u q1 = *(const v4u*)(q + (size_t)b * 4096 + (kvh * 2 + 1) * DH + 8 * c);
  const size_t base = ((size_t)(b * HKV + kvh) * SK + (size_t)s * TOK) * DH;
  const v4u* kp = (const v4u*)(kc + base) + (size_t)w * 128 * 32 + lane;
  for (int i0 = 0; i0 < 64; i0 += 8) {
    v4u kk[8];
#pragma unroll
    for (int i = 0; i < 8; ++i) kk[i] = __builtin_nontemporal_load(kp + (size_t)(i0 + i) * 64);
#pragma unroll
    for (int i = 0; i < 8; ++i) {
      float a0 = 0.f, a1 = 0.f;
#pragma unroll
      for (int j = 0; j < 4; ++j) { a0 = dot2(kk[i][j], q0[j], a0); a1 = dot2(kk[i][j], q1[j], a1); }
#pragma unroll
      for (int m = 16; m >= 1; m >>= 1) { a0 += __shfl_xor(a0, m); a1 += __shfl_xor(a1, m); }
      if (c == 0) { const int t = w * 128 + (i0 + i) * 2 + hf; sc[0][t] = a0 * 0.0625f; sc[1][t] = a1 * 0.0625f; }
    }
  }
  __syncthreads();
  float m0 = fmaxf(sc[0][tid], sc[0][tid + 256]), m1 = fmaxf(sc[1][tid], sc[1][tid + 256]);
  m0 = wave_max(m0); m1 = wave_max(m1);
  if (lane == 0) { wm[w][0] = m0; wm[w][1] = m1; }
  __syncthreads();
  m0 = fmaxf(fmaxf(wm[0][0], wm[1][0]), fmaxf(wm[2][0], wm[3][0]));
  m1 = fmaxf(fmaxf(wm[0][1], wm[1][1]), fmaxf(wm[2][1], wm[3][1]));
  __syncthreads();
  float l0 = 0.f, l1 = 0.f;
#pragma unroll
  for (int r = 0; r < 2; ++r) {
    const float p0 = __expf(sc[0][tid + 256 * r] - m0), p1 = __expf(sc[1][tid + 256 * r] - m1);
    sc[0][tid + 256 * r] = p0; sc[1][tid + 256 * r] = p1; l0 += p0; l1 += p1;
  }
  l0 = wave_sum(l0); l1 = wave_sum(l1);
  if (lane == 0) { wm[w][0] = l0; wm[w][1] = l1; }
  __syncthreads();
  l0 = (wm[0][0] + wm[1][0]) + (wm[2][0] + wm[3][0]);
  l1 = (wm[0][1] + wm[1][1]) + (wm[2][1] + wm[3][1]);
  const v4u* vp = (const v4u*)(vc + base) + (size_t)w * 128 * 32 + lane;
  float acc[2][8];
#pragma unroll
  for (int j = 0; j < 8; ++j) { acc[0][j] = 0.f; acc[1][j] = 0.f; }
  for (int i0 = 0; i0 < 64; i0 += 8) {
    v4u vv[8];
#pragma unroll
    for (int i = 0; i < 8; ++i) vv[i] = __builtin_nontemporal_load(vp + (size_t)(i0 + i) * 64);
#pragma unroll
    for (int i = 0; i < 8; ++i) {
      const int t = w * 128 + (i0 + i) * 2 + hf;
      const float p0 = sc[0][t], p1 = sc[1][t];
#pragma unroll
      for (int j = 0; j < 4; ++j) {
        const float lo = __builtin_bit_cast(float, vv[i][j] << 16), hi = __builtin_bit_cast(float, vv[i][j] & 0xffff0000u);
        acc[0][2 * j] = fmaf(p0, lo, acc[0][2 * j]); acc[0][2 * j + 1] = fmaf(p0, hi, acc[0][2 * j + 1]);
        acc[1][2 * j] = fmaf(p1, lo, acc[1][2 * j]); acc[1][2 * j + 1] = fmaf(p1, hi, acc[1][2 * j + 1]);
      }
    }
  }
#pragma unroll
  for (int j = 0; j < 8; ++j) { acc[0][j] += __shfl_xor(acc[0][j], 32); acc[1][j] += __shfl_xor(acc[1][j], 32); }
  if (hf == 0) {
#pragma unroll
    for (int j = 0; j < 8; ++j) { red[w][0][8 * c + j] = acc[0][j]; red[w][1][8 * c + j] = acc[1][j]; }
  }
  __syncthreads();
#pragma unroll
  for (int h = 0; h < 2; ++h) {
    const float o = (red[0][h][tid] + red[1][h][tid]) + (red[2][h][tid] + red[3][h][tid]);
    const size_t slot = ((size_t)(b * HQ + kvh * 2 + h) * NSPLIT + s);
    po[slot * DH + tid] = o;
    if (tid == 0) { pml[slot * 2] = h ? m1 : m0; pml[slot * 2 + 1] = h ? l1 : l0; }
  }
}

__global__ void __launch_bounds__(512) k_wo(const float* pml, const float* po, const u16* wo, float* part) {
  __shared__ float o[B][DH];
  __shared__ float red[8][B][64];
  const int h = blockIdx.x, cg = blockIdx.y, tid = threadIdx.x, lane = tid & 63, w = tid >> 6;
  {
    const int b = w;
    const size_t row = (size_t)(b * HQ + h) * NSPLIT;
    float M = -INFINITY;
    for (int s = 0; s < NSPLIT; ++s) M = fmaxf(M, pml[(row + s) * 2]);
    float L = 0.f, a[4] = {0.f, 0.f, 0.f, 0.f};
    for (int s = 0; s < NSPLIT; ++s) {
      const float wt = __expf(pml[(row + s) * 2] - M);
      L += wt * pml[(row + s) * 2 + 1];
#pragma unroll
      for (int j = 0; j < 4; ++j) a[j] += wt * po[(row + s) * DH + lane + 64 * j];
    }
#pragma unroll
    for (int j = 0; j < 4; ++j) o[b][lane + 64 * j] = rbf(a[j] / L);
  }
  __syncthreads();
  const int n = cg * 64 + lane;
  float acc[B];
#pragma unroll
  for (int b = 0; b < B; ++b) acc[b] = 0.f;
  for (int k = w * 32; k < w * 32 + 32; ++k) {
    const float wv = bf2f(wo[(size_t)(h * DH + k) * D + n]);
#pragma unroll
    for (int b = 0; b < B; ++b) acc[b] = fmaf(o[b][k], wv, acc[b]);
  }
#pragma unroll
  for (int b = 0; b < B; ++b) red[w][b][lane] = acc[b];
  __syncthreads();
  float s = 0.f;
#pragma unroll
  for (int ww = 0; ww < 8; ++ww) s += red[ww][w][lane];
  part[((size_t)h * B + w) * D + n] = s;
}

__global__ void __launch_bounds__(512) k_w1(const u16* x, const float* part, const u16* w1, const u16* b1, float* hout, float* h1) {
  __shared__ float ln[B][D];
  __shared__ float red[8][B][64];
  const int tid = threadIdx.x, lane = tid & 63, w = tid >> 6;
  {
    float v[7];
#pragma unroll
    for (int i = 0; i < 7; ++i) {
      float a = 0.f;
      for (int h = 0; h < HQ; ++h) a += part[((size_t)h * B + w) * D + lane + 64 * i];
      v[i] = rbf(bf2f(x[w * D + lane + 64 * i]) + rbf(a));
      if (blockIdx.x == 0) hout[w * D + lane + 64 * i] = v[i];
    }
    ln_row(v);
#pragma unroll
    for (int i = 0; i < 7; ++i) ln[w][lane + 64 * i] = v[i];
  }
  __syncthreads();
  const int n = blockIdx.x * 64 + lane;
  float acc[B];
#pragma unroll
  for (int b = 0; b < B; ++b) acc[b] = 0.f;
  for (int k = w * 56; k < w * 56 + 56; ++k) {
    const float wv = bf2f(w1[(size_t)k * F + n]);
#pragma unroll
    for (int b = 0; b < B; ++b) acc[b] = fmaf(ln[b][k], wv, acc[b]);
  }
#pragma unroll
  for (int b = 0; b < B; ++b) red[w][b][lane] = acc[b];
  __syncthreads();
  float s = 0.f;
#pragma unroll
  for (int ww = 0; ww < 8; ++ww) s += red[ww][w][lane];
  float hv = rbf(rbf(s) + bf2f(b1[n]));
  hv = hv > 0.f ? hv : 0.f;
  h1[w * F + n] = rbf(hv * hv);
}

__global__ void __launch_bounds__(512) k_w2(const float* hin, const float* h1, const u16* w2, const u16* b2, u16* out) {
  __shared__ float red[8][B][64];
  const int tid = threadIdx.x, lane = tid & 63, w = tid >> 6;
  const int n = blockIdx.x * 64 + lane;
  float acc[B];
#pragma unroll
  for (int b = 0; b < B; ++b) acc[b] = 0.f;
  for (int k = w * 224; k < w * 224 + 224; ++k) {
    const float wv = bf2f(w2[(size_t)k * D + n]);
#pragma unroll
    for (int b = 0; b < B; ++b) acc[b] = fmaf(h1[b * F + k], wv, acc[b]);
  }
#pragma unroll
  for (int b = 0; b < B; ++b) red[w][b][lane] = acc[b];
  __syncthreads();
  float s = 0.f;
#pragma unroll
  for (int ww = 0; ww < 8; ++ww) s += red[ww][w][lane];
  const float o = rbf(rbf(s) + bf2f(b2[n]));
  out[w * D + n] = f2bf(hin[w * D + n] + o);
}

extern "C" void kernel_launch(void* const* d_in, const int* in_sizes, int n_in, void* d_out, int out_size, void* d_ws,
                              size_t ws_size, hipStream_t stream) {
  const u16* x = (const u16*)d_in[0];
  const u16* kc = (const u16*)d_in[1];
  const u16* vc = (const u16*)d_in[2];
  const u16* wq = (const u16*)d_in[3];
  const u16* wo = (const u16*)d_in[4];
  const u16* w1 = (const u16*)d_in[6];
  const u16* b1 = (const u16*)d_in[7];
  const u16* w2 = (const u16*)d_in[8];
  const u16* b2 = (const u16*)d_in[9];
  char* ws = (char*)d_ws;
  u16* q = (u16*)ws;
  float* pml = (float*)(ws + 65536);
  float* po = (float*)(ws + 131072);
  float* part = (float*)(ws + 131072 + (1 << 20));
  float* hbuf = (float*)(ws + 131072 + (2 << 20));
  float* h1 = (float*)(ws + 131072 + (3 << 20));
  k_q<<<64, 512, 0, stream>>>(x, wq, q);
  k_attn<<<dim3(NSPLIT, HKV, B), 256, 0, stream>>>(q, kc, vc, pml, po);
  k_wo<<<dim3(16, 7), 512, 0, stream>>>(pml, po, wo, part);
  k_w1<<<28, 512, 0, stream>>>(x, part, w1, b1, hbuf, h1);
  k_w2<<<7, 512, 0, stream>>>(hbuf, h1, w2, b2, (u16*)d_out);
}
```

```cpp
#include <hip/hip_runtime.h>
#include <hip/hip_bf16.h>
#include <stdint.h>

#define DBG_STAMPS 0
#define W_AUX 2
#define Q_AUX 2
#define NEXTRA 2
#define RING 4
#define PACE 2
#define DRYRUN 1
#define PRETOUCH 1
#define TAU 0
#define T1 0
#define NOSTAGE 0
#define NOWQ 0
#define NORESET 0
#define QSLEEP 0
#define LEAD 2
#define PURE 0
#define HSTOP 0
#define HPRIO 3

namespace fk {
typedef unsigned short u16;
typedef unsigned long long u64;
typedef unsigned v4u __attribute__((ext_vector_type(4)));
typedef unsigned v2u __attribute__((ext_vector_type(2)));
typedef float v4f __attribute__((ext_vector_type(4)));
typedef float v2f __attribute__((ext_vector_type(2)));
typedef __bf16 bf16x2 __attribute__((ext_vector_type(2)));
typedef __bf16 bf16x8 __attribute__((ext_vector_type(8)));
typedef short s16x4 __attribute__((ext_vector_type(4)));
#define LAS __attribute__((address_space(3)))
#define GAS __attribute__((address_space(1)))

constexpr int D = 448, F = 1792, SK = 4096, DH = 256, NQ = 4096;
constexpr unsigned SENT = 0xFFFFFFFFu;
constexpr int AUX_NT = 2, AUX_SC1 = 16;
constexpr int RSRC3 = 0x00020000;
constexpr float SC2 = 0.0625f * 1.4426950408889634f;
constexpr unsigned SPIN_CAP = 1u << 16;

constexpr int WS_EPOCH = 0;
constexpr int WS_G = 4096;
constexpr int WS_ML = WS_G + 8 * 8 * 512 * 8;
constexpr int WS_P0 = WS_ML + 64 * 16 * 16;
constexpr int WS_PW = WS_P0 + 64 * 4 * 512 * 4;
constexpr int WS_H = WS_PW + 8 * 7 * 16 * 256;
constexpr int WS_PF = WS_H + 8192;
constexpr int WS_END = WS_PF + 8 * 28 * 112 * 64;
constexpr int WS_DBG = WS_END;

constexpr int LN_STR = 912;
constexpr int L_P = 0;
constexpr int L_MRG = 8192;
constexpr int L_Q = 16384;
constexpr int L_FLAG = 17408;
constexpr int L_QP = L_FLAG + 64;
constexpr int L_LN = L_QP + 2048;
constexpr int L_BF = L_LN + 8 * LN_STR;
constexpr int L_SINK = L_BF + 8192;
constexpr int L_RAW = L_SINK + 2048;
constexpr int L_END = 96 * 1024;
static_assert(L_RAW + 32768 <= L_END, "lds");
static_assert(L_QP % 16 == 0 && L_LN % 16 == 0 && L_BF % 16 == 0 && L_RAW % 16 == 0, "align");
enum { FL_LN = 0, FL_QP = 1, FL_Q = 2, FL_PRE = 3, FL_DONE = 4, FL_RAW = 5, FL_S1 = 6, FL_S2 = 7, FL_OK = 8, FL_ML = 9 };
constexpr int L_ML = L_QP;

__device__ __forceinline__ float bflo(unsigned v) { return __builtin_bit_cast(float, v << 16); }
__device__ __forceinline__ float bfhi(unsigned v) { return __builtin_bit_cast(float, v & 0xffff0000u); }
__device__ __forceinline__ float rbf(float f) { return (float)(__bf16)f; }
__device__ __forceinline__ unsigned pk2(float lo, float hi) {
  bf16x2 v = {(__bf16)lo, (__bf16)hi};
  return __builtin_bit_cast(unsigned, v);
}
__device__ __forceinline__ float dot2(unsigned a, unsigned b, float c) {
  return __builtin_amdgcn_fdot2_f32_bf16(__builtin_bit_cast(bf16x2, a), __builtin_bit_cast(bf16x2, b), c, false);
}
__device__ __forceinline__ v4f mfma(v4u a, v4u b, v4f c) {
  return __builtin_amdgcn_mfma_f32_16x16x32_bf16(__builtin_bit_cast(bf16x8, a), __builtin_bit_cast(bf16x8, b), c, 0, 0, 0);
}
template <int CTRL> __device__ __forceinline__ float dpp(float x) {
  return __builtin_bit_cast(float, __builtin_amdgcn_mov_dpp(__builtin_bit_cast(int, x), CTRL, 0xf, 0xf, true));
}
constexpr int XOR1 = 0xB1, XOR2 = 0x4E, HMIR = 0x141, ROR4 = 0x124, ROR8 = 0x128;
__device__ __forceinline__ float sw16_add(float a, float b) {
  auto s = __builtin_amdgcn_permlane16_swap(__float_as_uint(a), __float_as_uint(b), false, false);
  return __uint_as_float(s[0]) + __uint_as_float(s[1]);
}
__device__ __forceinline__ float sw32_add(float a, float b) {
  auto s = __builtin_amdgcn_permlane32_swap(__float_as_uint(a), __float_as_uint(b), false, false);
  return __uint_as_float(s[0]) + __uint_as_float(s[1]);
}
__device__ __forceinline__ float sw16_max(float a) {
  auto s = __builtin_amdgcn_permlane16_swap(__float_as_uint(a), __float_as_uint(a), false, false);
  return fmaxf(__uint_as_float(s[0]), __uint_as_float(s[1]));
}
__device__ __forceinline__ float sw32_max(float a) {
  auto s = __builtin_amdgcn_permlane32_swap(__float_as_uint(a), __float_as_uint(a), false, false);
  return fmaxf(__uint_as_float(s[0]), __uint_as_float(s[1]));
}
__device__ __forceinline__ unsigned canonu(unsigned u) { return u == SENT ? 0x7FC07FC0u : u; }
__device__ __forceinline__ unsigned canon(float f) { return canonu(__builtin_bit_cast(unsigned, f)); }
__device__ __forceinline__ v4u canon4(v4f v) { return v4u{canon(v.x), canon(v.y), canon(v.z), canon(v.w)}; }
__device__ __forceinline__ unsigned umax(unsigned a, unsigned b) { return a > b ? a : b; }
__device__ __forceinline__ unsigned max4(v4u v) { return umax(umax(v.x, v.y), umax(v.z, v.w)); }
__device__ __forceinline__ v4f asf(v4u v) { return __builtin_bit_cast(v4f, v); }
#define LGKM0() asm volatile("s_waitcnt lgkmcnt(0)" ::: "memory")
#define VM0() asm volatile("s_waitcnt vmcnt(0)" ::: "memory")
#define CLOB() asm volatile("" ::: "memory")
#define SB() __builtin_amdgcn_sched_barrier(0)
#define BAR() do { asm volatile("s_waitcnt lgkmcnt(0)" ::: "memory"); __builtin_amdgcn_s_barrier(); asm volatile("" ::: "memory"); __builtin_amdgcn_sched_barrier(0); } while (0)
#define LD128(rs, vo, so, aux) __builtin_amdgcn_raw_buffer_load_b128(rs, vo, so, aux)
#define ST128(v, rs, vo, so, aux) __builtin_amdgcn_raw_buffer_store_b128(v, rs, vo, so, aux)
#define STR2(x) #x
#define STR(x) STR2(x)

#define STAMP(i) do {} while (0)
#define STAMPR(i) do {} while (0)

__device__ __forceinline__ void lds_wait_ge(volatile LAS unsigned* f, unsigned want, int slp) {
#pragma nounroll
  for (unsigned sp = 0; sp < (SPIN_CAP << 4); ++sp) {
    if (*f >= want) break;
    if (slp) __builtin_amdgcn_s_sleep(8);
  }
  CLOB();
}
__device__ __forceinline__ void lds_signal(LAS unsigned char* L, int word, int lane, unsigned inc) {
  LGKM0();
  LAS unsigned* p = (LAS unsigned*)(L + (lane == 0 ? L_FLAG + word * 4 : L_SINK + lane * 4));
  __hip_atomic_fetch_add(p, inc, __ATOMIC_RELAXED, __HIP_MEMORY_SCOPE_WORKGROUP);
}

__device__ __forceinline__ void pace(unsigned tb, int n) {
  if (TAU) {
    const unsigned until = tb + (unsigned)(((n < 64 ? n : 0) * TAU) >> 4);
    asm volatile("8:\n\ts_memrealtime vcc\n\ts_waitcnt lgkmcnt(0)\n\ts_sub_i32 vcc_lo, vcc_lo, %0\n\ts_cmp_lt_i32 vcc_lo, 0\n\ts_cbranch_scc1 8b\n" :: "s"(until) : "scc", "vcc", "memory");
  }
}
__device__ __forceinline__ void issue_turn(v4u* r, __amdgpu_buffer_rsrc_t rs, int voff, int soff) {
#pragma unroll
  for (int i = 0; i < 4; ++i) r[i] = LD128(rs, voff + i * 1024, soff, AUX_NT);
  SB();
}
__device__ __forceinline__ float k_turn(const v4u* k, v4u q0, v4u q1, bool b0, bool b1, bool b2) {
  if (PURE == 1 || PURE == 3) { const v4u t = (k[0] ^ k[1]) ^ (k[2] ^ k[3]); return __uint_as_float((t.x ^ t.y ^ t.z ^ t.w) & 0x3fffffffu); }
  float x[8];
#pragma unroll
  for (int i = 0; i < 4; ++i) {
    float a0 = dot2(k[i].x, q0.x, 0.f), a1 = dot2(k[i].x, q1.x, 0.f);
    a0 = dot2(k[i].y, q0.y, a0); a1 = dot2(k[i].y, q1.y, a1);
    a0 = dot2(k[i].z, q0.z, a0); a1 = dot2(k[i].z, q1.z, a1);
    a0 = dot2(k[i].w, q0.w, a0); a1 = dot2(k[i].w, q1.w, a1);
    x[2 * i] = a0; x[2 * i + 1] = a1;
  }
  float y[4], z[2];
#pragma unroll
  for (int v = 0; v < 4; ++v) y[v] = (b2 ? x[v + 4] : x[v]) + dpp<HMIR>(b2 ? x[v] : x[v + 4]);
#pragma unroll
  for (int v = 0; v < 2; ++v) z[v] = (b1 ? y[v + 2] : y[v]) + dpp<XOR2>(b1 ? y[v] : y[v + 2]);
  return (b0 ? z[1] : z[0]) + dpp<XOR1>(b0 ? z[0] : z[1]);
}
__device__ __forceinline__ float k_group(float r0, float r1, float r2, float r3, bool b3) {
  const float u0 = (b3 ? r1 : r0) + dpp<ROR8>(b3 ? r0 : r1);
  const float u1 = (b3 ? r3 : r2) + dpp<ROR8>(b3 ? r2 : r3);
  return rbf(sw16_add(u0, u1));
}
__device__ __forceinline__ void v_turn(const v4u* v, v4f pa, v4f pb, v2f (&a0)[4], v2f (&a1)[4]) {
  if (PURE == 1 || PURE == 3) { const v4u t = (v[0] ^ v[1]) ^ (v[2] ^ v[3]); a0[0].x += __uint_as_float((t.x ^ t.y ^ t.z ^ t.w) & 0x3fffffffu) * pa.x; return; }
#pragma unroll
  for (int i = 0; i < 4; ++i) {
    const float p0 = i == 0 ? pa.x : i == 1 ? pa.z : i == 2 ? pb.x : pb.z;
    const float p1 = i == 0 ? pa.y : i == 1 ? pa.w : i == 2 ? pb.y : pb.w;
#pragma unroll
    for (int j = 0; j < 4; ++j) {
      const v2f e = {bflo(v[i][j]), bfhi(v[i][j])};
      a0[j] = __builtin_elementwise_fma(e, v2f{p0, p0}, a0[j]);
      a1[j] = __builtin_elementwise_fma(e, v2f{p1, p1}, a1[j]);
    }
  }
}
__device__ __forceinline__ float half_sum(float v) {
  v += dpp<XOR1>(v); v += dpp<XOR2>(v); v += dpp<HMIR>(v); v += dpp<ROR8>(v);
  return sw16_add(v, v);
}
__device__ __forceinline__ void ln_rows(v4u xa, v4u xb, bool act, v4u& o0, v4u& o1) {
  constexpr unsigned ONES = 0x3F803F80u;
  const float sa = dot2(xa.w, ONES, dot2(xa.z, ONES, dot2(xa.y, ONES, dot2(xa.x, ONES, 0.f))));
  const float sb = dot2(xb.w, ONES, dot2(xb.z, ONES, dot2(xb.y, ONES, dot2(xb.x, ONES, 0.f))));
  const float mean = half_sum(sa + sb) * (1.0f / D);
  v2f v[8];
#pragma unroll
  for (int k = 0; k < 4; ++k) { v[k] = v2f{bflo(xa[k]), bfhi(xa[k])}; v[4 + k] = v2f{bflo(xb[k]), bfhi(xb[k])}; }
  v2f q0 = {0.f, 0.f}, q1 = {0.f, 0.f};
#pragma unroll
  for (int k = 0; k < 8; k += 2) {
    const v2f d0 = v[k] - mean, d1 = v[k + 1] - mean;
    q0 = __builtin_elementwise_fma(d0, d0, q0); q1 = __builtin_elementwise_fma(d1, d1, q1);
  }
  q0 += q1;
  const float sq = half_sum(act ? q0.x + q0.y : 0.f);
  const float mb = rbf(mean);
#pragma unroll
  for (int k = 0; k < 8; ++k) { const v2f d = v[k] - mb; const unsigned p = pk2(d.x, d.y); v[k] = v2f{bflo(p), bfhi(p)}; }
  const float den = rbf(__builtin_amdgcn_sqrtf(rbf(rbf(sq * (1.0f / D)) + 1e-5f)));
  const float ri = __builtin_amdgcn_rcpf(den);
#pragma unroll
  for (int k = 0; k < 4; ++k) { const v2f a = v[k] * ri, c = v[4 + k] * ri; o0[k] = pk2(a.x, a.y); o1[k] = pk2(c.x, c.y); }
}
__device__ __forceinline__ v4u frag8(const LAS unsigned char* p, int str, int hi) {
  v4u r;
#pragma unroll
  for (int k = 0; k < 4; ++k) {
    const int o = (k >> 1) * hi + (k & 1) * 2 * str;
    r[k] = (unsigned)*(const LAS u16*)(p + o) | ((unsigned)*(const LAS u16*)(p + o + str) << 16);
  }
  return r;
}
__device__ __forceinline__ v4f red16(v4f t) {
#pragma unroll
  for (int k = 0; k < 4; ++k) { float a = t[k]; a += dpp<ROR8>(a); a += dpp<ROR4>(a); a = sw16_add(a, a); a = sw32_add(a, a); t[k] = a; }
  return t;
}

__global__ void __launch_bounds__(512, 2)
fused_decode(const u16* __restrict__ x, const u16* __restrict__ kc, const u16* __restrict__ vc, const u16* __restrict__ wq,
             const u16* __restrict__ wo, const u16* __restrict__ w1, const u16* __restrict__ b1, const u16* __restrict__ w2,
             const u16* __restrict__ b2, u16* __restrict__ out, unsigned char* __restrict__ ws) {
  extern __shared__ __attribute__((aligned(16))) unsigned char smem[];
  LAS unsigned char* L = (LAS unsigned char*)smem;
  const int w = blockIdx.x, tid = threadIdx.x, lane = tid & 63, wv = __builtin_amdgcn_readfirstlane(tid >> 6);
  const int kvh = w & 7, b = (w >> 3) & 7, s = w >> 6, grp = b * 8 + kvh;
  volatile LAS unsigned* FL = (volatile LAS unsigned*)(L + L_FLAG);
  const __amdgpu_buffer_rsrc_t rws = __builtin_amdgcn_make_buffer_rsrc((void*)ws, 0, WS_END, RSRC3);
  asm volatile("" :: "s"(x), "s"(kc), "s"(vc), "s"(wq), "s"(wo), "s"(w1), "s"(b1), "s"(w2), "s"(b2), "s"(out), "s"(ws));

  if (__builtin_expect(wv >= 4, 1)) {
    const int hw = wv - 4;
    STAMP(0);
    if (PURE) { if (tid < 256 + 16) FL[tid - 256] = 4u; if (PURE != 3) BAR(); return; }
    __builtin_amdgcn_s_setprio(HPRIO);
    const int r5 = w >> 3;
    const bool isW = r5 < 14;
    const bool isM = !isW && r5 < 28;
    const int t = r5 >> 1, hh = r5 & 1, m = kvh * 14 + (r5 - 14);
    const bool isR = isW && hh == 0 && hw == 0;
    const bool isF = isW && hw >= 2;
    const int cg = r5 * 2 + (hw & 1);
    const int row = hw * 2 + (lane >> 5), c = lane & 31, g = lane >> 4;
    const __amdgpu_buffer_rsrc_t rx = __builtin_amdgcn_make_buffer_rsrc((void*)x, 0, 8 * D * 2, RSRC3);
    const int xo = c < 28 ? row * (D * 2) + c * 32 : 8 * D * 2;
    const v4u xa = LD128(rx, xo, 0, 0), xb = LD128(rx, xo + 16, 0, 0);
    const v2u ep = __builtin_amdgcn_raw_buffer_load_b64(rws, WS_EPOCH, 0, AUX_SC1);
    unsigned wr[16];
    {
      const __amdgpu_buffer_rsrc_t rq = __builtin_amdgcn_make_buffer_rsrc((void*)wq, 0, NOWQ ? 0 : D * NQ * 2, RSRC3);
      const int vo = (((lane >> 3) & 1) * 32 + g * 8) * (NQ * 2) + (kvh * 512 + (b * 4 + s) * 16 + (lane & 7) * 2) * 2;
#pragma unroll
      for (int i = 0; i < 16; ++i) wr[i] = __builtin_amdgcn_raw_buffer_load_b32(rq, vo, (hw * 128 + (i >> 3) * 64 + (i & 7)) * (NQ * 2), Q_AUX);
    }
    if (tid < 256 + 16) FL[tid - 256] = 0u;
    SB();
    BAR();
    const int p_p0 = hw < 2 ? WS_P0 + (grp * 4 + s) * 2048 + hw * 1024 + lane * 16 : WS_END;
    const int p_pw = (isW && (lane & 8) == 0) ? WS_PW + (((lane & 7) * 7 + t) * 16 + kvh * 2 + hh) * 256 + hw * 64 + g * 16 : WS_END;
    const int p_h = (isR && lane < 16) ? WS_H + (kvh * D + t * 64 + lane * 4) * 2 : WS_END;
    const int p_pf = isM ? WS_PF + (((lane & 7) * 28 + hw * 7 + ((lane >> 3) & 1)) * 112 + m) * 64 + g * 16 : WS_END;
    const int p_pf3 = (lane & 8) ? WS_END : p_pf + 6 * 7168;
    {
      const v4u sent4 = {SENT, SENT, SENT, SENT};
      ST128(sent4, rws, hw < 2 ? p_p0 : (hw == 2 && lane < 4) ? WS_ML + (grp * 16 + s * 4 + lane) * 16 : WS_END, 0, AUX_SC1);
      if (isW) {
        ST128(sent4, rws, p_pw, 0, AUX_SC1);
        __builtin_amdgcn_raw_buffer_store_b64(v2u{SENT, SENT}, rws, p_h, 0, AUX_SC1);
      } else if (isM && !NORESET) {
#pragma unroll
        for (int i = 0; i < 3; ++i) ST128(sent4, rws, p_pf, i * 2 * 7168, AUX_SC1);
        ST128(sent4, rws, p_pf3, 0, AUX_SC1);
      }
    }
    SB();
    {
      v4u o0, o1;
      ln_rows(xa, xb, c < 28, o0, o1);
      const int a = c < 28 ? L_LN + row * LN_STR + c * 32 : L_SINK + lane * 32;
      *(LAS v4u*)(L + a) = o0; *(LAS v4u*)(L + a + 16) = o1;
    }
    lds_signal(L, FL_LN, lane, 1u);
    STAMP(1);
    const u64 e64 = (((u64)ep.y << 32) | ep.x) + 1ull;
    const unsigned tagl = __builtin_amdgcn_readfirstlane((unsigned)(e64 << 16)), tagh = __builtin_amdgcn_readfirstlane((unsigned)(e64 >> 16));
    const unsigned enl = __builtin_amdgcn_readfirstlane((unsigned)e64), enh = __builtin_amdgcn_readfirstlane((unsigned)(e64 >> 32));
    {
      v4u be[2], bo[2];
#pragma unroll
      for (int st = 0; st < 2; ++st)
#pragma unroll
        for (int k = 0; k < 4; ++k) {
          be[st][k] = __builtin_amdgcn_perm(wr[st * 8 + 2 * k + 1], wr[st * 8 + 2 * k], 0x05040100u);
          bo[st][k] = __builtin_amdgcn_perm(wr[st * 8 + 2 * k + 1], wr[st * 8 + 2 * k], 0x07060302u);
        }
      lds_wait_ge(FL + FL_LN, 4u, 0);
      const LAS unsigned char* ap = L + L_LN + (lane & 7) * LN_STR + (((lane >> 3) & 1) * 32 + g * 8) * 2 + hw * 256;
      const v4u a0 = *(const LAS v4u*)ap;
      const v4u a1 = *(const LAS v4u*)(ap + (hw == 3 ? 0 : 128));
      v4f de = {0.f, 0.f, 0.f, 0.f}, dd = {0.f, 0.f, 0.f, 0.f};
      de = mfma(a0, be[0], de); dd = mfma(a0, bo[0], dd);
      if (hw != 3) { de = mfma(a1, be[1], de); dd = mfma(a1, bo[1], dd); }
      const bool val = ((lane >> 3) & 1) == (lane >> 5);
      const int qa = val ? L_QP + hw * 512 + (((lane >> 4) & 1) * 4) * 64 + (lane & 7) * 8 : L_SINK + lane * 8;
      LAS unsigned char* qp = L + qa + ((val && lane >= 32) ? (L_BF - L_QP) : 0);
#pragma unroll
      for (int r = 0; r < 4; ++r) *(LAS v2f*)(qp + (val ? r * 64 : 0)) = v2f{de[r], dd[r]};
      VM0();
      lds_signal(L, FL_QP, lane, 1u);
    }
    if (hw == 0) {
      lds_wait_ge(FL + FL_QP, 4u, 0);
      v2f a = {0.f, 0.f};
#pragma unroll
      for (int i = 0; i < 4; ++i) a += *(const LAS v2f*)(L + L_QP + i * 512 + lane * 8) + *(const LAS v2f*)(L + L_BF + i * 512 + lane * 8);
      const unsigned pq = pk2(a.x, a.y);
      const v4u gr = {tagl | (pq & 0xffffu), tagh, tagl | (pq >> 16), tagh};
      ST128(gr, rws, WS_G + ((kvh * 8 + (lane >> 3)) * 512 + (b * 4 + s) * 16 + (lane & 7) * 2) * 8, 0, AUX_SC1);
      STAMP(2);
    }
    if (HSTOP == 2) { lds_signal(L, FL_Q, lane, 1u); return; }
    {
      const int off = WS_G + (kvh * 8 + b) * 4096 + (hw * 64 + lane) * 16;
      v4u gq;
#pragma nounroll
      for (unsigned sp = 0; sp < SPIN_CAP; ++sp) {
        CLOB();
        gq = LD128(rws, off, 0, AUX_SC1);
        const bool ok = (((gq.x & 0xffff0000u) ^ tagl) | (gq.y ^ tagh) | ((gq.z & 0xffff0000u) ^ tagl) | (gq.w ^ tagh)) == 0u;
        if (__all(ok)) break;
        if (QSLEEP) __builtin_amdgcn_s_sleep(QSLEEP);
      }
      *(LAS unsigned*)(L + L_Q + (hw * 64 + lane) * 4) = (gq.x & 0xffffu) | (gq.z << 16);
      lds_signal(L, FL_Q, lane, 1u);
    }
    STAMP(3);
    if (HSTOP == 3) return;
    v4u Fr[18];
    v4f cx = {0.f, 0.f, 0.f, 0.f}, cb = {0.f, 0.f, 0.f, 0.f};
#pragma unroll
    for (int i = 0; i < 18; ++i) Fr[i] = v4u{0u, 0u, 0u, 0u};
    if (isW) {
      const __amdgpu_buffer_rsrc_t ro = __builtin_amdgcn_make_buffer_rsrc((void*)wo, 0, NOSTAGE ? 0 : NQ * D * 2, RSRC3);
#pragma unroll 1
      for (int it = 0; it < 2; ++it) {
        v4u tt[4];
#pragma unroll
        for (int i = 0; i < 4; ++i) tt[i] = LD128(ro, (lane >> 3) * (D * 2) + (lane & 7) * 16, (kvh * 512 + hh * 256 + it * 128 + hw * 32 + i * 8) * (D * 2) + t * 128, W_AUX);
#pragma unroll
        for (int i = 0; i < 4; ++i) *(LAS v4u*)(L + L_RAW + (it * 128 + hw * 32 + i * 8) * 128 + lane * 16) = tt[i];
      }
      const v2u xv = __builtin_amdgcn_raw_buffer_load_b64(rx, (kvh * D + t * 64 + (lane & 15) * 4) * 2, 0, 0);
      const v2u bv = *(const GAS v2u*)(b2 + cg * 16 + (lane & 3) * 4);
      cx = v4f{bflo(xv.x), bfhi(xv.x), bflo(xv.y), bfhi(xv.y)};
      cb = v4f{bflo(bv.x), bfhi(bv.x), bflo(bv.y), bfhi(bv.y)};
      lds_signal(L, FL_RAW, lane, 1u);
      lds_wait_ge(FL + FL_RAW, 4u, 1);
#pragma unroll
      for (int ks = 0; ks < 8; ++ks) Fr[ks] = frag8(L + L_RAW + (ks * 32 + g * 4) * 128 + (hw * 16 + (lane & 15)) * 2, 128, 16 * 128);
    } else if (isM) {
      const __amdgpu_buffer_rsrc_t r1 = __builtin_amdgcn_make_buffer_rsrc((void*)w1, 0, NOSTAGE ? 0 : D * F * 2, RSRC3);
      const __amdgpu_buffer_rsrc_t r2 = __builtin_amdgcn_make_buffer_rsrc((void*)(w2 + m * 16 * D), 0, NOSTAGE ? 0 : 16 * D * 2, RSRC3);
      v4u tt[4], uu[4];
#pragma unroll
      for (int i = 0; i < 4; ++i) tt[i] = LD128(r1, (lane >> 1) * (F * 2) + (lane & 1) * 16, (i * 4 + hw) * 32 * (F * 2) + m * 32, W_AUX);
#pragma unroll
      for (int i = 0; i < 4; ++i) *(LAS v4u*)(L + L_RAW + (i * 4 + hw) * 1024 + lane * 16) = tt[i];
#pragma unroll
      for (int i = 0; i < 4; ++i) uu[i] = LD128(r2, lane * 16, (i * 4 + hw) * 1024, W_AUX);
#pragma unroll
      for (int i = 0; i < 4; ++i) *(LAS v4u*)(L + L_RAW + 16384 + (i * 4 + hw) * 1024 + lane * 16) = uu[i];
      const v2u bv = *(const GAS v2u*)(b1 + m * 16 + g * 4);
      cb = v4f{bflo(bv.x), bfhi(bv.x), bflo(bv.y), bfhi(bv.y)};
      lds_signal(L, FL_RAW, lane, 1u);
      lds_wait_ge(FL + FL_RAW, 4u, 1);
#pragma unroll
      for (int ks = 0; ks < 14; ++ks) Fr[ks] = frag8(L + L_RAW + (ks * 32 + g * 8) * 32 + (lane & 15) * 2, 32, 4 * 32);
#pragma unroll
      for (int i = 0; i < 4; ++i) Fr[14 + i] = frag8(L + L_RAW + 16384 + g * 4 * 896 + ((hw * 7 + 2 * i) * 16 + (lane & 15)) * 2, 896, i < 3 ? 32 : 0);
    }
    STAMP(4);
    if (HSTOP == 4) { asm volatile("" :: "v"(Fr[0]), "v"(Fr[7]), "v"(Fr[13]), "v"(Fr[17])); return; }
    {
      const int ww = hw * 64 + lane;
      const int off = WS_G + ((ww >> 5) * 8 * 512 + (ww & 31) * 16) * 8;
#pragma nounroll
      for (unsigned sp = 0; sp < SPIN_CAP; ++sp) {
        CLOB();
        const v2u g2 = __builtin_amdgcn_raw_buffer_load_b64(rws, off, 0, AUX_SC1);
        if (__all((((g2.x & 0xffff0000u) ^ tagl) | (g2.y ^ tagh)) == 0u)) break;
        __builtin_amdgcn_s_sleep(16);
      }
      lds_signal(L, FL_OK, lane, 1u);
    }
    if (hw == 0) {
      lds_wait_ge(FL + FL_ML, 4u, 1);
      const v4f mlv = *(const LAS v4f*)(L + L_ML + (lane & 3) * 16);
      ST128(canon4(mlv), rws, lane < 4 ? WS_ML + (grp * 16 + s * 4 + lane) * 16 : WS_END, 0, AUX_SC1);
    }
    if (hw == 1) lds_wait_ge(FL + FL_ML, 4u, 1);
    float fw[4];
    {
      const int off = lane < 16 ? WS_ML + (grp * 16 + lane) * 16 + (hw & 1) * 8 : WS_END;
      v2u ml;
#pragma nounroll
      for (unsigned sp = 0; sp < SPIN_CAP; ++sp) {
        CLOB();
        ml = __builtin_amdgcn_raw_buffer_load_b64(rws, off, 0, AUX_SC1);
        if (__all(umax(ml.x, ml.y) != SENT) || hw >= 2) break;
        __builtin_amdgcn_s_sleep(64);
      }
      const float mv = __uint_as_float(ml.x), lv = __uint_as_float(ml.y);
      float M = mv;
      M = fmaxf(M, dpp<XOR1>(M)); M = fmaxf(M, dpp<XOR2>(M)); M = fmaxf(M, dpp<HMIR>(M)); M = fmaxf(M, dpp<ROR8>(M));
      const float e = __builtin_amdgcn_exp2f((mv - M) * SC2);
      float Ls = e * lv;
      Ls += dpp<XOR1>(Ls); Ls += dpp<XOR2>(Ls); Ls += dpp<HMIR>(Ls); Ls += dpp<ROR8>(Ls);
      const float f = e / Ls;
#pragma unroll
      for (int i = 0; i < 4; ++i) fw[i] = __builtin_bit_cast(float, __builtin_amdgcn_readlane(__builtin_bit_cast(int, f), s * 4 + i));
    }
    lds_wait_ge(FL + FL_OK, 4u, 1);
    STAMP(5);
    if (HSTOP == 5) { asm volatile("" :: "v"(Fr[0]), "v"(Fr[7]), "v"(Fr[13]), "v"(Fr[17]), "v"(fw[0])); return; }
    const int g_p0 = WS_P0 + (((lane & 7) * 8 + kvh) * 4 + ((lane >> 3) & 1) * 2) * 2048 + (hh * 256 + hw * 64 + g * 4) * 4;
    const int g_pw = WS_PW + (kvh * 7 + t) * 4096 + lane * 16;
    const int g_pf = WS_PF + (kvh * 28 + cg) * 7168 + lane * 16;
    const int g_h = isM ? (c < 28 ? WS_H + row * (D * 2) + c * 32 : WS_END) : WS_H + (kvh * D + cg * 16 + (lane & 3) * 4) * 2;
    const int a_ln = c < 28 ? L_LN + row * LN_STR + c * 32 : L_SINK + lane * 32;
    const int g_o = lane < 4 ? (kvh * D + cg * 16 + lane * 4) * 2 : 8 * D * 2;
    const int g_e = (w == 0 && hw == 2 && lane == 0) ? WS_EPOCH : WS_END;
    const __amdgpu_buffer_rsrc_t rout = __builtin_amdgcn_make_buffer_rsrc((void*)out, 0, 8 * D * 2, RSRC3);
#pragma unroll 1
    for (int pass = DRYRUN ? 0 : 1; pass < (HSTOP == 6 ? 1 : 2); ++pass) {
      int real = __builtin_amdgcn_readfirstlane(pass);
      asm volatile("" : "+s"(real));
      const int oob = real ? 0 : WS_END;
      if (real) {
        lds_wait_ge(FL + FL_PRE, 4u, 1);
        STAMP(6);
        const unsigned t0 = __builtin_amdgcn_raw_buffer_load_b32(rws, isW ? p_pw : p_pf, 0, AUX_SC1), t1 = __builtin_amdgcn_raw_buffer_load_b32(rws, isW ? p_p0 : p_pf3, 0, AUX_SC1);
        const unsigned t2 = __builtin_amdgcn_raw_buffer_load_b32(rws, isW ? (hw < 2 ? g_pw : g_pf) : p_p0, 0, AUX_SC1), t3 = __builtin_amdgcn_raw_buffer_load_b32(rout, g_o, 0, 0);
        asm volatile("" :: "v"(t0), "v"(t1), "v"(t2), "v"(t3));
      }
      if (hw < 2) {
        lds_wait_ge(FL + FL_DONE, real ? 4u : 0u, 0);
        STAMPR(7);
        const LAS v4f* mp = (const LAS v4f*)(L + L_MRG + hw * 1024 + lane * 16);
        const v4f o = (mp[0] * fw[0] + mp[128] * fw[1]) + (mp[256] * fw[2] + mp[384] * fw[3]);
        ST128(canon4(o), rws, p_p0, oob, AUX_SC1);
        STAMPR(8);
      }
      if (isW) {
        v4u pv[8];
#pragma nounroll
        for (unsigned sp = 0; sp < SPIN_CAP; ++sp) {
          CLOB();
#pragma unroll
          for (int i = 0; i < 8; ++i) pv[i] = LD128(rws, g_p0 + (i & 3) * 64 + (i >> 2) * 2048, oob, AUX_SC1);
          unsigned mm = max4(pv[0]);
#pragma unroll
          for (int i = 1; i < 8; ++i) mm = umax(mm, max4(pv[i]));
          if (__all(mm != SENT)) break;
        }
        STAMPR(9);
        v4f sv[4];
#pragma unroll
        for (int i = 0; i < 4; ++i) {
          sv[i] = asf(pv[i]) + asf(pv[4 + i]);
#pragma unroll
          for (int k = 0; k < 4; ++k) sv[i][k] += dpp<ROR8>(sv[i][k]);
        }
        *(LAS v4u*)(L + L_BF + hw * 2048 + lane * 16) = v4u{pk2(sv[0].x, sv[0].y), pk2(sv[0].z, sv[0].w), pk2(sv[1].x, sv[1].y), pk2(sv[1].z, sv[1].w)};
        *(LAS v4u*)(L + L_BF + hw * 2048 + 1024 + lane * 16) = v4u{pk2(sv[2].x, sv[2].y), pk2(sv[2].z, sv[2].w), pk2(sv[3].x, sv[3].y), pk2(sv[3].z, sv[3].w)};
        if (real) BAR();
        v4u bf[8];
#pragma unroll
        for (int ks = 0; ks < 8; ++ks) bf[ks] = *(const LAS v4u*)(L + L_BF + ks * 1024 + lane * 16);
        SB();
        v4f d0 = {0.f, 0.f, 0.f, 0.f}, d1 = {0.f, 0.f, 0.f, 0.f};
#pragma unroll
        for (int ks = 0; ks < 8; ks += 2) { d0 = mfma(Fr[ks], bf[ks], d0); d1 = mfma(Fr[ks + 1], bf[ks + 1], d1); }
        ST128(canon4(d0 + d1), rws, p_pw, oob, AUX_SC1);
        STAMPR(10);
        if (isR) {
          v4u u[4];
#pragma nounroll
          for (unsigned sp = 0; sp < SPIN_CAP; ++sp) {
            CLOB();
#pragma unroll
            for (int i = 0; i < 4; ++i) u[i] = LD128(rws, g_pw + i * 1024, oob, AUX_SC1);
            if (__all(umax(umax(max4(u[0]), max4(u[1])), umax(max4(u[2]), max4(u[3]))) != SENT)) break;
          }
          STAMPR(11);
          v4f hv = (asf(u[0]) + asf(u[1])) + (asf(u[2]) + asf(u[3]));
#pragma unroll
          for (int k = 0; k < 4; ++k) { float a = hv[k]; a = sw16_add(a, a); a = sw32_add(a, a); hv[k] = cx[k] + rbf(a); }
          __builtin_amdgcn_raw_buffer_store_b64(v2u{canonu(pk2(hv.x, hv.y)), canonu(pk2(hv.z, hv.w))}, rws, p_h, oob, AUX_SC1);
          STAMPR(12);
        }
        if (isF) {
          v4u u[7];
          v2u hq;
#pragma nounroll
          for (unsigned sp = 0; sp < SPIN_CAP; ++sp) {
            CLOB();
            hq = __builtin_amdgcn_raw_buffer_load_b64(rws, g_h, oob, AUX_SC1);
            if (__all(umax(hq.x, hq.y) != SENT)) break;
            __builtin_amdgcn_s_sleep(2);
          }
#pragma nounroll
          for (unsigned sp = 0; sp < SPIN_CAP; ++sp) {
            CLOB();
#pragma unroll
            for (int i = 0; i < 7; ++i) u[i] = LD128(rws, g_pf + i * 1024, oob, AUX_SC1);
            unsigned mm = max4(u[0]);
#pragma unroll
            for (int i = 1; i < 7; ++i) mm = umax(mm, max4(u[i]));
            if (__all(mm != SENT)) break;
          }
          STAMPR(13);
          v4f ov = red16(((asf(u[0]) + asf(u[1])) + (asf(u[2]) + asf(u[3]))) + ((asf(u[4]) + asf(u[5])) + asf(u[6])));
          const v4f hv = {bflo(hq.x), bfhi(hq.x), bflo(hq.y), bfhi(hq.y)};
#pragma unroll
          for (int k = 0; k < 4; ++k) ov[k] = hv[k] + rbf(rbf(ov[k]) + cb[k]);
          __builtin_amdgcn_raw_buffer_store_b64(v2u{pk2(ov.x, ov.y), pk2(ov.z, ov.w)}, rout, g_o, oob, 0);
          __builtin_amdgcn_raw_buffer_store_b64(v2u{enl, enh}, rws, g_e, oob, AUX_SC1);
          STAMPR(14);
        }
      } else if (isM) {
        v4u ha, hb;
#pragma nounroll
        for (unsigned sp = 0; sp < SPIN_CAP; ++sp) {
          CLOB();
          ha = LD128(rws, g_h, oob, AUX_SC1); hb = LD128(rws, g_h + 16, oob, AUX_SC1);
          if (__all(umax(max4(ha), max4(hb)) != SENT)) break;
        }
        STAMPR(15);
        v4u o0, o1;
        ln_rows(ha, hb, c < 28, o0, o1);
        *(LAS v4u*)(L + a_ln) = o0; *(LAS v4u*)(L + a_ln + 16) = o1;
        if (real) BAR();
        const LAS unsigned char* bp = L + L_LN + (lane & 7) * LN_STR + g * 16;
        v4u bf[14];
#pragma unroll
        for (int ks = 0; ks < 14; ++ks) bf[ks] = *(const LAS v4u*)(bp + ks * 64);
        SB();
        v4f d0 = {0.f, 0.f, 0.f, 0.f}, d1 = {0.f, 0.f, 0.f, 0.f};
#pragma unroll
        for (int ks = 0; ks < 14; ks += 2) { d0 = mfma(Fr[ks], bf[ks], d0); d1 = mfma(Fr[ks + 1], bf[ks + 1], d1); }
        d0 += d1;
        float u[4];
#pragma unroll
        for (int k = 0; k < 4; ++k) { float a = rbf(rbf(d0[k]) + cb[k]); a = a > 0.f ? a : 0.f; u[k] = a * a; }
        const s16x4 hb4 = __builtin_bit_cast(s16x4, v2u{pk2(u[0], u[1]), pk2(u[2], u[3])});
        v4f e[8];
#pragma unroll
        for (int i = 0; i < 7; ++i) {
          const v2u af = (i & 1) ? v2u{Fr[14 + (i >> 1)].z, Fr[14 + (i >> 1)].w} : v2u{Fr[14 + (i >> 1)].x, Fr[14 + (i >> 1)].y};
          e[i] = __builtin_amdgcn_mfma_f32_16x16x16bf16_1k(__builtin_bit_cast(s16x4, af), hb4, v4f{0.f, 0.f, 0.f, 0.f}, 0, 0, 0);
        }
        e[7] = e[6];
        v4u ev[4];
        const bool up = lane & 8;
#pragma unroll
        for (int i = 0; i < 4; ++i) {
          const v4f ea = e[2 * i], eb = e[2 * i + 1];
          ev[i] = v4u{canon(up ? eb.x : ea.x), canon(up ? eb.y : ea.y), canon(up ? eb.z : ea.z), canon(up ? eb.w : ea.w)};
        }
        SB();
#pragma unroll
        for (int i = 0; i < 3; ++i) ST128(ev[i], rws, p_pf + i * 2 * 7168, oob, AUX_SC1);
        ST128(ev[3], rws, p_pf3, oob, AUX_SC1);
        STAMPR(16);
      }
    }
  } else {
    const size_t kvoff = ((size_t)grp * SK + (size_t)s * 1024) * DH;
    const __amdgpu_buffer_rsrc_t rk = __builtin_amdgcn_make_buffer_rsrc((void*)(kc + kvoff), 0, 524288, RSRC3);
    const int voff = lane * 16 + wv * 4096;
    v4u X[NEXTRA * 4][4], R[4][4];
    const unsigned tb = (unsigned)__builtin_amdgcn_s_memrealtime() + (unsigned)(T1);
#define LANDED() asm volatile("s_waitcnt vmcnt(%0)" :: "i"(4 * (LEAD - 1)) : "memory")
    STAMP(0);
    if (PURE != 3) BAR();
#pragma unroll
    for (int n = 0; n < NEXTRA * 4 + LEAD; ++n) {
      if (n >= LEAD) LANDED();
      pace(tb, n);
      issue_turn(n < NEXTRA * 4 ? X[n] : R[n - NEXTRA * 4], rk, voff, n * 16384);
    }
    const bool b0 = lane & 1, b1_ = lane & 2, b2_ = lane & 4, b3 = lane & 8;
    const int hf = lane >> 5, c = lane & 31;
    LAS float* P = (LAS float*)(L + L_P) + wv * 512;
    LAS float* Pl = P + ((lane >> 3) & 3) * 16 + hf * 8 + (lane & 7);
    STAMP(1);
    if (PURE != 3) lds_wait_ge(FL + FL_Q, 4u, 0);
    STAMP(2);
    const v4u q0 = *(const LAS v4u*)(L + L_Q + c * 16), q1 = *(const LAS v4u*)(L + L_Q + 512 + c * 16);
    float mx = -INFINITY;
#pragma unroll
    for (int g = 0; g < NEXTRA; ++g) {
      float r[4], rx[4];
#pragma unroll
      for (int d = 0; d < 4; ++d) {
        rx[d] = k_turn(X[4 * g + d], q0, q1, b0, b1_, b2_);
        SB();
        LANDED();
        pace(tb, (NEXTRA + g) * 4 + d + LEAD);
        issue_turn(R[(d + LEAD) & 3], rk, voff, ((NEXTRA + g) * 4 + d + LEAD) * 16384);
        r[d] = k_turn(R[d], q0, q1, b0, b1_, b2_);
        SB();
      }
      const float sx = k_group(rx[0], rx[1], rx[2], rx[3], b3), sc = k_group(r[0], r[1], r[2], r[3], b3);
      mx = fmaxf(mx, fmaxf(sx, sc));
      Pl[g * 64] = sx; Pl[(NEXTRA + g) * 64] = sc;
      SB();
    }
    STAMP(3);
    const __amdgpu_buffer_rsrc_t rv = __builtin_amdgcn_make_buffer_rsrc((void*)(vc + kvoff), 0, 524288, RSRC3);
#pragma unroll 1
    for (int g = 2 * NEXTRA; g < 8; ++g) {
      float r[4];
#pragma unroll
      for (int d = 0; d < 4; ++d) {
        const int nx = g * 4 + d + LEAD;
        const bool tov = (d + LEAD >= 4) && g == 7;
        const __amdgpu_buffer_rsrc_t rn = __builtin_amdgcn_make_buffer_rsrc((void*)((tov ? vc : kc) + kvoff), 0, 524288, RSRC3);
        LANDED();
        pace(tb, nx);
        issue_turn(R[(d + LEAD) & 3], rn, voff, (nx & 31) * 16384);
        r[d] = k_turn(R[d], q0, q1, b0, b1_, b2_);
        SB();
      }
      const float sc = k_group(r[0], r[1], r[2], r[3], b3);
      mx = fmaxf(mx, sc);
      Pl[g * 64] = sc;
    }
    STAMP(4);
    mx = fmaxf(mx, dpp<XOR2>(mx)); mx = fmaxf(mx, dpp<ROR4>(mx)); mx = fmaxf(mx, dpp<ROR8>(mx));
    mx = sw16_max(mx); mx = sw32_max(mx);
    const float msub = mx == -INFINITY ? 0.f : mx;
    float ls = 0.f;
    {
      float sv[8];
#pragma unroll
      for (int g = 0; g < 8; ++g) sv[g] = Pl[g * 64];
#pragma unroll
      for (int g = 0; g < 8; ++g) { const float p = __builtin_amdgcn_exp2f((sv[g] - msub) * SC2); ls += p; Pl[g * 64] = p; }
    }
    ls += dpp<XOR2>(ls); ls += dpp<ROR8>(ls); ls += dpp<ROR4>(ls);
    ls = sw16_add(ls, ls); ls = sw32_add(ls, ls);
    *(LAS v2f*)(L + (lane < 2 ? L_ML + (wv * 2 + lane) * 8 : L_SINK + 1024 + lane * 8)) = v2f{mx, ls};
    lds_signal(L, FL_ML, lane, 1u);
    v2f a0[4], a1[4];
#pragma unroll
    for (int k = 0; k < 4; ++k) { a0[k] = v2f{0.f, 0.f}; a1[k] = v2f{0.f, 0.f}; }
    const LAS v4f* Pr = (const LAS v4f*)(P + hf * 8);
    LAS v4f* mo = (LAS v4f*)(L + L_MRG + wv * 2048 + lane * 32);
#pragma unroll 1
    for (int g = 0; g < 8; ++g) {
      v4f pa[4], pb[4];
#pragma unroll
      for (int d = 0; d < 4; ++d) { pa[d] = Pr[(g * 4 + d) * 4]; pb[d] = Pr[(g * 4 + d) * 4 + 1]; }
#pragma unroll
      for (int d = 0; d < 4; ++d) {
        LANDED();
        pace(tb, 32 + g * 4 + d + LEAD);
        issue_turn(R[(d + LEAD) & 3], rv, voff, (g * 4 + d + LEAD) * 16384);
        v_turn(R[d], pa[d], pb[d], a0, a1);
        SB();
        if (d == 1) lds_signal(L, FL_PRE, lane, g == 7 ? 1u : 0u);
        if (d == 3) {
          mo[0] = v4f{sw32_add(a0[0].x, a1[0].x), sw32_add(a0[0].y, a1[0].y), sw32_add(a0[1].x, a1[1].x), sw32_add(a0[1].y, a1[1].y)};
          mo[1] = v4f{sw32_add(a0[2].x, a1[2].x), sw32_add(a0[2].y, a1[2].y), sw32_add(a0[3].x, a1[3].x), sw32_add(a0[3].y, a1[3].y)};
          lds_signal(L, FL_DONE, lane, g == 7 ? 1u : 0u);
          SB();
        }
      }
    }
    STAMP(5);
  }
}
}

extern "C" void kernel_launch(void* const* d_in, const int* in_sizes, int n_in, void* d_out, int out_size, void* d_ws,
                              size_t ws_size, hipStream_t stream) {
  using namespace fk;
  (void)hipFuncSetAttribute((const void*)fused_decode, hipFuncAttributeMaxDynamicSharedMemorySize, L_END + DBG_STAMPS * 2048);
  hipLaunchKernelGGL(fused_decode, dim3(256), dim3(512), L_END + DBG_STAMPS * 2048, stream, (const u16*)d_in[0], (const u16*)d_in[1],
                     (const u16*)d_in[2], (const u16*)d_in[3], (const u16*)d_in[4], (const u16*)d_in[6], (const u16*)d_in[7],
                     (const u16*)d_in[8], (const u16*)d_in[9], (u16*)d_out, (unsigned char*)d_ws);
}
```

```cpp
#include <hip/hip_runtime.h>
#include <hip/hip_bf16.h>
#include <stdint.h>

#define DBG_STAMPS 0
#define W_AUX 2
#define Q_AUX 2
#define NEXTRA 2
#define RING 4
#define PACE 2
#define DRYRUN 1
#define PRETOUCH 1
#define TAU 0
#define T1 0
#define NOSTAGE 0
#define NOWQ 0
#define NORESET 0
#define QSLEEP 0
#define LEAD 2
#define PURE 0
#define HSTOP 0
#define K_AUX 2
#define V_AUX 2
#define HPRIO 3

namespace fk {
typedef unsigned short u16;
typedef unsigned long long u64;
typedef unsigned v4u __attribute__((ext_vector_type(4)));
typedef unsigned v2u __attribute__((ext_vector_type(2)));
typedef float v4f __attribute__((ext_vector_type(4)));
typedef float v2f __attribute__((ext_vector_type(2)));
typedef __bf16 bf16x2 __attribute__((ext_vector_type(2)));
typedef __bf16 bf16x8 __attribute__((ext_vector_type(8)));
typedef short s16x4 __attribute__((ext_vector_type(4)));
#define LAS __attribute__((address_space(3)))
#define GAS __attribute__((address_space(1)))

constexpr int D = 448, F = 1792, SK = 4096, DH = 256, NQ = 4096;
constexpr unsigned SENT = 0xFFFFFFFFu;
constexpr int AUX_NT = 2, AUX_SC1 = 16;
constexpr int RSRC3 = 0x00020000;
constexpr float SC2 = 0.0625f * 1.4426950408889634f;
constexpr unsigned SPIN_CAP = 1u << 16;

constexpr int WS_EPOCH = 0;
constexpr int WS_G = 4096;
constexpr int WS_ML = WS_G + 8 * 8 * 512 * 8;
constexpr int WS_P0 = WS_ML + 64 * 16 * 16;
constexpr int WS_PW = WS_P0 + 64 * 4 * 512 * 4;
constexpr int WS_H = WS_PW + 8 * 7 * 16 * 256;
constexpr int WS_PF = WS_H + 8192;
constexpr int WS_END = WS_PF + 8 * 28 * 112 * 64;
constexpr int WS_DBG = WS_END;

constexpr int LN_STR = 912;
constexpr int L_P = 0;
constexpr int L_MRG = 8192;
constexpr int L_Q = 16384;
constexpr int L_FLAG = 17408;
constexpr int L_QP = L_FLAG + 64;
constexpr int L_LN = L_QP + 2048;
constexpr int L_BF = L_LN + 8 * LN_STR;
constexpr int L_SINK = L_BF + 8192;
constexpr int L_RAW = L_SINK + 2048;
constexpr int L_END = 96 * 1024;
static_assert(L_RAW + 32768 <= L_END, "lds");
static_assert(L_QP % 16 == 0 && L_LN % 16 == 0 && L_BF % 16 == 0 && L_RAW % 16 == 0, "align");
enum { FL_LN = 0, FL_QP = 1, FL_Q = 2, FL_PRE = 3, FL_DONE = 4, FL_RAW = 5, FL_S1 = 6, FL_S2 = 7, FL_OK = 8, FL_ML = 9 };
constexpr int L_ML = L_QP;

__device__ __forceinline__ float bflo(unsigned v) { return __builtin_bit_cast(float, v << 16); }
__device__ __forceinline__ float bfhi(unsigned v) { return __builtin_bit_cast(float, v & 0xffff0000u); }
__device__ __forceinline__ float rbf(float f) { return (float)(__bf16)f; }
__device__ __forceinline__ unsigned pk2(float lo, float hi) {
  bf16x2 v = {(__bf16)lo, (__bf16)hi};
  return __builtin_bit_cast(unsigned, v);
}
__device__ __forceinline__ float dot2(unsigned a, unsigned b, float c) {
  return __builtin_amdgcn_fdot2_f32_bf16(__builtin_bit_cast(bf16x2, a), __builtin_bit_cast(bf16x2, b), c, false);
}
__device__ __forceinline__ v4f mfma(v4u a, v4u b, v4f c) {
  return __builtin_amdgcn_mfma_f32_16x16x32_bf16(__builtin_bit_cast(bf16x8, a), __builtin_bit_cast(bf16x8, b), c, 0, 0, 0);
}
template <int CTRL> __device__ __forceinline__ float dpp(float x) {
  return __builtin_bit_cast(float, __builtin_amdgcn_mov_dpp(__builtin_bit_cast(int, x), CTRL, 0xf, 0xf, true));
}
constexpr int XOR1 = 0xB1, XOR2 = 0x4E, HMIR = 0x141, ROR4 = 0x124, ROR8 = 0x128;
__device__ __forceinline__ float sw16_add(float a, float b) {
  auto s = __builtin_amdgcn_permlane16_swap(__float_as_uint(a), __float_as_uint(b), false, false);
  return __uint_as_float(s[0]) + __uint_as_float(s[1]);
}
__device__ __forceinline__ float sw32_add(float a, float b) {
  auto s = __builtin_amdgcn_permlane32_swap(__float_as_uint(a), __float_as_uint(b), false, false);
  return __uint_as_float(s[0]) + __uint_as_float(s[1]);
}
__device__ __forceinline__ float sw16_max(float a) {
  auto s = __builtin_amdgcn_permlane16_swap(__float_as_uint(a), __float_as_uint(a), false, false);
  return fmaxf(__uint_as_float(s[0]), __uint_as_float(s[1]));
}
__device__ __forceinline__ float sw32_max(float a) {
  auto s = __builtin_amdgcn_permlane32_swap(__float_as_uint(a), __float_as_uint(a), false, false);
  return fmaxf(__uint_as_float(s[0]), __uint_as_float(s[1]));
}
__device__ __forceinline__ unsigned canonu(unsigned u) { return u == SENT ? 0x7FC07FC0u : u; }
__device__ __forceinline__ unsigned canon(float f) { return canonu(__builtin_bit_cast(unsigned, f)); }
__device__ __forceinline__ v4u canon4(v4f v) { return v4u{canon(v.x), canon(v.y), canon(v.z), canon(v.w)}; }
__device__ __forceinline__ unsigned umax(unsigned a, unsigned b) { return a > b ? a : b; }
__device__ __forceinline__ unsigned max4(v4u v) { return umax(umax(v.x, v.y), umax(v.z, v.w)); }
__device__ __forceinline__ v4f asf(v4u v) { return __builtin_bit_cast(v4f, v); }
#define LGKM0() asm volatile("s_waitcnt lgkmcnt(0)" ::: "memory")
#define VM0() asm volatile("s_waitcnt vmcnt(0)" ::: "memory")
#define CLOB() asm volatile("" ::: "memory")
#define SB() __builtin_amdgcn_sched_barrier(0)
#define BAR() do { asm volatile("s_waitcnt lgkmcnt(0)" ::: "memory"); __builtin_amdgcn_s_barrier(); asm volatile("" ::: "memory"); __builtin_amdgcn_sched_barrier(0); } while (0)
#define LD128(rs, vo, so, aux) __builtin_amdgcn_raw_buffer_load_b128(rs, vo, so, aux)
#define ST128(v, rs, vo, so, aux) __builtin_amdgcn_raw_buffer_store_b128(v, rs, vo, so, aux)
#define STR2(x) #x
#define STR(x) STR2(x)

#define STAMP(i) do {} while (0)
#define STAMPR(i) do {} while (0)

__device__ __forceinline__ void lds_wait_ge(volatile LAS unsigned* f, unsigned want, int slp) {
#pragma nounroll
  for (unsigned sp = 0; sp < (SPIN_CAP << 4); ++sp) {
    if (*f >= want) break;
    if (slp) __builtin_amdgcn_s_sleep(8);
  }
  CLOB();
}
__device__ __forceinline__ void lds_signal(LAS unsigned char* L, int word, int lane, unsigned inc) {
  LGKM0();
  LAS unsigned* p = (LAS unsigned*)(L + (lane == 0 ? L_FLAG + word * 4 : L_SINK + lane * 4));
  __hip_atomic_fetch_add(p, inc, __ATOMIC_RELAXED, __HIP_MEMORY_SCOPE_WORKGROUP);
}

__device__ __forceinline__ void pace(unsigned tb, int n) {
  if (TAU) {
    const unsigned until = tb + (unsigned)(((n < 64 ? n : 0) * TAU) >> 4);
    asm volatile("8:\n\ts_memrealtime vcc\n\ts_waitcnt lgkmcnt(0)\n\ts_sub_i32 vcc_lo, vcc_lo, %0\n\ts_cmp_lt_i32 vcc_lo, 0\n\ts_cbranch_scc1 8b\n" :: "s"(until) : "scc", "vcc", "memory");
  }
}
template <int AUX = K_AUX> __device__ __forceinline__ void issue_turn(v4u* r, __amdgpu_buffer_rsrc_t rs, int voff, int soff) {
#pragma unroll
  for (int i = 0; i < 4; ++i) r[i] = LD128(rs, voff + i * 1024, soff, AUX);
  SB();
}
__device__ __forceinline__ float k_turn(const v4u* k, v4u q0, v4u q1, bool b0, bool b1, bool b2) {
  if (PURE == 1 || PURE == 3) { const v4u t = (k[0] ^ k[1]) ^ (k[2] ^ k[3]); return __uint_as_float((t.x ^ t.y ^ t.z ^ t.w) & 0x3fffffffu); }
  float x[8];
#pragma unroll
  for (int i = 0; i < 4; ++i) {
    float a0 = dot2(k[i].x, q0.x, 0.f), a1 = dot2(k[i].x, q1.x, 0.f);
    a0 = dot2(k[i].y, q0.y, a0); a1 = dot2(k[i].y, q1.y, a1);
    a0 = dot2(k[i].z, q0.z, a0); a1 = dot2(k[i].z, q1.z, a1);
    a0 = dot2(k[i].w, q0.w, a0); a1 = dot2(k[i].w, q1.w, a1);
    x[2 * i] = a0; x[2 * i + 1] = a1;
  }
  float y[4], z[2];
#pragma unroll
  for (int v = 0; v < 4; ++v) y[v] = (b2 ? x[v + 4] : x[v]) + dpp<HMIR>(b2 ? x[v] : x[v + 4]);
#pragma unroll
  for (int v = 0; v < 2; ++v) z[v] = (b1 ? y[v + 2] : y[v]) + dpp<XOR2>(b1 ? y[v] : y[v + 2]);
  return (b0 ? z[1] : z[0]) + dpp<XOR1>(b0 ? z[0] : z[1]);
}
__device__ __forceinline__ float k_group(float r0, float r1, float r2, float r3, bool b3) {
  const float u0 = (b3 ? r1 : r0) + dpp<ROR8>(b3 ? r0 : r1);
  const float u1 = (b3 ? r3 : r2) + dpp<ROR8>(b3 ? r2 : r3);
  return rbf(sw16_add(u0, u1));
}
__device__ __forceinline__ void v_turn(const v4u* v, v4f pa, v4f pb, v2f (&a0)[4], v2f (&a1)[4]) {
  if (PURE == 1 || PURE == 3) { const v4u t = (v[0] ^ v[1]) ^ (v[2] ^ v[3]); a0[0].x += __uint_as_float((t.x ^ t.y ^ t.z ^ t.w) & 0x3fffffffu) * pa.x; return; }
#pragma unroll
  for (int i = 0; i < 4; ++i) {
    const float p0 = i == 0 ? pa.x : i == 1 ? pa.z : i == 2 ? pb.x : pb.z;
    const float p1 = i == 0 ? pa.y : i == 1 ? pa.w : i == 2 ? pb.y : pb.w;
#pragma unroll
    for (int j = 0; j < 4; ++j) {
      const v2f e = {bflo(v[i][j]), bfhi(v[i][j])};
      a0[j] = __builtin_elementwise_fma(e, v2f{p0, p0}, a0[j]);
      a1[j] = __builtin_elementwise_fma(e, v2f{p1, p1}, a1[j]);
    }
  }
}
__device__ __forceinline__ float half_sum(float v) {
  v += dpp<XOR1>(v); v += dpp<XOR2>(v); v += dpp<HMIR>(v); v += dpp<ROR8>(v);
  return sw16_add(v, v);
}
__device__ __forceinline__ void ln_rows(v4u xa, v4u xb, bool act, v4u& o0, v4u& o1) {
  constexpr unsigned ONES = 0x3F803F80u;
  const float sa = dot2(xa.w, ONES, dot2(xa.z, ONES, dot2(xa.y, ONES, dot2(xa.x, ONES, 0.f))));
  const float sb = dot2(xb.w, ONES, dot2(xb.z, ONES, dot2(xb.y, ONES, dot2(xb.x, ONES, 0.f))));
  const float mean = half_sum(sa + sb) * (1.0f / D);
  v2f v[8];
#pragma unroll
  for (int k = 0; k < 4; ++k) { v[k] = v2f{bflo(xa[k]), bfhi(xa[k])}; v[4 + k] = v2f{bflo(xb[k]), bfhi(xb[k])}; }
  v2f q0 = {0.f, 0.f}, q1 = {0.f, 0.f};
#pragma unroll
  for (int k = 0; k < 8; k += 2) {
    const v2f d0 = v[k] - mean, d1 = v[k + 1] - mean;
    q0 = __builtin_elementwise_fma(d0, d0, q0); q1 = __builtin_elementwise_fma(d1, d1, q1);
  }
  q0 += q1;
  const float sq = half_sum(act ? q0.x + q0.y : 0.f);
  const float mb = rbf(mean);
#pragma unroll
  for (int k = 0; k < 8; ++k) { const v2f d = v[k] - mb; const unsigned p = pk2(d.x, d.y); v[k] = v2f{bflo(p), bfhi(p)}; }
  const float den = rbf(__builtin_amdgcn_sqrtf(rbf(rbf(sq * (1.0f / D)) + 1e-5f)));
  const float ri = __builtin_amdgcn_rcpf(den);
#pragma unroll
  for (int k = 0; k < 4; ++k) { const v2f a = v[k] * ri, c = v[4 + k] * ri; o0[k] = pk2(a.x, a.y); o1[k] = pk2(c.x, c.y); }
}
__device__ __forceinline__ v4u frag8(const LAS unsigned char* p, int str, int hi) {
  v4u r;
#pragma unroll
  for (int k = 0; k < 4; ++k) {
    const int o = (k >> 1) * hi + (k & 1) * 2 * str;
    r[k] = (unsigned)*(const LAS u16*)(p + o) | ((unsigned)*(const LAS u16*)(p + o + str) << 16);
  }
  return r;
}
__device__ __forceinline__ v4f red16(v4f t) {
#pragma unroll
  for (int k = 0; k < 4; ++k) { float a = t[k]; a += dpp<ROR8>(a); a += dpp<ROR4>(a); a = sw16_add(a, a); a = sw32_add(a, a); t[k] = a; }
  return t;
}

__global__ void __launch_bounds__(512, 2)
fused_decode(const u16* __restrict__ x, const u16* __restrict__ kc, const u16* __restrict__ vc, const u16* __restrict__ wq,
             const u16* __restrict__ wo, const u16* __restrict__ w1, const u16* __restrict__ b1, const u16* __restrict__ w2,
             const u16* __restrict__ b2, u16* __restrict__ out, unsigned char* __restrict__ ws) {
  extern __shared__ __attribute__((aligned(16))) unsigned char smem[];
  LAS unsigned char* L = (LAS unsigned char*)smem;
  const int w = blockIdx.x, tid = threadIdx.x, lane = tid & 63, wv = __builtin_amdgcn_readfirstlane(tid >> 6);
  const int kvh = w & 7, b = (w >> 3) & 7, s = w >> 6, grp = b * 8 + kvh;
  volatile LAS unsigned* FL = (volatile LAS unsigned*)(L + L_FLAG);
  const __amdgpu_buffer_rsrc_t rws = __builtin_amdgcn_make_buffer_rsrc((void*)ws, 0, WS_END, RSRC3);
  asm volatile("" :: "s"(x), "s"(kc), "s"(vc), "s"(wq), "s"(wo), "s"(w1), "s"(b1), "s"(w2), "s"(b2), "s"(out), "s"(ws));

  if (__builtin_expect(wv >= 4, 1)) {
    const int hw = wv - 4;
    STAMP(0);
    if (PURE) { if (tid < 256 + 16) FL[tid - 256] = 4u; if (PURE != 3) BAR(); return; }
    __builtin_amdgcn_s_setprio(HPRIO);
    const int r5 = w >> 3;
    const bool isW = r5 < 14;
    const bool isM = !isW && r5 < 28;
    const int t = r5 >> 1, hh = r5 & 1, m = kvh * 14 + (r5 - 14);
    const bool isR = isW && hh == 0 && hw == 0;
    const bool isF = isW && hw >= 2;
    const int cg = r5 * 2 + (hw & 1);
    const int row = hw * 2 + (lane >> 5), c = lane & 31, g = lane >> 4;
    const __amdgpu_buffer_rsrc_t rx = __builtin_amdgcn_make_buffer_rsrc((void*)x, 0, 8 * D * 2, RSRC3);
    const int xo = c < 28 ? row * (D * 2) + c * 32 : 8 * D * 2;
    const v4u xa = LD128(rx, xo, 0, 0), xb = LD128(rx, xo + 16, 0, 0);
    const v2u ep = __builtin_amdgcn_raw_buffer_load_b64(rws, WS_EPOCH, 0, AUX_SC1);
    unsigned wr[16];
    {
      const __amdgpu_buffer_rsrc_t rq = __builtin_amdgcn_make_buffer_rsrc((void*)wq, 0, NOWQ ? 0 : D * NQ * 2, RSRC3);
      const int vo = (((lane >> 3) & 1) * 32 + g * 8) * (NQ * 2) + (kvh * 512 + (b * 4 + s) * 16 + (lane & 7) * 2) * 2;
#pragma unroll
      for (int i = 0; i < 16; ++i) wr[i] = __builtin_amdgcn_raw_buffer_load_b32(rq, vo, (hw * 128 + (i >> 3) * 64 + (i & 7)) * (NQ * 2), Q_AUX);
    }
    if (tid < 256 + 16) FL[tid - 256] = 0u;
    SB();
    BAR();
    const int p_p0 = hw < 2 ? WS_P0 + (grp * 4 + s) * 2048 + hw * 1024 + lane * 16 : WS_END;
    const int p_pw = (isW && (lane & 8) == 0) ? WS_PW + (((lane & 7) * 7 + t) * 16 + kvh * 2 + hh) * 256 + hw * 64 + g * 16 : WS_END;
    const int p_h = (isR && lane < 16) ? WS_H + (kvh * D + t * 64 + lane * 4) * 2 : WS_END;
    const int p_pf = isM ? WS_PF + (((lane & 7) * 28 + hw * 7 + ((lane >> 3) & 1)) * 112 + m) * 64 + g * 16 : WS_END;
    const int p_pf3 = (lane & 8) ? WS_END : p_pf + 6 * 7168;
    {
      const v4u sent4 = {SENT, SENT, SENT, SENT};
      ST128(sent4, rws, hw < 2 ? p_p0 : (hw == 2 && lane < 4) ? WS_ML + (grp * 16 + s * 4 + lane) * 16 : WS_END, 0, AUX_SC1);
      if (isW) {
        ST128(sent4, rws, p_pw, 0, AUX_SC1);
        __builtin_amdgcn_raw_buffer_store_b64(v2u{SENT, SENT}, rws, p_h, 0, AUX_SC1);
      } else if (isM && !NORESET) {
#pragma unroll
        for (int i = 0; i < 3; ++i) ST128(sent4, rws, p_pf, i * 2 * 7168, AUX_SC1);
        ST128(sent4, rws, p_pf3, 0, AUX_SC1);
      }
    }
    SB();
    {
      v4u o0, o1;
      ln_rows(xa, xb, c < 28, o0, o1);
      const int a = c < 28 ? L_LN + row * LN_STR + c * 32 : L_SINK + lane * 32;
      *(LAS v4u*)(L + a) = o0; *(LAS v4u*)(L + a + 16) = o1;
    }
    lds_signal(L, FL_LN, lane, 1u);
    STAMP(1);
    const u64 e64 = (((u64)ep.y << 32) | ep.x) + 1ull;
    const unsigned tagl = __builtin_amdgcn_readfirstlane((unsigned)(e64 << 16)), tagh = __builtin_amdgcn_readfirstlane((unsigned)(e64 >> 16));
    const unsigned enl = __builtin_amdgcn_readfirstlane((unsigned)e64), enh = __builtin_amdgcn_readfirstlane((unsigned)(e64 >> 32));
    {
      v4u be[2], bo[2];
#pragma unroll
      for (int st = 0; st < 2; ++st)
#pragma unroll
        for (int k = 0; k < 4; ++k) {
          be[st][k] = __builtin_amdgcn_perm(wr[st * 8 + 2 * k + 1], wr[st * 8 + 2 * k], 0x05040100u);
          bo[st][k] = __builtin_amdgcn_perm(wr[st * 8 + 2 * k + 1], wr[st * 8 + 2 * k], 0x07060302u);
        }
      lds_wait_ge(FL + FL_LN, 4u, 0);
      const LAS unsigned char* ap = L + L_LN + (lane & 7) * LN_STR + (((lane >> 3) & 1) * 32 + g * 8) * 2 + hw * 256;
      const v4u a0 = *(const LAS v4u*)ap;
      const v4u a1 = *(const LAS v4u*)(ap + (hw == 3 ? 0 : 128));
      v4f de = {0.f, 0.f, 0.f, 0.f}, dd = {0.f, 0.f, 0.f, 0.f};
      de = mfma(a0, be[0], de); dd = mfma(a0, bo[0], dd);
      if (hw != 3) { de = mfma(a1, be[1], de); dd = mfma(a1, bo[1], dd); }
      const bool val = ((lane >> 3) & 1) == (lane >> 5);
      const int qa = val ? L_QP + hw * 512 + (((lane >> 4) & 1) * 4) * 64 + (lane & 7) * 8 : L_SINK + lane * 8;
      LAS unsigned char* qp = L + qa + ((val && lane >= 32) ? (L_BF - L_QP) : 0);
#pragma unroll
      for (int r = 0; r < 4; ++r) *(LAS v2f*)(qp + (val ? r * 64 : 0)) = v2f{de[r], dd[r]};
      VM0();
      lds_signal(L, FL_QP, lane, 1u);
    }
    if (hw == 0) {
      lds_wait_ge(FL + FL_QP, 4u, 0);
      v2f a = {0.f, 0.f};
#pragma unroll
      for (int i = 0; i < 4; ++i) a += *(const LAS v2f*)(L + L_QP + i * 512 + lane * 8) + *(const LAS v2f*)(L + L_BF + i * 512 + lane * 8);
      const unsigned pq = pk2(a.x, a.y);
      const v4u gr = {tagl | (pq & 0xffffu), tagh, tagl | (pq >> 16), tagh};
      ST128(gr, rws, WS_G + ((kvh * 8 + (lane >> 3)) * 512 + (b * 4 + s) * 16 + (lane & 7) * 2) * 8, 0, AUX_SC1);
      STAMP(2);
    }
    if (HSTOP == 2) { lds_signal(L, FL_Q, lane, 1u); return; }
    {
      const int off = WS_G + (kvh * 8 + b) * 4096 + (hw * 64 + lane) * 16;
      v4u gq;
#pragma nounroll
      for (unsigned sp = 0; sp < SPIN_CAP; ++sp) {
        CLOB();
        gq = LD128(rws, off, 0, AUX_SC1);
        const bool ok = (((gq.x & 0xffff0000u) ^ tagl) | (gq.y ^ tagh) | ((gq.z & 0xffff0000u) ^ tagl) | (gq.w ^ tagh)) == 0u;
        if (__all(ok)) break;
        if (QSLEEP) __builtin_amdgcn_s_sleep(QSLEEP);
      }
      *(LAS unsigned*)(L + L_Q + (hw * 64 + lane) * 4) = (gq.x & 0xffffu) | (gq.z << 16);
      lds_signal(L, FL_Q, lane, 1u);
    }
    STAMP(3);
    if (HSTOP == 3) return;
    v4u Fr[18];
    v4f cx = {0.f, 0.f, 0.f, 0.f}, cb = {0.f, 0.f, 0.f, 0.f};
#pragma unroll
    for (int i = 0; i < 18; ++i) Fr[i] = v4u{0u, 0u, 0u, 0u};
    if (isW) {
      const __amdgpu_buffer_rsrc_t ro = __builtin_amdgcn_make_buffer_rsrc((void*)wo, 0, NOSTAGE ? 0 : NQ * D * 2, RSRC3);
#pragma unroll 1
      for (int it = 0; it < 2; ++it) {
        v4u tt[4];
#pragma unroll
        for (int i = 0; i < 4; ++i) tt[i] = LD128(ro, (lane >> 3) * (D * 2) + (lane & 7) * 16, (kvh * 512 + hh * 256 + it * 128 + hw * 32 + i * 8) * (D * 2) + t * 128, W_AUX);
#pragma unroll
        for (int i = 0; i < 4; ++i) *(LAS v4u*)(L + L_RAW + (it * 128 + hw * 32 + i * 8) * 128 + lane * 16) = tt[i];
      }
      const v2u xv = __builtin_amdgcn_raw_buffer_load_b64(rx, (kvh * D + t * 64 + (lane & 15) * 4) * 2, 0, 0);
      const v2u bv = *(const GAS v2u*)(b2 + cg * 16 + (lane & 3) * 4);
      cx = v4f{bflo(xv.x), bfhi(xv.x), bflo(xv.y), bfhi(xv.y)};
      cb = v4f{bflo(bv.x), bfhi(bv.x), bflo(bv.y), bfhi(bv.y)};
      lds_signal(L, FL_RAW, lane, 1u);
      lds_wait_ge(FL + FL_RAW, 4u, 1);
#pragma unroll
      for (int ks = 0; ks < 8; ++ks) Fr[ks] = frag8(L + L_RAW + (ks * 32 + g * 4) * 128 + (hw * 16 + (lane & 15)) * 2, 128, 16 * 128);
    } else if (isM) {
      const __amdgpu_buffer_rsrc_t r1 = __builtin_amdgcn_make_buffer_rsrc((void*)w1, 0, NOSTAGE ? 0 : D * F * 2, RSRC3);
      const __amdgpu_buffer_rsrc_t r2 = __builtin_amdgcn_make_buffer_rsrc((void*)(w2 + m * 16 * D), 0, NOSTAGE ? 0 : 16 * D * 2, RSRC3);
      v4u tt[4], uu[4];
#pragma unroll
      for (int i = 0; i < 4; ++i) tt[i] = LD128(r1, (lane >> 1) * (F * 2) + (lane & 1) * 16, (i * 4 + hw) * 32 * (F * 2) + m * 32, W_AUX);
#pragma unroll
      for (int i = 0; i < 4; ++i) *(LAS v4u*)(L + L_RAW + (i * 4 + hw) * 1024 + lane * 16) = tt[i];
#pragma unroll
      for (int i = 0; i < 4; ++i) uu[i] = LD128(r2, lane * 16, (i * 4 + hw) * 1024, W_AUX);
#pragma unroll
      for (int i = 0; i < 4; ++i) *(LAS v4u*)(L + L_RAW + 16384 + (i * 4 + hw) * 1024 + lane * 16) = uu[i];
      const v2u bv = *(const GAS v2u*)(b1 + m * 16 + g * 4);
      cb = v4f{bflo(bv.x), bfhi(bv.x), bflo(bv.y), bfhi(bv.y)};
      lds_signal(L, FL_RAW, lane, 1u);
      lds_wait_ge(FL + FL_RAW, 4u, 1);
#pragma unroll
      for (int ks = 0; ks < 14; ++ks) Fr[ks] = frag8(L + L_RAW + (ks * 32 + g * 8) * 32 + (lane & 15) * 2, 32, 4 * 32);
#pragma unroll
      for (int i = 0; i < 4; ++i) Fr[14 + i] = frag8(L + L_RAW + 16384 + g * 4 * 896 + ((hw * 7 + 2 * i) * 16 + (lane & 15)) * 2, 896, i < 3 ? 32 : 0);
    }
    STAMP(4);
    if (HSTOP == 4) { asm volatile("" :: "v"(Fr[0]), "v"(Fr[7]), "v"(Fr[13]), "v"(Fr[17])); return; }
    {
      const int ww = hw * 64 + lane;
      const int off = WS_G + ((ww >> 5) * 8 * 512 + (ww & 31) * 16) * 8;
#pragma nounroll
      for (unsigned sp = 0; sp < SPIN_CAP; ++sp) {
        CLOB();
        const v2u g2 = __builtin_amdgcn_raw_buffer_load_b64(rws, off, 0, AUX_SC1);
        if (__all((((g2.x & 0xffff0000u) ^ tagl) | (g2.y ^ tagh)) == 0u)) break;
        __builtin_amdgcn_s_sleep(16);
      }
      lds_signal(L, FL_OK, lane, 1u);
    }
    if (hw == 0) {
      lds_wait_ge(FL + FL_ML, 4u, 1);
      const v4f mlv = *(const LAS v4f*)(L + L_ML + (lane & 3) * 16);
      ST128(canon4(mlv), rws, lane < 4 ? WS_ML + (grp * 16 + s * 4 + lane) * 16 : WS_END, 0, AUX_SC1);
    }
    if (hw == 1) lds_wait_ge(FL + FL_ML, 4u, 1);
    float fw[4];
    {
      const int off = lane < 16 ? WS_ML + (grp * 16 + lane) * 16 + (hw & 1) * 8 : WS_END;
      v2u ml;
#pragma nounroll
      for (unsigned sp = 0; sp < SPIN_CAP; ++sp) {
        CLOB();
        ml = __builtin_amdgcn_raw_buffer_load_b64(rws, off, 0, AUX_SC1);
        if (__all(umax(ml.x, ml.y) != SENT) || hw >= 2) break;
        __builtin_amdgcn_s_sleep(64);
      }
      const float mv = __uint_as_float(ml.x), lv = __uint_as_float(ml.y);
      float M = mv;
      M = fmaxf(M, dpp<XOR1>(M)); M = fmaxf(M, dpp<XOR2>(M)); M = fmaxf(M, dpp<HMIR>(M)); M = fmaxf(M, dpp<ROR8>(M));
      const float e = __builtin_amdgcn_exp2f((mv - M) * SC2);
      float Ls = e * lv;
      Ls += dpp<XOR1>(Ls); Ls += dpp<XOR2>(Ls); Ls += dpp<HMIR>(Ls); Ls += dpp<ROR8>(Ls);
      const float f = e / Ls;
#pragma unroll
      for (int i = 0; i < 4; ++i) fw[i] = __builtin_bit_cast(float, __builtin_amdgcn_readlane(__builtin_bit_cast(int, f), s * 4 + i));
    }
    lds_wait_ge(FL + FL_OK, 4u, 1);
    STAMP(5);
    if (HSTOP == 5) { asm volatile("" :: "v"(Fr[0]), "v"(Fr[7]), "v"(Fr[13]), "v"(Fr[17]), "v"(fw[0])); return; }
    const int g_p0 = WS_P0 + (((lane & 7) * 8 + kvh) * 4 + ((lane >> 3) & 1) * 2) * 2048 + (hh * 256 + hw * 64 + g * 4) * 4;
    const int g_pw = WS_PW + (kvh * 7 + t) * 4096 + lane * 16;
    const int g_pf = WS_PF + (kvh * 28 + cg) * 7168 + lane * 16;
    const int g_h = isM ? (c < 28 ? WS_H + row * (D * 2) + c * 32 : WS_END) : WS_H + (kvh * D + cg * 16 + (lane & 3) * 4) * 2;
    const int a_ln = c < 28 ? L_LN + row * LN_STR + c * 32 : L_SINK + lane * 32;
    const int g_o = lane < 4 ? (kvh * D + cg * 16 + lane * 4) * 2 : 8 * D * 2;
    const int g_e = (w == 0 && hw == 2 && lane == 0) ? WS_EPOCH : WS_END;
    const __amdgpu_buffer_rsrc_t rout = __builtin_amdgcn_make_buffer_rsrc((void*)out, 0, 8 * D * 2, RSRC3);
#pragma unroll 1
    for (int pass = DRYRUN ? 0 : 1; pass < (HSTOP == 6 ? 1 : 2); ++pass) {
      int real = __builtin_amdgcn_readfirstlane(pass);
      asm volatile("" : "+s"(real));
      const int oob = real ? 0 : WS_END;
      if (real) {
        lds_wait_ge(FL + FL_PRE, 4u, 1);
        STAMP(6);
        const unsigned t0 = __builtin_amdgcn_raw_buffer_load_b32(rws, isW ? p_pw : p_pf, 0, AUX_SC1), t1 = __builtin_amdgcn_raw_buffer_load_b32(rws, isW ? p_p0 : p_pf3, 0, AUX_SC1);
        const unsigned t2 = __builtin_amdgcn_raw_buffer_load_b32(rws, isW ? (hw < 2 ? g_pw : g_pf) : p_p0, 0, AUX_SC1), t3 = __builtin_amdgcn_raw_buffer_load_b32(rout, g_o, 0, 0);
        asm volatile("" :: "v"(t0), "v"(t1), "v"(t2), "v"(t3));
      }
      if (hw < 2) {
        lds_wait_ge(FL + FL_DONE, real ? 4u : 0u, 0);
        STAMPR(7);
        const LAS v4f* mp = (const LAS v4f*)(L + L_MRG + hw * 1024 + lane * 16);
        const v4f o = (mp[0] * fw[0] + mp[128] * fw[1]) + (mp[256] * fw[2] + mp[384] * fw[3]);
        ST128(canon4(o), rws, p_p0, oob, AUX_SC1);
        STAMPR(8);
      }
      if (isW) {
        v4u pv[8];
#pragma nounroll
        for (unsigned sp = 0; sp < SPIN_CAP; ++sp) {
          CLOB();
#pragma unroll
          for (int i = 0; i < 8; ++i) pv[i] = LD128(rws, g_p0 + (i & 3) * 64 + (i >> 2) * 2048, oob, AUX_SC1);
          unsigned mm = max4(pv[0]);
#pragma unroll
          for (int i = 1; i < 8; ++i) mm = umax(mm, max4(pv[i]));
          if (__all(mm != SENT)) break;
        }
        STAMPR(9);
        v4f sv[4];
#pragma unroll
        for (int i = 0; i < 4; ++i) {
          sv[i] = asf(pv[i]) + asf(pv[4 + i]);
#pragma unroll
          for (int k = 0; k < 4; ++k) sv[i][k] += dpp<ROR8>(sv[i][k]);
        }
        *(LAS v4u*)(L + L_BF + hw * 2048 + lane * 16) = v4u{pk2(sv[0].x, sv[0].y), pk2(sv[0].z, sv[0].w), pk2(sv[1].x, sv[1].y), pk2(sv[1].z, sv[1].w)};
        *(LAS v4u*)(L + L_BF + hw * 2048 + 1024 + lane * 16) = v4u{pk2(sv[2].x, sv[2].y), pk2(sv[2].z, sv[2].w), pk2(sv[3].x, sv[3].y), pk2(sv[3].z, sv[3].w)};
        if (real) BAR();
        v4u bf[8];
#pragma unroll
        for (int ks = 0; ks < 8; ++ks) bf[ks] = *(const LAS v4u*)(L + L_BF + ks * 1024 + lane * 16);
        SB();
        v4f d0 = {0.f, 0.f, 0.f, 0.f}, d1 = {0.f, 0.f, 0.f, 0.f};
#pragma unroll
        for (int ks = 0; ks < 8; ks += 2) { d0 = mfma(Fr[ks], bf[ks], d0); d1 = mfma(Fr[ks + 1], bf[ks + 1], d1); }
        ST128(canon4(d0 + d1), rws, p_pw, oob, AUX_SC1);
        STAMPR(10);
        if (isR) {
          v4u u[4];
#pragma nounroll
          for (unsigned sp = 0; sp < SPIN_CAP; ++sp) {
            CLOB();
#pragma unroll
            for (int i = 0; i < 4; ++i) u[i] = LD128(rws, g_pw + i * 1024, oob, AUX_SC1);
            if (__all(umax(umax(max4(u[0]), max4(u[1])), umax(max4(u[2]), max4(u[3]))) != SENT)) break;
          }
          STAMPR(11);
          v4f hv = (asf(u[0]) + asf(u[1])) + (asf(u[2]) + asf(u[3]));
#pragma unroll
          for (int k = 0; k < 4; ++k) { float a = hv[k]; a = sw16_add(a, a); a = sw32_add(a, a); hv[k] = cx[k] + rbf(a); }
          __builtin_amdgcn_raw_buffer_store_b64(v2u{canonu(pk2(hv.x, hv.y)), canonu(pk2(hv.z, hv.w))}, rws, p_h, oob, AUX_SC1);
          STAMPR(12);
        }
        if (isF) {
          v4u u[7];
          v2u hq;
#pragma nounroll
          for (unsigned sp = 0; sp < SPIN_CAP; ++sp) {
            CLOB();
            hq = __builtin_amdgcn_raw_buffer_load_b64(rws, g_h, oob, AUX_SC1);
            if (__all(umax(hq.x, hq.y) != SENT)) break;
            __builtin_amdgcn_s_sleep(2);
          }
#pragma nounroll
          for (unsigned sp = 0; sp < SPIN_CAP; ++sp) {
            CLOB();
#pragma unroll
            for (int i = 0; i < 7; ++i) u[i] = LD128(rws, g_pf + i * 1024, oob, AUX_SC1);
            unsigned mm = max4(u[0]);
#pragma unroll
            for (int i = 1; i < 7; ++i) mm = umax(mm, max4(u[i]));
            if (__all(mm != SENT)) break;
          }
          STAMPR(13);
          v4f ov = red16(((asf(u[0]) + asf(u[1])) + (asf(u[2]) + asf(u[3]))) + ((asf(u[4]) + asf(u[5])) + asf(u[6])));
          const v4f hv = {bflo(hq.x), bfhi(hq.x), bflo(hq.y), bfhi(hq.y)};
#pragma unroll
          for (int k = 0; k < 4; ++k) ov[k] = hv[k] + rbf(rbf(ov[k]) + cb[k]);
          __builtin_amdgcn_raw_buffer_store_b64(v2u{pk2(ov.x, ov.y), pk2(ov.z, ov.w)}, rout, g_o, oob, 0);
          __builtin_amdgcn_raw_buffer_store_b64(v2u{enl, enh}, rws, g_e, oob, AUX_SC1);
          STAMPR(14);
        }
      } else if (isM) {
        v4u ha, hb;
#pragma nounroll
        for (unsigned sp = 0; sp < SPIN_CAP; ++sp) {
          CLOB();
          ha = LD128(rws, g_h, oob, AUX_SC1); hb = LD128(rws, g_h + 16, oob, AUX_SC1);
          if (__all(umax(max4(ha), max4(hb)) != SENT)) break;
        }
        STAMPR(15);
        v4u o0, o1;
        ln_rows(ha, hb, c < 28, o0, o1);
        *(LAS v4u*)(L + a_ln) = o0; *(LAS v4u*)(L + a_ln + 16) = o1;
        if (real) BAR();
        const LAS unsigned char* bp = L + L_LN + (lane & 7) * LN_STR + g * 16;
        v4u bf[14];
#pragma unroll
        for (int ks = 0; ks < 14; ++ks) bf[ks] = *(const LAS v4u*)(bp + ks * 64);
        SB();
        v4f d0 = {0.f, 0.f, 0.f, 0.f}, d1 = {0.f, 0.f, 0.f, 0.f};
#pragma unroll
        for (int ks = 0; ks < 14; ks += 2) { d0 = mfma(Fr[ks], bf[ks], d0); d1 = mfma(Fr[ks + 1], bf[ks + 1], d1); }
        d0 += d1;
        float u[4];
#pragma unroll
        for (int k = 0; k < 4; ++k) { float a = rbf(rbf(d0[k]) + cb[k]); a = a > 0.f ? a : 0.f; u[k] = a * a; }
        const s16x4 hb4 = __builtin_bit_cast(s16x4, v2u{pk2(u[0], u[1]), pk2(u[2], u[3])});
        v4f e[8];
#pragma unroll
        for (int i = 0; i < 7; ++i) {
          const v2u af = (i & 1) ? v2u{Fr[14 + (i >> 1)].z, Fr[14 + (i >> 1)].w} : v2u{Fr[14 + (i >> 1)].x, Fr[14 + (i >> 1)].y};
          e[i] = __builtin_amdgcn_mfma_f32_16x16x16bf16_1k(__builtin_bit_cast(s16x4, af), hb4, v4f{0.f, 0.f, 0.f, 0.f}, 0, 0, 0);
        }
        e[7] = e[6];
        v4u ev[4];
        const bool up = lane & 8;
#pragma unroll
        for (int i = 0; i < 4; ++i) {
          const v4f ea = e[2 * i], eb = e[2 * i + 1];
          ev[i] = v4u{canon(up ? eb.x : ea.x), canon(up ? eb.y : ea.y), canon(up ? eb.z : ea.z), canon(up ? eb.w : ea.w)};
        }
        SB();
#pragma unroll
        for (int i = 0; i < 3; ++i) ST128(ev[i], rws, p_pf + i * 2 * 7168, oob, AUX_SC1);
        ST128(ev[3], rws, p_pf3, oob, AUX_SC1);
        STAMPR(16);
      }
    }
  } else {
    const size_t kvoff = ((size_t)grp * SK + (size_t)s * 1024) * DH;
    const __amdgpu_buffer_rsrc_t rk = __builtin_amdgcn_make_buffer_rsrc((void*)(kc + kvoff), 0, 524288, RSRC3);
    const int voff = lane * 16 + wv * 4096;
    v4u X[NEXTRA * 4][4], R[4][4];
    const unsigned tb = (unsigned)__builtin_amdgcn_s_memrealtime() + (unsigned)(T1);
#define LANDED() asm volatile("s_waitcnt vmcnt(%0)" :: "i"(4 * (LEAD - 1)) : "memory")
    STAMP(0);
    if (PURE != 3) BAR();
#pragma unroll
    for (int n = 0; n < NEXTRA * 4 + LEAD; ++n) {
      if (n >= LEAD) LANDED();
      pace(tb, n);
      issue_turn(n < NEXTRA * 4 ? X[n] : R[n - NEXTRA * 4], rk, voff, n * 16384);
    }
    const bool b0 = lane & 1, b1_ = lane & 2, b2_ = lane & 4, b3 = lane & 8;
    const int hf = lane >> 5, c = lane & 31;
    LAS float* P = (LAS float*)(L + L_P) + wv * 512;
    LAS float* Pl = P + ((lane >> 3) & 3) * 16 + hf * 8 + (lane & 7);
    STAMP(1);
    if (PURE != 3) lds_wait_ge(FL + FL_Q, 4u, 0);
    STAMP(2);
    const v4u q0 = *(const LAS v4u*)(L + L_Q + c * 16), q1 = *(const LAS v4u*)(L + L_Q + 512 + c * 16);
    float mx = -INFINITY;
#pragma unroll
    for (int g = 0; g < NEXTRA; ++g) {
      float r[4], rx[4];
#pragma unroll
      for (int d = 0; d < 4; ++d) {
        rx[d] = k_turn(X[4 * g + d], q0, q1, b0, b1_, b2_);
        SB();
        LANDED();
        pace(tb, (NEXTRA + g) * 4 + d + LEAD);
        issue_turn(R[(d + LEAD) & 3], rk, voff, ((NEXTRA + g) * 4 + d + LEAD) * 16384);
        r[d] = k_turn(R[d], q0, q1, b0, b1_, b2_);
        SB();
      }
      const float sx = k_group(rx[0], rx[1], rx[2], rx[3], b3), sc = k_group(r[0], r[1], r[2], r[3], b3);
      mx = fmaxf(mx, fmaxf(sx, sc));
      Pl[g * 64] = sx; Pl[(NEXTRA + g) * 64] = sc;
      SB();
    }
    STAMP(3);
    const __amdgpu_buffer_rsrc_t rv = __builtin_amdgcn_make_buffer_rsrc((void*)(vc + kvoff), 0, 524288, RSRC3);
#pragma unroll 1
    for (int g = 2 * NEXTRA; g < 8; ++g) {
      float r[4];
#pragma unroll
      for (int d = 0; d < 4; ++d) {
        const int nx = g * 4 + d + LEAD;
        const bool tov = (d + LEAD >= 4) && g == 7;
        const __amdgpu_buffer_rsrc_t rn = __builtin_amdgcn_make_buffer_rsrc((void*)((tov ? vc : kc) + kvoff), 0, 524288, RSRC3);
        LANDED();
        pace(tb, nx);
        issue_turn(R[(d + LEAD) & 3], rn, voff, (nx & 31) * 16384);
        r[d] = k_turn(R[d], q0, q1, b0, b1_, b2_);
        SB();
      }
      const float sc = k_group(r[0], r[1], r[2], r[3], b3);
      mx = fmaxf(mx, sc);
      Pl[g * 64] = sc;
    }
    STAMP(4);
    mx = fmaxf(mx, dpp<XOR2>(mx)); mx = fmaxf(mx, dpp<ROR4>(mx)); mx = fmaxf(mx, dpp<ROR8>(mx));
    mx = sw16_max(mx); mx = sw32_max(mx);
    const float msub = mx == -INFINITY ? 0.f : mx;
    float ls = 0.f;
    {
      float sv[8];
#pragma unroll
      for (int g = 0; g < 8; ++g) sv[g] = Pl[g * 64];
#pragma unroll
      for (int g = 0; g < 8; ++g) { const float p = __builtin_amdgcn_exp2f((sv[g] - msub) * SC2); ls += p; Pl[g * 64] = p; }
    }
    ls += dpp<XOR2>(ls); ls += dpp<ROR8>(ls); ls += dpp<ROR4>(ls);
    ls = sw16_add(ls, ls); ls = sw32_add(ls, ls);
    *(LAS v2f*)(L + (lane < 2 ? L_ML + (wv * 2 + lane) * 8 : L_SINK + 1024 + lane * 8)) = v2f{mx, ls};
    lds_signal(L, FL_ML, lane, 1u);
    v2f a0[4], a1[4];
#pragma unroll
    for (int k = 0; k < 4; ++k) { a0[k] = v2f{0.f, 0.f}; a1[k] = v2f{0.f, 0.f}; }
    const LAS v4f* Pr = (const LAS v4f*)(P + hf * 8);
    LAS v4f* mo = (LAS v4f*)(L + L_MRG + wv * 2048 + lane * 32);
#pragma unroll 1
    for (int g = 0; g < 8; ++g) {
      v4f pa[4], pb[4];
#pragma unroll
      for (int d = 0; d < 4; ++d) { pa[d] = Pr[(g * 4 + d) * 4]; pb[d] = Pr[(g * 4 + d) * 4 + 1]; }
#pragma unroll
      for (int d = 0; d < 4; ++d) {
        LANDED();
        pace(tb, 32 + g * 4 + d + LEAD);
        issue_turn<V_AUX>(R[(d + LEAD) & 3], rv, voff, (g * 4 + d + LEAD) * 16384);
        v_turn(R[d], pa[d], pb[d], a0, a1);
        SB();
        if (d == 1) lds_signal(L, FL_PRE, lane, g == 7 ? 1u : 0u);
        if (d == 3) {
          mo[0] = v4f{sw32_add(a0[0].x, a1[0].x), sw32_add(a0[0].y, a1[0].y), sw32_add(a0[1].x, a1[1].x), sw32_add(a0[1].y, a1[1].y)};
          mo[1] = v4f{sw32_add(a0[2].x, a1[2].x), sw32_add(a0[2].y, a1[2].y), sw32_add(a0[3].x, a1[3].x), sw32_add(a0[3].y, a1[3].y)};
          lds_signal(L, FL_DONE, lane, g == 7 ? 1u : 0u);
          SB();
        }
      }
    }
    STAMP(5);
  }
}
}

extern "C" void kernel_launch(void* const* d_in, const int* in_sizes, int n_in, void* d_out, int out_size, void* d_ws,
                              size_t ws_size, hipStream_t stream) {
  using namespace fk;
  (void)hipFuncSetAttribute((const void*)fused_decode, hipFuncAttributeMaxDynamicSharedMemorySize, L_END + DBG_STAMPS * 2048);
  hipLaunchKernelGGL(fused_decode, dim3(256), dim3(512), L_END + DBG_STAMPS * 2048, stream, (const u16*)d_in[0], (const u16*)d_in[1],
                     (const u16*)d_in[2], (const u16*)d_in[3], (const u16*)d_in[4], (const u16*)d_in[6], (const u16*)d_in[7],
                     (const u16*)d_in[8], (const u16*)d_in[9], (u16*)d_out, (unsigned char*)d_ws);
}
```

```cpp
#define QWAIT 40
#define QSLEEP 8
#define LATEW2 1
#define DRYRUN 0
#include <hip/hip_runtime.h>
#include <hip/hip_bf16.h>
#include <stdint.h>

#define DBG_STAMPS 0
#define W_AUX 2
#define Q_AUX 2
#define NEXTRA 2
#define RING 4
#define PACE 2
#define PRETOUCH 1
#define TAU 0
#define T1 0
#define NOSTAGE 0
#define NOWQ 0
#define NORESET 0
#define LEAD 2
#define PURE 0
#define HSTOP 0
#define K_AUX 2
#define V_AUX 2
#define X_AUX 2
#define FWAIT 16
#define PSLEEP 0
#define HPRIO 3

namespace fk {
typedef unsigned short u16;
typedef unsigned long long u64;
typedef unsigned v4u __attribute__((ext_vector_type(4)));
typedef unsigned v2u __attribute__((ext_vector_type(2)));
typedef float v4f __attribute__((ext_vector_type(4)));
typedef float v2f __attribute__((ext_vector_type(2)));
typedef __bf16 bf16x2 __attribute__((ext_vector_type(2)));
typedef __bf16 bf16x8 __attribute__((ext_vector_type(8)));
typedef short s16x4 __attribute__((ext_vector_type(4)));
#define LAS __attribute__((address_space(3)))
#define GAS __attribute__((address_space(1)))

constexpr int D = 448, F = 1792, SK = 4096, DH = 256, NQ = 4096;
constexpr unsigned SENT = 0xFFFFFFFFu;
constexpr int AUX_NT = 2, AUX_SC1 = 16;
constexpr int RSRC3 = 0x00020000;
constexpr float SC2 = 0.0625f * 1.4426950408889634f;
constexpr unsigned SPIN_CAP = 1u << 16;

constexpr int WS_EPOCH = 0;
constexpr int WS_G = 4096;
constexpr int WS_ML = WS_G + 8 * 8 * 128 * 16;
constexpr int WS_H = WS_ML + 64 * 8 * 16;
constexpr int WS_P0 = WS_H + 16384;
constexpr int WS_PW = WS_P0 + 64 * 4 * 2048;
constexpr int WS_PF = WS_PW + 8 * 28 * 2048;
constexpr int WS_END = WS_PF + 8 * 28 * 2048;
constexpr int WS_DBG = WS_END;

constexpr int LN_STR = 912;
constexpr int L_P = 0;
constexpr int L_MRG = 8192;
constexpr int L_Q = 16384;
constexpr int L_FLAG = 17408;
constexpr int L_QP = L_FLAG + 64;
constexpr int L_LN = L_QP + 2048;
constexpr int L_BF = L_LN + 8 * LN_STR;
constexpr int L_SINK = L_BF + 4096;
constexpr int L_RAW = L_SINK + 2048;
constexpr int L_END = 96 * 1024;
static_assert(L_RAW + 57344 <= L_END, "lds");
static_assert(L_QP % 16 == 0 && L_LN % 16 == 0 && L_BF % 16 == 0 && L_RAW % 16 == 0, "align");
enum { FL_LN = 0, FL_QP = 1, FL_Q = 2, FL_PRE = 3, FL_DONE = 4, FL_RAW = 5, FL_RAW2 = 6, FL_RAW3 = 7, FL_ML = 9 };
constexpr int L_ML = L_QP;

__device__ __forceinline__ float bflo(unsigned v) { return __builtin_bit_cast(float, v << 16); }
__device__ __forceinline__ float bfhi(unsigned v) { return __builtin_bit_cast(float, v & 0xffff0000u); }
__device__ __forceinline__ float rbf(float f) { return (float)(__bf16)f; }
__device__ __forceinline__ unsigned pk2(float lo, float hi) {
  bf16x2 v = {(__bf16)lo, (__bf16)hi};
  return __builtin_bit_cast(unsigned, v);
}
__device__ __forceinline__ float dot2(unsigned a, unsigned b, float c) {
  return __builtin_amdgcn_fdot2_f32_bf16(__builtin_bit_cast(bf16x2, a), __builtin_bit_cast(bf16x2, b), c, false);
}
__device__ __forceinline__ v4f mfma(v4u a, v4u b, v4f c) {
  return __builtin_amdgcn_mfma_f32_16x16x32_bf16(__builtin_bit_cast(bf16x8, a), __builtin_bit_cast(bf16x8, b), c, 0, 0, 0);
}
template <int CTRL> __device__ __forceinline__ float dpp(float x) {
  return __builtin_bit_cast(float, __builtin_amdgcn_mov_dpp(__builtin_bit_cast(int, x), CTRL, 0xf, 0xf, true));
}
constexpr int XOR1 = 0xB1, XOR2 = 0x4E, HMIR = 0x141, ROR4 = 0x124, ROR8 = 0x128;
__device__ __forceinline__ float sw16_add(float a, float b) {
  auto s = __builtin_amdgcn_permlane16_swap(__float_as_uint(a), __float_as_uint(b), false, false);
  return __uint_as_float(s[0]) + __uint_as_float(s[1]);
}
__device__ __forceinline__ float sw32_add(float a, float b) {
  auto s = __builtin_amdgcn_permlane32_swap(__float_as_uint(a), __float_as_uint(b), false, false);
  return __uint_as_float(s[0]) + __uint_as_float(s[1]);
}
__device__ __forceinline__ float sw16_max(float a) {
  auto s = __builtin_amdgcn_permlane16_swap(__float_as_uint(a), __float_as_uint(a), false, false);
  return fmaxf(__uint_as_float(s[0]), __uint_as_float(s[1]));
}
__device__ __forceinline__ float sw32_max(float a) {
  auto s = __builtin_amdgcn_permlane32_swap(__float_as_uint(a), __float_as_uint(a), false, false);
  return fmaxf(__uint_as_float(s[0]), __uint_as_float(s[1]));
}
__device__ __forceinline__ v4u pack24(v4f v, unsigned tag) {
  const unsigned a = __float_as_uint(v.x), b = __float_as_uint(v.y), c = __float_as_uint(v.z), d = __float_as_uint(v.w);
  return v4u{__builtin_amdgcn_perm(b, a, 0x05030201u), __builtin_amdgcn_perm(c, b, 0x06050302u), __builtin_amdgcn_perm(d, c, 0x07060503u), tag};
}
__device__ __forceinline__ v4f unpack24(v4u g) {
  return v4f{__uint_as_float(g.x << 8), __uint_as_float(__builtin_amdgcn_perm(g.y, g.x, 0x0504030Cu)),
             __uint_as_float(__builtin_amdgcn_perm(g.z, g.y, 0x0403020Cu)), __uint_as_float(g.z & 0xFFFFFF00u)};
}
__device__ __forceinline__ v4f asf(v4u v) { return __builtin_bit_cast(v4f, v); }
#define LGKM0() asm volatile("s_waitcnt lgkmcnt(0)" ::: "memory")
#define VM0() asm volatile("s_waitcnt vmcnt(0)" ::: "memory")
#define CLOB() asm volatile("" ::: "memory")
#define SB() __builtin_amdgcn_sched_barrier(0)
#define BAR() do { asm volatile("s_waitcnt lgkmcnt(0)" ::: "memory"); __builtin_amdgcn_s_barrier(); asm volatile("" ::: "memory"); __builtin_amdgcn_sched_barrier(0); } while (0)
#define LD128(rs, vo, so, aux) __builtin_amdgcn_raw_buffer_load_b128(rs, vo, so, aux)
#define ST128(v, rs, vo, so, aux) __builtin_amdgcn_raw_buffer_store_b128(v, rs, vo, so, aux)
#define STR2(x) #x
#define STR(x) STR2(x)

#define STAMP(i) do {} while (0)
#define STAMPR(i) do {} while (0)

__device__ __forceinline__ void lds_wait_ge(volatile LAS unsigned* f, unsigned want, int slp) {
#pragma nounroll
  for (unsigned sp = 0; sp < (SPIN_CAP << 4); ++sp) {
    if (*f >= want) break;
    if (slp) __builtin_amdgcn_s_sleep(8);
  }
  CLOB();
}
__device__ __forceinline__ void lds_signal(LAS unsigned char* L, int word, int lane, unsigned inc) {
  LGKM0();
  LAS unsigned* p = (LAS unsigned*)(L + (lane == 0 ? L_FLAG + word * 4 : L_SINK + lane * 4));
  __hip_atomic_fetch_add(p, inc, __ATOMIC_RELAXED, __HIP_MEMORY_SCOPE_WORKGROUP);
}

__device__ __forceinline__ void pace(unsigned tb, int n) {
  if (TAU) {
    const unsigned until = tb + (unsigned)(((n < 64 ? n : 0) * TAU) >> 4);
    asm volatile("8:\n\ts_memrealtime vcc\n\ts_waitcnt lgkmcnt(0)\n\ts_sub_i32 vcc_lo, vcc_lo, %0\n\ts_cmp_lt_i32 vcc_lo, 0\n\ts_cbranch_scc1 8b\n" :: "s"(until) : "scc", "vcc", "memory");
  }
}
template <int AUX = K_AUX> __device__ __forceinline__ void issue_turn(v4u* r, __amdgpu_buffer_rsrc_t rs, int voff, int soff) {
#pragma unroll
  for (int i = 0; i < 4; ++i) r[i] = LD128(rs, voff + i * 1024, soff, AUX);
  SB();
}
__device__ __forceinline__ float k_turn(const v4u* k, v4u q0, v4u q1, bool b0, bool b1, bool b2) {
  if (PURE == 1 || PURE == 3) { const v4u t = (k[0] ^ k[1]) ^ (k[2] ^ k[3]); return __uint_as_float((t.x ^ t.y ^ t.z ^ t.w) & 0x3fffffffu); }
  float x[8];
#pragma unroll
  for (int i = 0; i < 4; ++i) {
    float a0 = dot2(k[i].x, q0.x, 0.f), a1 = dot2(k[i].x, q1.x, 0.f);
    a0 = dot2(k[i].y, q0.y, a0); a1 = dot2(k[i].y, q1.y, a1);
    a0 = dot2(k[i].z, q0.z, a0); a1 = dot2(k[i].z, q1.z, a1);
    a0 = dot2(k[i].w, q0.w, a0); a1 = dot2(k[i].w, q1.w, a1);
    x[2 * i] = a0; x[2 * i + 1] = a1;
  }
  float y[4], z[2];
#pragma unroll
  for (int v = 0; v < 4; ++v) y[v] = (b2 ? x[v + 4] : x[v]) + dpp<HMIR>(b2 ? x[v] : x[v + 4]);
#pragma unroll
  for (int v = 0; v < 2; ++v) z[v] = (b1 ? y[v + 2] : y[v]) + dpp<XOR2>(b1 ? y[v] : y[v + 2]);
  return (b0 ? z[1] : z[0]) + dpp<XOR1>(b0 ? z[0] : z[1]);
}
__device__ __forceinline__ float k_group(float r0, float r1, float r2, float r3, bool b3) {
  const float u0 = (b3 ? r1 : r0) + dpp<ROR8>(b3 ? r0 : r1);
  const float u1 = (b3 ? r3 : r2) + dpp<ROR8>(b3 ? r2 : r3);
  return rbf(sw16_add(u0, u1));
}
__device__ __forceinline__ void v_turn(const v4u* v, v4f pa, v4f pb, v2f (&a0)[4], v2f (&a1)[4]) {
  if (PURE == 1 || PURE == 3) { const v4u t = (v[0] ^ v[1]) ^ (v[2] ^ v[3]); a0[0].x += __uint_as_float((t.x ^ t.y ^ t.z ^ t.w) & 0x3fffffffu) * pa.x; return; }
#pragma unroll
  for (int i = 0; i < 4; ++i) {
    const float p0 = i == 0 ? pa.x : i == 1 ? pa.z : i == 2 ? pb.x : pb.z;
    const float p1 = i == 0 ? pa.y : i == 1 ? pa.w : i == 2 ? pb.y : pb.w;
#pragma unroll
    for (int j = 0; j < 4; ++j) {
      const v2f e = {bflo(v[i][j]), bfhi(v[i][j])};
      a0[j] = __builtin_elementwise_fma(e, v2f{p0, p0}, a0[j]);
      a1[j] = __builtin_elementwise_fma(e, v2f{p1, p1}, a1[j]);
    }
  }
}
__device__ __forceinline__ float half_sum(float v) {
  v += dpp<XOR1>(v); v += dpp<XOR2>(v); v += dpp<HMIR>(v); v += dpp<ROR8>(v);
  return sw16_add(v, v);
}
__device__ __forceinline__ void ln_rows(v4u xa, v4u xb, bool act, v4u& o0, v4u& o1) {
  constexpr unsigned ONES = 0x3F803F80u;
  const float sa = dot2(xa.w, ONES, dot2(xa.z, ONES, dot2(xa.y, ONES, dot2(xa.x, ONES, 0.f))));
  const float sb = dot2(xb.w, ONES, dot2(xb.z, ONES, dot2(xb.y, ONES, dot2(xb.x, ONES, 0.f))));
  const float mean = half_sum(sa + sb) * (1.0f / D);
  v2f v[8];
#pragma unroll
  for (int k = 0; k < 4; ++k) { v[k] = v2f{bflo(xa[k]), bfhi(xa[k])}; v[4 + k] = v2f{bflo(xb[k]), bfhi(xb[k])}; }
  v2f q0 = {0.f, 0.f}, q1 = {0.f, 0.f};
#pragma unroll
  for (int k = 0; k < 8; k += 2) {
    const v2f d0 = v[k] - mean, d1 = v[k + 1] - mean;
    q0 = __builtin_elementwise_fma(d0, d0, q0); q1 = __builtin_elementwise_fma(d1, d1, q1);
  }
  q0 += q1;
  const float sq = half_sum(act ? q0.x + q0.y : 0.f);
  const float mb = rbf(mean);
#pragma unroll
  for (int k = 0; k < 8; ++k) { const v2f d = v[k] - mb; const unsigned p = pk2(d.x, d.y); v[k] = v2f{bflo(p), bfhi(p)}; }
  const float den = rbf(__builtin_amdgcn_sqrtf(rbf(rbf(sq * (1.0f / D)) + 1e-5f)));
  const float ri = __builtin_amdgcn_rcpf(den);
#pragma unroll
  for (int k = 0; k < 4; ++k) { const v2f a = v[k] * ri, c = v[4 + k] * ri; o0[k] = pk2(a.x, a.y); o1[k] = pk2(c.x, c.y); }
}
__device__ __forceinline__ v4u frag8(const LAS unsigned char* p, int str, int hi) {
  v4u r;
#pragma unroll
  for (int k = 0; k < 4; ++k) {
    const int o = (k >> 1) * hi + (k & 1) * 2 * str;
    r[k] = (unsigned)*(const LAS u16*)(p + o) | ((unsigned)*(const LAS u16*)(p + o + str) << 16);
  }
  return r;
}
__device__ __forceinline__ v4f red16(v4f t) {
#pragma unroll
  for (int k = 0; k < 4; ++k) { float a = t[k]; a += dpp<ROR8>(a); a += dpp<ROR4>(a); a = sw16_add(a, a); a = sw32_add(a, a); t[k] = a; }
  return t;
}

__global__ void __launch_bounds__(512, 2)
fused_decode(const u16* __restrict__ x, const u16* __restrict__ kc, const u16* __restrict__ vc, const u16* __restrict__ wq,
             const u16* __restrict__ wo, const u16* __restrict__ w1, const u16* __restrict__ b1, const u16* __restrict__ w2,
             const u16* __restrict__ b2, u16* __restrict__ out, unsigned char* __restrict__ ws) {
  extern __shared__ __attribute__((aligned(16))) unsigned char smem[];
  LAS unsigned char* L = (LAS unsigned char*)smem;
  const int w = blockIdx.x, tid = threadIdx.x, lane = tid & 63, wv = __builtin_amdgcn_readfirstlane(tid >> 6);
  const int kvh = w & 7, b = (w >> 3) & 7, s = w >> 6, grp = b * 8 + kvh;
  volatile LAS unsigned* FL = (volatile LAS unsigned*)(L + L_FLAG);
  const __amdgpu_buffer_rsrc_t rws = __builtin_amdgcn_make_buffer_rsrc((void*)ws, 0, WS_END, RSRC3);
  asm volatile("" :: "s"(x), "s"(kc), "s"(vc), "s"(wq), "s"(wo), "s"(w1), "s"(b1), "s"(w2), "s"(b2), "s"(out), "s"(ws));

  if (__builtin_expect(wv >= 4, 1)) {
    const int hw = wv - 4;
    STAMP(0);
    if (PURE) { if (tid < 256 + 16) FL[tid - 256] = 4u; if (PURE != 3) BAR(); return; }
    __builtin_amdgcn_s_setprio(HPRIO);
    const int r5 = w >> 3;
    const bool isW = r5 < 28;
    const int m = (r5 - 28) * 8 + kvh;
    const bool isM = !isW && m < 28;
    const int t = r5 >> 2, kq = r5 & 3;
    const int row = hw * 2 + (lane >> 5), c = lane & 31, g = lane >> 4;
    const __amdgpu_buffer_rsrc_t rx = __builtin_amdgcn_make_buffer_rsrc((void*)x, 0, 8 * D * 2, RSRC3);
    const int xo = c < 28 ? row * (D * 2) + c * 32 : 8 * D * 2;
    const v4u xa = LD128(rx, xo, 0, X_AUX), xb = LD128(rx, xo + 16, 0, X_AUX);
    const v2u ep = __builtin_amdgcn_raw_buffer_load_b64(rws, WS_EPOCH, 0, AUX_SC1);
    unsigned wr[16];
    {
      const __amdgpu_buffer_rsrc_t rq = __builtin_amdgcn_make_buffer_rsrc((void*)wq, 0, NOWQ ? 0 : D * NQ * 2, RSRC3);
      const int vo = (((lane >> 3) & 1) * 32 + g * 8) * (NQ * 2) + (kvh * 512 + (b * 4 + s) * 16 + (lane & 7) * 2) * 2;
#pragma unroll
      for (int i = 0; i < 16; ++i) wr[i] = __builtin_amdgcn_raw_buffer_load_b32(rq, vo, (hw * 128 + (i >> 3) * 64 + (i & 7)) * (NQ * 2), Q_AUX);
    }
    if (tid < 256 + 16) FL[tid - 256] = 0u;
    SB();
    BAR();
    {
      v4u o0, o1;
      ln_rows(xa, xb, c < 28, o0, o1);
      const int a = c < 28 ? L_LN + row * LN_STR + c * 32 : L_SINK + lane * 32;
      *(LAS v4u*)(L + a) = o0; *(LAS v4u*)(L + a + 16) = o1;
    }
    lds_signal(L, FL_LN, lane, 1u);
    STAMP(1);
    const u64 e64 = (((u64)ep.y << 32) | ep.x) + 1ull;
    const unsigned tagl = __builtin_amdgcn_readfirstlane((unsigned)e64), tagh = __builtin_amdgcn_readfirstlane((unsigned)(e64 >> 32));
    {
      v4u be[2], bo[2];
#pragma unroll
      for (int st = 0; st < 2; ++st)
#pragma unroll
        for (int k = 0; k < 4; ++k) {
          be[st][k] = __builtin_amdgcn_perm(wr[st * 8 + 2 * k + 1], wr[st * 8 + 2 * k], 0x05040100u);
          bo[st][k] = __builtin_amdgcn_perm(wr[st * 8 + 2 * k + 1], wr[st * 8 + 2 * k], 0x07060302u);
        }
      lds_wait_ge(FL + FL_LN, 4u, 0);
      const LAS unsigned char* ap = L + L_LN + (lane & 7) * LN_STR + (((lane >> 3) & 1) * 32 + g * 8) * 2 + hw * 256;
      const v4u a0 = *(const LAS v4u*)ap;
      const v4u a1 = *(const LAS v4u*)(ap + (hw == 3 ? 0 : 128));
      v4f de = {0.f, 0.f, 0.f, 0.f}, dd = {0.f, 0.f, 0.f, 0.f};
      de = mfma(a0, be[0], de); dd = mfma(a0, bo[0], dd);
      if (hw != 3) { de = mfma(a1, be[1], de); dd = mfma(a1, bo[1], dd); }
      const bool val = ((lane >> 3) & 1) == (lane >> 5);
      const int qa = val ? L_QP + hw * 512 + (((lane >> 4) & 1) * 4) * 64 + (lane & 7) * 8 : L_SINK + lane * 8;
      LAS unsigned char* qp = L + qa + ((val && lane >= 32) ? (L_BF - L_QP) : 0);
#pragma unroll
      for (int r = 0; r < 4; ++r) *(LAS v2f*)(qp + (val ? r * 64 : 0)) = v2f{de[r], dd[r]};
      lds_signal(L, FL_QP, lane, 1u);
    }
    if (hw == 0) {
      lds_wait_ge(FL + FL_QP, 4u, 0);
      v4f a = {0.f, 0.f, 0.f, 0.f};
#pragma unroll
      for (int i = 0; i < 4; ++i) a += *(const LAS v4f*)(L + L_QP + i * 512 + c * 16) + *(const LAS v4f*)(L + L_BF + i * 512 + c * 16);
      ST128((v4u{pk2(a.x, a.y), pk2(a.z, a.w), tagl, tagh}), rws, lane < 32 ? WS_G + ((kvh * 8 + (c >> 2)) * 128 + (b * 4 + s) * 4 + (c & 3)) * 16 : WS_END, 0, AUX_SC1);
      STAMP(2);
    }
    if (hw >= 2) {
      const int off = WS_G + (kvh * 8 + b) * 2048 + ((hw - 2) * 64 + lane) * 16;
      if (QWAIT) __builtin_amdgcn_s_sleep(QWAIT);
      v4u gq;
      bool ok = false;
#pragma nounroll
      for (unsigned sp = 0; sp < SPIN_CAP; ++sp) {
        CLOB();
        if (!ok) { gq = LD128(rws, off, 0, AUX_SC1); ok = ((gq.z ^ tagl) | (gq.w ^ tagh)) == 0u; }
        if (__all(ok)) break;
        if (QSLEEP) __builtin_amdgcn_s_sleep(QSLEEP);
      }
      *(LAS v2u*)(L + L_Q + ((hw - 2) * 64 + lane) * 8) = v2u{gq.x, gq.y};
      lds_signal(L, FL_Q, lane, 2u);
    }
    lds_wait_ge(FL + FL_Q, 4u, 1);
    STAMP(3);
    if (HSTOP == 3) return;
    v4u Fr[28];
    v4f cx = {0.f, 0.f, 0.f, 0.f}, cb = {0.f, 0.f, 0.f, 0.f};
#pragma unroll
    for (int i = 0; i < 28; ++i) Fr[i] = v4u{0u, 0u, 0u, 0u};
    auto stage_w2 = [&]() {
      const __amdgpu_buffer_rsrc_t r2 = __builtin_amdgcn_make_buffer_rsrc((void*)(w2 + m * 64 * D), 0, NOSTAGE ? 0 : 64 * D * 2, RSRC3);
      v4u uu[14];
#pragma unroll
      for (int i = 0; i < 14; ++i) uu[i] = LD128(r2, lane * 16, (i * 4 + hw) * 1024, W_AUX);
      lds_wait_ge(FL + FL_RAW2, 4u, 1);
#pragma unroll
      for (int i = 0; i < 14; ++i) *(LAS v4u*)(L + L_RAW + (i * 4 + hw) * 1024 + lane * 16) = uu[i];
      lds_signal(L, FL_RAW3, lane, 1u);
      lds_wait_ge(FL + FL_RAW3, 4u, 0);
#pragma unroll
      for (int i = 0; i < 14; ++i) Fr[14 + i] = frag8(L + L_RAW + ((i & 1) * 32 + g * 8) * 896 + ((hw * 7 + (i >> 1)) * 16 + (lane & 15)) * 2, 896, 4 * 896);
    };
    if (isW) {
      const __amdgpu_buffer_rsrc_t ro = __builtin_amdgcn_make_buffer_rsrc((void*)wo, 0, NOSTAGE ? 0 : NQ * D * 2, RSRC3);
      v4u tt[4];
#pragma unroll
      for (int i = 0; i < 4; ++i) tt[i] = LD128(ro, (lane >> 3) * (D * 2) + (lane & 7) * 16, (kvh * 512 + kq * 128 + hw * 32 + i * 8) * (D * 2) + t * 128, W_AUX);
      const v2u xv = __builtin_amdgcn_raw_buffer_load_b64(rx, (kvh * D + r5 * 16 + (lane & 3) * 4) * 2, 0, X_AUX);
      const v2u bv = __builtin_nontemporal_load((const GAS v2u*)(b2 + r5 * 16 + (lane & 3) * 4));
#pragma unroll
      for (int i = 0; i < 4; ++i) *(LAS v4u*)(L + L_RAW + (hw * 32 + i * 8) * 128 + lane * 16) = tt[i];
      cx = v4f{bflo(xv.x), bfhi(xv.x), bflo(xv.y), bfhi(xv.y)};
      cb = v4f{bflo(bv.x), bfhi(bv.x), bflo(bv.y), bfhi(bv.y)};
      lds_signal(L, FL_RAW, lane, 1u);
      lds_wait_ge(FL + FL_RAW, 4u, 1);
#pragma unroll
      for (int ks = 0; ks < 4; ++ks) Fr[ks] = frag8(L + L_RAW + (ks * 32 + g * 4) * 128 + (hw * 16 + (lane & 15)) * 2, 128, 16 * 128);
    } else if (isM) {
      const __amdgpu_buffer_rsrc_t r1 = __builtin_amdgcn_make_buffer_rsrc((void*)w1, 0, NOSTAGE ? 0 : D * F * 2, RSRC3);
#pragma unroll 1
      for (int it = 0; it < 7; ++it) {
        v4u tt[2];
#pragma unroll
        for (int i = 0; i < 2; ++i) tt[i] = LD128(r1, (lane >> 3) * (F * 2) + (lane & 7) * 16, ((it * 4 + hw) * 16 + i * 8) * (F * 2) + m * 128, W_AUX);
#pragma unroll
        for (int i = 0; i < 2; ++i) *(LAS v4u*)(L + L_RAW + ((it * 4 + hw) * 16 + i * 8) * 128 + lane * 16) = tt[i];
      }
      const v2u bv = __builtin_nontemporal_load((const GAS v2u*)(b1 + m * 64 + hw * 16 + g * 4));
      cb = v4f{bflo(bv.x), bfhi(bv.x), bflo(bv.y), bfhi(bv.y)};
      lds_signal(L, FL_RAW, lane, 1u);
      lds_wait_ge(FL + FL_RAW, 4u, 1);
#pragma unroll
      for (int ks = 0; ks < 14; ++ks) Fr[ks] = frag8(L + L_RAW + (ks * 32 + g * 8) * 128 + (hw * 16 + (lane & 15)) * 2, 128, 4 * 128);
#pragma unroll
      for (int ks = 0; ks < 14; ++ks) asm volatile("" : "+v"(Fr[ks]));
      lds_signal(L, FL_RAW2, lane, 1u);
      if (!LATEW2) stage_w2();
    }
    STAMP(4);
    float fw[4] = {0.f, 0.f, 0.f, 0.f};
    if (hw < 2) {
      lds_wait_ge(FL + FL_ML, 4u, 1);
      const v2f ml = *(const LAS v2f*)(L + L_ML + ((lane & 3) * 2 + hw) * 8);
      float Ms = ml.x;
      Ms = fmaxf(Ms, dpp<XOR1>(Ms)); Ms = fmaxf(Ms, dpp<XOR2>(Ms));
      const float Msub = Ms == -INFINITY ? 0.f : Ms;
      float Lw = __builtin_amdgcn_exp2f((ml.x - Msub) * SC2) * ml.y;
      Lw += dpp<XOR1>(Lw); Lw += dpp<XOR2>(Lw);
      ST128((v4u{__float_as_uint(Ms), __float_as_uint(Lw), tagl, tagh}), rws, lane == 0 ? WS_ML + ((grp * 2 + hw) * 4 + s) * 16 : WS_END, 0, AUX_SC1);
      const int off = WS_ML + ((grp * 2 + hw) * 4 + (lane & 3)) * 16;
      v4u gm;
#pragma nounroll
      for (unsigned sp = 0; sp < SPIN_CAP; ++sp) {
        __builtin_amdgcn_s_sleep(64);
        CLOB();
        gm = LD128(rws, off, 0, AUX_SC1);
        if (__all(((gm.z ^ tagl) | (gm.w ^ tagh)) == 0u)) break;
      }
      float M = __uint_as_float(gm.x);
      M = fmaxf(M, dpp<XOR1>(M)); M = fmaxf(M, dpp<XOR2>(M));
      float Ls = __builtin_amdgcn_exp2f((__uint_as_float(gm.x) - M) * SC2) * __uint_as_float(gm.y);
      Ls += dpp<XOR1>(Ls); Ls += dpp<XOR2>(Ls);
      const float f = __builtin_amdgcn_exp2f((ml.x - M) * SC2) / Ls;
#pragma unroll
      for (int i = 0; i < 4; ++i) fw[i] = __builtin_bit_cast(float, __builtin_amdgcn_readlane(__builtin_bit_cast(int, f), i));
    }
    STAMP(5);
    if (HSTOP == 5) { asm volatile("" :: "v"(Fr[0]), "v"(Fr[7]), "v"(Fr[13]), "v"(Fr[27]), "v"(fw[0])); return; }
    const int p_p0 = WS_P0 + (grp * 4 + s) * 2048 + hw * 1024 + lane * 16;
    const int p_pw = (lane & 8) == 0 ? WS_PW + (((lane & 7) * 28 + t * 4 + hw) * 32 + kvh * 4 + kq) * 64 + g * 16 : WS_END;
    const int p_h = lane < 4 ? WS_H + (kvh * 112 + r5 * 4 + lane) * 16 : WS_END;
    const int p_pf = WS_PF + (((lane & 7) * 28 + hw * 7 + ((lane >> 3) & 1)) * 32 + m) * 64 + g * 16;
    const int p_pf3 = (lane & 8) ? WS_END : p_pf + 6 * 2048;
    const int g_p0 = WS_P0 + (((lane & 7) * 8 + kvh) * 4 + ((lane >> 3) & 1) * 2) * 2048 + (kq * 128 + hw * 32 + g * 4) * 4;
    const int g_pw = WS_PW + (kvh * 28 + r5) * 2048 + lane * 16;
    const int g_h = WS_H + (row * 112 + (c < 28 ? c : 27)) * 16;
    const int a_ln = c < 28 ? L_LN + row * LN_STR + c * 8 : L_SINK + lane * 8;
    const int g_o = lane < 4 ? (kvh * D + r5 * 16 + lane * 4) * 2 : 8 * D * 2;
    const int g_e = (w == 0 && lane == 0) ? WS_EPOCH : WS_END;
    const __amdgpu_buffer_rsrc_t rout = __builtin_amdgcn_make_buffer_rsrc((void*)out, 0, 8 * D * 2, RSRC3);
#pragma unroll 1
    for (int pass = DRYRUN ? 0 : 1; pass < (HSTOP == 6 ? 1 : 2); ++pass) {
      int real = __builtin_amdgcn_readfirstlane(pass);
      asm volatile("" : "+s"(real));
      const int oob = real ? 0 : WS_END;
      const unsigned tl = real ? tagl : 0u, th = real ? tagh : 0u;
      if (real) { lds_wait_ge(FL + FL_PRE, 4u, 1); STAMP(6); }
      lds_wait_ge(FL + FL_DONE, real ? 4u : 0u, 0);
      if (hw < 2) {
        STAMPR(7);
        const LAS v4f* mp = (const LAS v4f*)(L + L_MRG + hw * 1024 + lane * 16);
        const v4f o = (mp[0] * fw[0] + mp[128] * fw[1]) + (mp[256] * fw[2] + mp[384] * fw[3]);
        ST128(pack24(o, tl), rws, p_p0, oob, AUX_SC1);
        STAMPR(8);
      }
      if (isW) {
        v4u pv[4];
        bool ok = false;
#pragma nounroll
        for (unsigned sp = 0; sp < SPIN_CAP; ++sp) {
          CLOB();
          if (!ok) {
#pragma unroll
            for (int i = 0; i < 4; ++i) pv[i] = LD128(rws, g_p0 + (i & 1) * 64 + (i >> 1) * 2048, oob, AUX_SC1);
            ok = (((pv[0].w ^ tl) | (pv[1].w ^ tl)) | ((pv[2].w ^ tl) | (pv[3].w ^ tl))) == 0u;
          }
          if (__all(ok)) break;
          if (PSLEEP) __builtin_amdgcn_s_sleep(PSLEEP);
        }
        STAMPR(9);
        v4f lo = unpack24(pv[0]) + unpack24(pv[2]), hi = unpack24(pv[1]) + unpack24(pv[3]);
#pragma unroll
        for (int k = 0; k < 4; ++k) { lo[k] += dpp<ROR8>(lo[k]); hi[k] += dpp<ROR8>(hi[k]); }
        *(LAS v4u*)(L + L_BF + hw * 1024 + lane * 16) = v4u{pk2(lo.x, lo.y), pk2(lo.z, lo.w), pk2(hi.x, hi.y), pk2(hi.z, hi.w)};
        if (real) BAR();
        v4u bf[4];
#pragma unroll
        for (int ks = 0; ks < 4; ++ks) bf[ks] = *(const LAS v4u*)(L + L_BF + ks * 1024 + lane * 16);
        SB();
        v4f d0 = {0.f, 0.f, 0.f, 0.f}, d1 = {0.f, 0.f, 0.f, 0.f};
        d0 = mfma(Fr[0], bf[0], d0); d1 = mfma(Fr[1], bf[1], d1); d0 = mfma(Fr[2], bf[2], d0); d1 = mfma(Fr[3], bf[3], d1);
        ST128(pack24(d0 + d1, tl), rws, p_pw, oob, AUX_SC1);
        STAMPR(10);
        if (hw == 0) {
          v4u u0, u1;
          ok = false;
#pragma nounroll
          for (unsigned sp = 0; sp < SPIN_CAP; ++sp) {
            CLOB();
            if (!ok) { u0 = LD128(rws, g_pw, oob, AUX_SC1); u1 = LD128(rws, g_pw + 1024, oob, AUX_SC1); ok = ((u0.w ^ tl) | (u1.w ^ tl)) == 0u; }
            if (__all(ok)) break;
            if (PSLEEP) __builtin_amdgcn_s_sleep(PSLEEP);
          }
          STAMPR(11);
          v4f hv = red16(unpack24(u0) + unpack24(u1));
          const unsigned h01 = pk2(cx.x + rbf(hv.x), cx.y + rbf(hv.y)), h23 = pk2(cx.z + rbf(hv.z), cx.w + rbf(hv.w));
          ST128((v4u{h01, h23, tl, th}), rws, p_h, oob, AUX_SC1);
          STAMPR(12);
          if (real) __builtin_amdgcn_s_sleep(FWAIT);
          const bool has1 = lane < 48;
          ok = false;
#pragma nounroll
          for (unsigned sp = 0; sp < SPIN_CAP; ++sp) {
            CLOB();
            if (!ok) {
              u0 = LD128(rws, g_pw + (WS_PF - WS_PW), oob, AUX_SC1); u1 = LD128(rws, has1 ? g_pw + (WS_PF - WS_PW) + 1024 : WS_END, oob, AUX_SC1);
              ok = ((u0.w ^ tl) | (has1 ? u1.w ^ tl : 0u)) == 0u;
            }
            if (__all(ok)) break;
          }
          STAMPR(13);
          const v4f ov = red16(unpack24(u0) + unpack24(u1));
          const unsigned o01 = pk2(bflo(h01) + rbf(rbf(ov.x) + cb.x), bfhi(h01) + rbf(rbf(ov.y) + cb.y));
          const unsigned o23 = pk2(bflo(h23) + rbf(rbf(ov.z) + cb.z), bfhi(h23) + rbf(rbf(ov.w) + cb.w));
          __builtin_amdgcn_raw_buffer_store_b64(v2u{o01, o23}, rout, g_o, oob, 0);
          __builtin_amdgcn_raw_buffer_store_b64(v2u{tagl, tagh}, rws, g_e, oob, AUX_SC1);
          STAMPR(14);
        }
      } else if (isM) {
        if (LATEW2 && real) stage_w2();
        v4u hg[4];
        bool ok = false;
#pragma nounroll
        for (unsigned sp = 0; sp < SPIN_CAP; ++sp) {
          CLOB();
          if (!ok) {
#pragma unroll
            for (int i = 0; i < 4; ++i) hg[i] = LD128(rws, g_h + i * 448, oob, AUX_SC1);
            unsigned bad = 0u;
#pragma unroll
            for (int i = 0; i < 4; ++i) bad |= (hg[i].z ^ tl) | (hg[i].w ^ th);
            ok = bad == 0u;
          }
          if (__all(ok)) break;
          if (PSLEEP) __builtin_amdgcn_s_sleep(PSLEEP);
        }
        STAMPR(15);
        const bool act = c < 28;
        v4u o0, o1;
        ln_rows(v4u{act ? hg[0].x : 0u, act ? hg[0].y : 0u, act ? hg[1].x : 0u, act ? hg[1].y : 0u},
                v4u{act ? hg[2].x : 0u, act ? hg[2].y : 0u, act ? hg[3].x : 0u, act ? hg[3].y : 0u}, act, o0, o1);
        *(LAS v2u*)(L + a_ln) = v2u{o0.x, o0.y}; *(LAS v2u*)(L + a_ln + 224) = v2u{o0.z, o0.w};
        *(LAS v2u*)(L + a_ln + 448) = v2u{o1.x, o1.y}; *(LAS v2u*)(L + a_ln + 672) = v2u{o1.z, o1.w};
        if (real) BAR();
        const LAS unsigned char* bp = L + L_LN + (lane & 7) * LN_STR + g * 16;
        v4u bf[14];
#pragma unroll
        for (int ks = 0; ks < 14; ++ks) bf[ks] = *(const LAS v4u*)(bp + ks * 64);
        SB();
        v4f d0 = {0.f, 0.f, 0.f, 0.f}, d1 = {0.f, 0.f, 0.f, 0.f};
#pragma unroll
        for (int ks = 0; ks < 14; ks += 2) { d0 = mfma(Fr[ks], bf[ks], d0); d1 = mfma(Fr[ks + 1], bf[ks + 1], d1); }
        d0 += d1;
        float u[4];
#pragma unroll
        for (int k = 0; k < 4; ++k) { float a = rbf(rbf(d0[k]) + cb[k]); a = a > 0.f ? a : 0.f; u[k] = a * a; }
        *(LAS v2u*)(L + L_BF + (lane & 15) * 144 + (hw * 16 + g * 4) * 2) = v2u{pk2(u[0], u[1]), pk2(u[2], u[3])};
        if (real) BAR();
        const v4u h0 = *(const LAS v4u*)(L + L_BF + (lane & 15) * 144 + g * 16), h1 = *(const LAS v4u*)(L + L_BF + (lane & 15) * 144 + 64 + g * 16);
        v4f e[8];
#pragma unroll
        for (int i = 0; i < 7; ++i) e[i] = mfma(Fr[14 + 2 * i], h0, v4f{0.f, 0.f, 0.f, 0.f});
#pragma unroll
        for (int i = 0; i < 7; ++i) e[i] = mfma(Fr[14 + 2 * i + 1], h1, e[i]);
        e[7] = e[6];
        const bool up = lane & 8;
        v4u ev[4];
#pragma unroll
        for (int i = 0; i < 4; ++i) {
          const v4f ea = e[2 * i], eb = e[2 * i + 1];
          ev[i] = pack24(v4f{up ? eb.x : ea.x, up ? eb.y : ea.y, up ? eb.z : ea.z, up ? eb.w : ea.w}, tl);
        }
#pragma unroll
        for (int i = 0; i < 3; ++i) ST128(ev[i], rws, p_pf + i * 4096, oob, AUX_SC1);
        ST128(ev[3], rws, p_pf3, oob, AUX_SC1);
        STAMPR(16);
      }
    }
  } else {
    const size_t kvoff = ((size_t)grp * SK + (size_t)s * 1024) * DH;
    const __amdgpu_buffer_rsrc_t rk = __builtin_amdgcn_make_buffer_rsrc((void*)(kc + kvoff), 0, 524288, RSRC3);
    const int voff = lane * 16 + wv * 4096;
    v4u X[NEXTRA * 4][4], R[4][4];
    const unsigned tb = (unsigned)__builtin_amdgcn_s_memrealtime() + (unsigned)(T1);
#define LANDED() asm volatile("s_waitcnt vmcnt(%0)" :: "i"(4 * (LEAD - 1)) : "memory")
    STAMP(0);
    if (PURE != 3) BAR();
#pragma unroll
    for (int n = 0; n < NEXTRA * 4 + LEAD; ++n) {
      if (n >= LEAD) LANDED();
      pace(tb, n);
      issue_turn(n < NEXTRA * 4 ? X[n] : R[n - NEXTRA * 4], rk, voff, n * 16384);
    }
    const bool b0 = lane & 1, b1_ = lane & 2, b2_ = lane & 4, b3 = lane & 8;
    const int hf = lane >> 5, c = lane & 31;
    LAS float* P = (LAS float*)(L + L_P) + wv * 512;
    LAS float* Pl = P + ((lane >> 3) & 3) * 16 + hf * 8 + (lane & 7);
    STAMP(1);
    if (PURE != 3) lds_wait_ge(FL + FL_Q, 4u, 0);
    STAMP(2);
    const v4u q0 = *(const LAS v4u*)(L + L_Q + c * 16), q1 = *(const LAS v4u*)(L + L_Q + 512 + c * 16);
    float mx = -INFINITY;
#pragma unroll
    for (int g = 0; g < NEXTRA; ++g) {
      float r[4], rx[4];
#pragma unroll
      for (int d = 0; d < 4; ++d) {
        rx[d] = k_turn(X[4 * g + d], q0, q1, b0, b1_, b2_);
        SB();
        LANDED();
        pace(tb, (NEXTRA + g) * 4 + d + LEAD);
        issue_turn(R[(d + LEAD) & 3], rk, voff, ((NEXTRA + g) * 4 + d + LEAD) * 16384);
        r[d] = k_turn(R[d], q0, q1, b0, b1_, b2_);
        SB();
      }
      const float sx = k_group(rx[0], rx[1], rx[2], rx[3], b3), sc = k_group(r[0], r[1], r[2], r[3], b3);
      mx = fmaxf(mx, fmaxf(sx, sc));
      Pl[g * 64] = sx; Pl[(NEXTRA + g) * 64] = sc;
      SB();
    }
    STAMP(3);
    const __amdgpu_buffer_rsrc_t rv = __builtin_amdgcn_make_buffer_rsrc((void*)(vc + kvoff), 0, 524288, RSRC3);
#pragma unroll 1
    for (int g = 2 * NEXTRA; g < 8; ++g) {
      float r[4];
#pragma unroll
      for (int d = 0; d < 4; ++d) {
        const int nx = g * 4 + d + LEAD;
        const bool tov = (d + LEAD >= 4) && g == 7;
        const __amdgpu_buffer_rsrc_t rn = __builtin_amdgcn_make_buffer_rsrc((void*)((tov ? vc : kc) + kvoff), 0, 524288, RSRC3);
        LANDED();
        pace(tb, nx);
        issue_turn(R[(d + LEAD) & 3], rn, voff, (nx & 31) * 16384);
        r[d] = k_turn(R[d], q0, q1, b0, b1_, b2_);
        SB();
      }
      const float sc = k_group(r[0], r[1], r[2], r[3], b3);
      mx = fmaxf(mx, sc);
      Pl[g * 64] = sc;
    }
    STAMP(4);
    mx = fmaxf(mx, dpp<XOR2>(mx)); mx = fmaxf(mx, dpp<ROR4>(mx)); mx = fmaxf(mx, dpp<ROR8>(mx));
    mx = sw16_max(mx); mx = sw32_max(mx);
    const float msub = mx == -INFINITY ? 0.f : mx;
    float ls = 0.f;
    {
      float sv[8];
#pragma unroll
      for (int g = 0; g < 8; ++g) sv[g] = Pl[g * 64];
#pragma unroll
      for (int g = 0; g < 8; ++g) { const float p = __builtin_amdgcn_exp2f((sv[g] - msub) * SC2); ls += p; Pl[g * 64] = p; }
    }
    ls += dpp<XOR2>(ls); ls += dpp<ROR8>(ls); ls += dpp<ROR4>(ls);
    ls = sw16_add(ls, ls); ls = sw32_add(ls, ls);
    *(LAS v2f*)(L + (lane < 2 ? L_ML + (wv * 2 + lane) * 8 : L_SINK + 1024 + lane * 8)) = v2f{mx, ls};
    lds_signal(L, FL_ML, lane, 1u);
    v2f a0[4], a1[4];
#pragma unroll
    for (int k = 0; k < 4; ++k) { a0[k] = v2f{0.f, 0.f}; a1[k] = v2f{0.f, 0.f}; }
    const LAS v4f* Pr = (const LAS v4f*)(P + hf * 8);
    LAS v4f* mo = (LAS v4f*)(L + L_MRG + wv * 2048 + lane * 32);
#pragma unroll 1
    for (int g = 0; g < 8; ++g) {
      v4f pa[4], pb[4];
#pragma unroll
      for (int d = 0; d < 4; ++d) { pa[d] = Pr[(g * 4 + d) * 4]; pb[d] = Pr[(g * 4 + d) * 4 + 1]; }
#pragma unroll
      for (int d = 0; d < 4; ++d) {
        LANDED();
        pace(tb, 32 + g * 4 + d + LEAD);
        issue_turn<V_AUX>(R[(d + LEAD) & 3], rv, voff, (g * 4 + d + LEAD) * 16384);
        v_turn(R[d], pa[d], pb[d], a0, a1);
        SB();
        if (d == 1) lds_signal(L, FL_PRE, lane, g == 7 ? 1u : 0u);
        if (d == 3) {
          mo[0] = v4f{sw32_add(a0[0].x, a1[0].x), sw32_add(a0[0].y, a1[0].y), sw32_add(a0[1].x, a1[1].x), sw32_add(a0[1].y, a1[1].y)};
          mo[1] = v4f{sw32_add(a0[2].x, a1[2].x), sw32_add(a0[2].y, a1[2].y), sw32_add(a0[3].x, a1[3].x), sw32_add(a0[3].y, a1[3].y)};
          lds_signal(L, FL_DONE, lane, g == 7 ? 1u : 0u);
          SB();
        }
      }
    }
    STAMP(5);
  }
}
}

extern "C" void kernel_launch(void* const* d_in, const int* in_sizes, int n_in, void* d_out, int out_size, void* d_ws,
                              size_t ws_size, hipStream_t stream) {
  using namespace fk;
  (void)hipFuncSetAttribute((const void*)fused_decode, hipFuncAttributeMaxDynamicSharedMemorySize, L_END + DBG_STAMPS * 2048);
  hipLaunchKernelGGL(fused_decode, dim3(256), dim3(512), L_END + DBG_STAMPS * 2048, stream, (const u16*)d_in[0], (const u16*)d_in[1],
                     (const u16*)d_in[2], (const u16*)d_in[3], (const u16*)d_in[4], (const u16*)d_in[6], (const u16*)d_in[7],
                     (const u16*)d_in[8], (const u16*)d_in[9], (u16*)d_out, (unsigned char*)d_ws);
}
```

```cpp
#define QWAIT 40
#define QSLEEP 8
#define LATEW2 1
#define DRYRUN 0
#define PADAT 0
#include <hip/hip_runtime.h>
#include <hip/hip_bf16.h>
#include <stdint.h>

#define DBG_STAMPS 0
#define W_AUX 2
#define Q_AUX 2
#define NEXTRA 2
#define RING 4
#define PACE 2
#define PRETOUCH 1
#define TAU 0
#define T1 0
#define NOSTAGE 0
#define NOWQ 0
#define NORESET 0
#define LEAD 2
#define PURE 0
#define HSTOP 0
#define K_AUX 2
#define V_AUX 2
#define X_AUX 2
#define FWAIT 16
#define PSLEEP 0
#define PADPT(k) do { if (PADAT == (k)) padt = __builtin_amdgcn_s_memrealtime(); } while (0)
#define HPRIO 3

namespace fk {
typedef unsigned short u16;
typedef unsigned long long u64;
typedef unsigned v4u __attribute__((ext_vector_type(4)));
typedef unsigned v2u __attribute__((ext_vector_type(2)));
typedef float v4f __attribute__((ext_vector_type(4)));
typedef float v2f __attribute__((ext_vector_type(2)));
typedef __bf16 bf16x2 __attribute__((ext_vector_type(2)));
typedef __bf16 bf16x8 __attribute__((ext_vector_type(8)));
typedef short s16x4 __attribute__((ext_vector_type(4)));
#define LAS __attribute__((address_space(3)))
#define GAS __attribute__((address_space(1)))

constexpr int D = 448, F = 1792, SK = 4096, DH = 256, NQ = 4096;
constexpr unsigned SENT = 0xFFFFFFFFu;
constexpr int AUX_NT = 2, AUX_SC1 = 16;
constexpr int RSRC3 = 0x00020000;
constexpr float SC2 = 0.0625f * 1.4426950408889634f;
constexpr unsigned SPIN_CAP = 1u << 16;

constexpr int WS_EPOCH = 0;
constexpr int WS_G = 4096;
constexpr int WS_ML = WS_G + 8 * 8 * 128 * 16;
constexpr int WS_H = WS_ML + 64 * 8 * 16;
constexpr int WS_P0 = WS_H + 16384;
constexpr int WS_PW = WS_P0 + 64 * 4 * 2048;
constexpr int WS_PF = WS_PW + 8 * 28 * 2048;
constexpr int WS_END = WS_PF + 8 * 28 * 2048;
constexpr int WS_DBG = WS_END;

constexpr int LN_STR = 912;
constexpr int L_P = 0;
constexpr int L_MRG = 8192;
constexpr int L_Q = 16384;
constexpr int L_FLAG = 17408;
constexpr int L_QP = L_FLAG + 64;
constexpr int L_LN = L_QP + 2048;
constexpr int L_BF = L_LN + 8 * LN_STR;
constexpr int L_SINK = L_BF + 4096;
constexpr int L_RAW = L_SINK + 2048;
constexpr int L_END = 96 * 1024;
static_assert(L_RAW + 57344 <= L_END, "lds");
static_assert(L_QP % 16 == 0 && L_LN % 16 == 0 && L_BF % 16 == 0 && L_RAW % 16 == 0, "align");
enum { FL_LN = 0, FL_QP = 1, FL_Q = 2, FL_PRE = 3, FL_DONE = 4, FL_RAW = 5, FL_RAW2 = 6, FL_RAW3 = 7, FL_ML = 9 };
constexpr int L_ML = L_QP;

__device__ __forceinline__ float bflo(unsigned v) { return __builtin_bit_cast(float, v << 16); }
__device__ __forceinline__ float bfhi(unsigned v) { return __builtin_bit_cast(float, v & 0xffff0000u); }
__device__ __forceinline__ float rbf(float f) { return (float)(__bf16)f; }
__device__ __forceinline__ unsigned pk2(float lo, float hi) {
  bf16x2 v = {(__bf16)lo, (__bf16)hi};
  return __builtin_bit_cast(unsigned, v);
}
__device__ __forceinline__ float dot2(unsigned a, unsigned b, float c) {
  return __builtin_amdgcn_fdot2_f32_bf16(__builtin_bit_cast(bf16x2, a), __builtin_bit_cast(bf16x2, b), c, false);
}
__device__ __forceinline__ v4f mfma(v4u a, v4u b, v4f c) {
  return __builtin_amdgcn_mfma_f32_16x16x32_bf16(__builtin_bit_cast(bf16x8, a), __builtin_bit_cast(bf16x8, b), c, 0, 0, 0);
}
template <int CTRL> __device__ __forceinline__ float dpp(float x) {
  return __builtin_bit_cast(float, __builtin_amdgcn_mov_dpp(__builtin_bit_cast(int, x), CTRL, 0xf, 0xf, true));
}
constexpr int XOR1 = 0xB1, XOR2 = 0x4E, HMIR = 0x141, ROR4 = 0x124, ROR8 = 0x128;
__device__ __forceinline__ float sw16_add(float a, float b) {
  auto s = __builtin_amdgcn_permlane16_swap(__float_as_uint(a), __float_as_uint(b), false, false);
  return __uint_as_float(s[0]) + __uint_as_float(s[1]);
}
__device__ __forceinline__ float sw32_add(float a, float b) {
  auto s = __builtin_amdgcn_permlane32_swap(__float_as_uint(a), __float_as_uint(b), false, false);
  return __uint_as_float(s[0]) + __uint_as_float(s[1]);
}
__device__ __forceinline__ float sw16_max(float a) {
  auto s = __builtin_amdgcn_permlane16_swap(__float_as_uint(a), __float_as_uint(a), false, false);
  return fmaxf(__uint_as_float(s[0]), __uint_as_float(s[1]));
}
__device__ __forceinline__ float sw32_max(float a) {
  auto s = __builtin_amdgcn_permlane32_swap(__float_as_uint(a), __float_as_uint(a), false, false);
  return fmaxf(__uint_as_float(s[0]), __uint_as_float(s[1]));
}
__device__ __forceinline__ v4u pack24(v4f v, unsigned tag) {
  const unsigned a = __float_as_uint(v.x), b = __float_as_uint(v.y), c = __float_as_uint(v.z), d = __float_as_uint(v.w);
  return v4u{__builtin_amdgcn_perm(b, a, 0x05030201u), __builtin_amdgcn_perm(c, b, 0x06050302u), __builtin_amdgcn_perm(d, c, 0x07060503u), tag};
}
__device__ __forceinline__ v4f unpack24(v4u g) {
  return v4f{__uint_as_float(g.x << 8), __uint_as_float(__builtin_amdgcn_perm(g.y, g.x, 0x0504030Cu)),
             __uint_as_float(__builtin_amdgcn_perm(g.z, g.y, 0x0403020Cu)), __uint_as_float(g.z & 0xFFFFFF00u)};
}
__device__ __forceinline__ v4f asf(v4u v) { return __builtin_bit_cast(v4f, v); }
#define LGKM0() asm volatile("s_waitcnt lgkmcnt(0)" ::: "memory")
#define VM0() asm volatile("s_waitcnt vmcnt(0)" ::: "memory")
#define CLOB() asm volatile("" ::: "memory")
#define SB() __builtin_amdgcn_sched_barrier(0)
#define BAR() do { asm volatile("s_waitcnt lgkmcnt(0)" ::: "memory"); __builtin_amdgcn_s_barrier(); asm volatile("" ::: "memory"); __builtin_amdgcn_sched_barrier(0); } while (0)
#define LD128(rs, vo, so, aux) __builtin_amdgcn_raw_buffer_load_b128(rs, vo, so, aux)
#define ST128(v, rs, vo, so, aux) __builtin_amdgcn_raw_buffer_store_b128(v, rs, vo, so, aux)
#define STR2(x) #x
#define STR(x) STR2(x)

#define STAMP(i) do {} while (0)
#define STAMPR(i) do {} while (0)

__device__ __forceinline__ void lds_wait_ge(volatile LAS unsigned* f, unsigned want, int slp) {
#pragma nounroll
  for (unsigned sp = 0; sp < (SPIN_CAP << 4); ++sp) {
    if (*f >= want) break;
    if (slp) __builtin_amdgcn_s_sleep(8);
  }
  CLOB();
}
__device__ __forceinline__ void lds_signal(LAS unsigned char* L, int word, int lane, unsigned inc) {
  LGKM0();
  LAS unsigned* p = (LAS unsigned*)(L + (lane == 0 ? L_FLAG + word * 4 : L_SINK + lane * 4));
  __hip_atomic_fetch_add(p, inc, __ATOMIC_RELAXED, __HIP_MEMORY_SCOPE_WORKGROUP);
}

__device__ __forceinline__ void pace(unsigned tb, int n) {
  if (TAU) {
    const unsigned until = tb + (unsigned)(((n < 64 ? n : 0) * TAU) >> 4);
    asm volatile("8:\n\ts_memrealtime vcc\n\ts_waitcnt lgkmcnt(0)\n\ts_sub_i32 vcc_lo, vcc_lo, %0\n\ts_cmp_lt_i32 vcc_lo, 0\n\ts_cbranch_scc1 8b\n" :: "s"(until) : "scc", "vcc", "memory");
  }
}
template <int AUX = K_AUX> __device__ __forceinline__ void issue_turn(v4u* r, __amdgpu_buffer_rsrc_t rs, int voff, int soff) {
#pragma unroll
  for (int i = 0; i < 4; ++i) r[i] = LD128(rs, voff + i * 1024, soff, AUX);
  SB();
}
__device__ __forceinline__ float k_turn(const v4u* k, v4u q0, v4u q1, bool b0, bool b1, bool b2) {
  if (PURE == 1 || PURE == 3) { const v4u t = (k[0] ^ k[1]) ^ (k[2] ^ k[3]); return __uint_as_float((t.x ^ t.y ^ t.z ^ t.w) & 0x3fffffffu); }
  float x[8];
#pragma unroll
  for (int i = 0; i < 4; ++i) {
    float a0 = dot2(k[i].x, q0.x, 0.f), a1 = dot2(k[i].x, q1.x, 0.f);
    a0 = dot2(k[i].y, q0.y, a0); a1 = dot2(k[i].y, q1.y, a1);
    a0 = dot2(k[i].z, q0.z, a0); a1 = dot2(k[i].z, q1.z, a1);
    a0 = dot2(k[i].w, q0.w, a0); a1 = dot2(k[i].w, q1.w, a1);
    x[2 * i] = a0; x[2 * i + 1] = a1;
  }
  float y[4], z[2];
#pragma unroll
  for (int v = 0; v < 4; ++v) y[v] = (b2 ? x[v + 4] : x[v]) + dpp<HMIR>(b2 ? x[v] : x[v + 4]);
#pragma unroll
  for (int v = 0; v < 2; ++v) z[v] = (b1 ? y[v + 2] : y[v]) + dpp<XOR2>(b1 ? y[v] : y[v + 2]);
  return (b0 ? z[1] : z[0]) + dpp<XOR1>(b0 ? z[0] : z[1]);
}
__device__ __forceinline__ float k_group(float r0, float r1, float r2, float r3, bool b3) {
  const float u0 = (b3 ? r1 : r0) + dpp<ROR8>(b3 ? r0 : r1);
  const float u1 = (b3 ? r3 : r2) + dpp<ROR8>(b3 ? r2 : r3);
  return rbf(sw16_add(u0, u1));
}
__device__ __forceinline__ void v_turn(const v4u* v, v4f pa, v4f pb, v2f (&a0)[4], v2f (&a1)[4]) {
  if (PURE == 1 || PURE == 3) { const v4u t = (v[0] ^ v[1]) ^ (v[2] ^ v[3]); a0[0].x += __uint_as_float((t.x ^ t.y ^ t.z ^ t.w) & 0x3fffffffu) * pa.x; return; }
#pragma unroll
  for (int i = 0; i < 4; ++i) {
    const float p0 = i == 0 ? pa.x : i == 1 ? pa.z : i == 2 ? pb.x : pb.z;
    const float p1 = i == 0 ? pa.y : i == 1 ? pa.w : i == 2 ? pb.y : pb.w;
#pragma unroll
    for (int j = 0; j < 4; ++j) {
      const v2f e = {bflo(v[i][j]), bfhi(v[i][j])};
      a0[j] = __builtin_elementwise_fma(e, v2f{p0, p0}, a0[j]);
      a1[j] = __builtin_elementwise_fma(e, v2f{p1, p1}, a1[j]);
    }
  }
}
__device__ __forceinline__ float half_sum(float v) {
  v += dpp<XOR1>(v); v += dpp<XOR2>(v); v += dpp<HMIR>(v); v += dpp<ROR8>(v);
  return sw16_add(v, v);
}
__device__ __forceinline__ void ln_rows(v4u xa, v4u xb, bool act, v4u& o0, v4u& o1) {
  constexpr unsigned ONES = 0x3F803F80u;
  const float sa = dot2(xa.w, ONES, dot2(xa.z, ONES, dot2(xa.y, ONES, dot2(xa.x, ONES, 0.f))));
  const float sb = dot2(xb.w, ONES, dot2(xb.z, ONES, dot2(xb.y, ONES, dot2(xb.x, ONES, 0.f))));
  const float mean = half_sum(sa + sb) * (1.0f / D);
  v2f v[8];
#pragma unroll
  for (int k = 0; k < 4; ++k) { v[k] = v2f{bflo(xa[k]), bfhi(xa[k])}; v[4 + k] = v2f{bflo(xb[k]), bfhi(xb[k])}; }
  v2f q0 = {0.f, 0.f}, q1 = {0.f, 0.f};
#pragma unroll
  for (int k = 0; k < 8; k += 2) {
    const v2f d0 = v[k] - mean, d1 = v[k + 1] - mean;
    q0 = __builtin_elementwise_fma(d0, d0, q0); q1 = __builtin_elementwise_fma(d1, d1, q1);
  }
  q0 += q1;
  const float sq = half_sum(act ? q0.x + q0.y : 0.f);
  const float mb = rbf(mean);
#pragma unroll
  for (int k = 0; k < 8; ++k) { const v2f d = v[k] - mb; const unsigned p = pk2(d.x, d.y); v[k] = v2f{bflo(p), bfhi(p)}; }
  const float den = rbf(__builtin_amdgcn_sqrtf(rbf(rbf(sq * (1.0f / D)) + 1e-5f)));
  const float ri = __builtin_amdgcn_rcpf(den);
#pragma unroll
  for (int k = 0; k < 4; ++k) { const v2f a = v[k] * ri, c = v[4 + k] * ri; o0[k] = pk2(a.x, a.y); o1[k] = pk2(c.x, c.y); }
}
__device__ __forceinline__ v4u frag8(const LAS unsigned char* p, int str, int hi) {
  v4u r;
#pragma unroll
  for (int k = 0; k < 4; ++k) {
    const int o = (k >> 1) * hi + (k & 1) * 2 * str;
    r[k] = (unsigned)*(const LAS u16*)(p + o) | ((unsigned)*(const LAS u16*)(p + o + str) << 16);
  }
  return r;
}
__device__ __forceinline__ v4f red16(v4f t) {
#pragma unroll
  for (int k = 0; k < 4; ++k) { float a = t[k]; a += dpp<ROR8>(a); a += dpp<ROR4>(a); a = sw16_add(a, a); a = sw32_add(a, a); t[k] = a; }
  return t;
}

__global__ void __launch_bounds__(512, 2)
fused_decode(const u16* __restrict__ x, const u16* __restrict__ kc, const u16* __restrict__ vc, const u16* __restrict__ wq,
             const u16* __restrict__ wo, const u16* __restrict__ w1, const u16* __restrict__ b1, const u16* __restrict__ w2,
             const u16* __restrict__ b2, u16* __restrict__ out, unsigned char* __restrict__ ws) {
  extern __shared__ __attribute__((aligned(16))) unsigned char smem[];
  LAS unsigned char* L = (LAS unsigned char*)smem;
  const int w = blockIdx.x, tid = threadIdx.x, lane = tid & 63, wv = __builtin_amdgcn_readfirstlane(tid >> 6);
  const int kvh = w & 7, b = (w >> 3) & 7, s = w >> 6, grp = b * 8 + kvh;
  volatile LAS unsigned* FL = (volatile LAS unsigned*)(L + L_FLAG);
  const __amdgpu_buffer_rsrc_t rws = __builtin_amdgcn_make_buffer_rsrc((void*)ws, 0, WS_END, RSRC3);
  asm volatile("" :: "s"(x), "s"(kc), "s"(vc), "s"(wq), "s"(wo), "s"(w1), "s"(b1), "s"(w2), "s"(b2), "s"(out), "s"(ws));

  if (__builtin_expect(wv >= 4, 1)) {
    const int hw = wv - 4;
    u64 padt = 0;
    PADPT(0);
    STAMP(0);
    if (PURE) { if (tid < 256 + 16) FL[tid - 256] = 4u; if (PURE != 3) BAR(); return; }
    __builtin_amdgcn_s_setprio(HPRIO);
    const int r5 = w >> 3;
    const bool isW = r5 < 28;
    const int m = (r5 - 28) * 8 + kvh;
    const bool isM = !isW && m < 28;
    const int t = r5 >> 2, kq = r5 & 3;
    const int row = hw * 2 + (lane >> 5), c = lane & 31, g = lane >> 4;
    const __amdgpu_buffer_rsrc_t rx = __builtin_amdgcn_make_buffer_rsrc((void*)x, 0, 8 * D * 2, RSRC3);
    const int xo = c < 28 ? row * (D * 2) + c * 32 : 8 * D * 2;
    const v4u xa = LD128(rx, xo, 0, X_AUX), xb = LD128(rx, xo + 16, 0, X_AUX);
    const v2u ep = __builtin_amdgcn_raw_buffer_load_b64(rws, WS_EPOCH, 0, AUX_SC1);
    unsigned wr[16];
    {
      const __amdgpu_buffer_rsrc_t rq = __builtin_amdgcn_make_buffer_rsrc((void*)wq, 0, NOWQ ? 0 : D * NQ * 2, RSRC3);
      const int vo = (((lane >> 3) & 1) * 32 + g * 8) * (NQ * 2) + (kvh * 512 + (b * 4 + s) * 16 + (lane & 7) * 2) * 2;
#pragma unroll
      for (int i = 0; i < 16; ++i) wr[i] = __builtin_amdgcn_raw_buffer_load_b32(rq, vo, (hw * 128 + (i >> 3) * 64 + (i & 7)) * (NQ * 2), Q_AUX);
    }
    if (tid < 256 + 16) FL[tid - 256] = 0u;
    SB();
    BAR();
    {
      v4u o0, o1;
      ln_rows(xa, xb, c < 28, o0, o1);
      const int a = c < 28 ? L_LN + row * LN_STR + c * 32 : L_SINK + lane * 32;
      *(LAS v4u*)(L + a) = o0; *(LAS v4u*)(L + a + 16) = o1;
    }
    lds_signal(L, FL_LN, lane, 1u);
    STAMP(1);
    const u64 e64 = (((u64)ep.y << 32) | ep.x) + 1ull;
    const unsigned tagl = __builtin_amdgcn_readfirstlane((unsigned)e64), tagh = __builtin_amdgcn_readfirstlane((unsigned)(e64 >> 32));
    {
      v4u be[2], bo[2];
#pragma unroll
      for (int st = 0; st < 2; ++st)
#pragma unroll
        for (int k = 0; k < 4; ++k) {
          be[st][k] = __builtin_amdgcn_perm(wr[st * 8 + 2 * k + 1], wr[st * 8 + 2 * k], 0x05040100u);
          bo[st][k] = __builtin_amdgcn_perm(wr[st * 8 + 2 * k + 1], wr[st * 8 + 2 * k], 0x07060302u);
        }
      lds_wait_ge(FL + FL_LN, 4u, 0);
      const LAS unsigned char* ap = L + L_LN + (lane & 7) * LN_STR + (((lane >> 3) & 1) * 32 + g * 8) * 2 + hw * 256;
      const v4u a0 = *(const LAS v4u*)ap;
      const v4u a1 = *(const LAS v4u*)(ap + (hw == 3 ? 0 : 128));
      v4f de = {0.f, 0.f, 0.f, 0.f}, dd = {0.f, 0.f, 0.f, 0.f};
      de = mfma(a0, be[0], de); dd = mfma(a0, bo[0], dd);
      if (hw != 3) { de = mfma(a1, be[1], de); dd = mfma(a1, bo[1], dd); }
      const bool val = ((lane >> 3) & 1) == (lane >> 5);
      const int qa = val ? L_QP + hw * 512 + (((lane >> 4) & 1) * 4) * 64 + (lane & 7) * 8 : L_SINK + lane * 8;
      LAS unsigned char* qp = L + qa + ((val && lane >= 32) ? (L_BF - L_QP) : 0);
#pragma unroll
      for (int r = 0; r < 4; ++r) *(LAS v2f*)(qp + (val ? r * 64 : 0)) = v2f{de[r], dd[r]};
      lds_signal(L, FL_QP, lane, 1u);
    }
    if (hw == 0) {
      lds_wait_ge(FL + FL_QP, 4u, 0);
      v4f a = {0.f, 0.f, 0.f, 0.f};
#pragma unroll
      for (int i = 0; i < 4; ++i) a += *(const LAS v4f*)(L + L_QP + i * 512 + c * 16) + *(const LAS v4f*)(L + L_BF + i * 512 + c * 16);
      ST128((v4u{pk2(a.x, a.y), pk2(a.z, a.w), tagl, tagh}), rws, lane < 32 ? WS_G + ((kvh * 8 + (c >> 2)) * 128 + (b * 4 + s) * 4 + (c & 3)) * 16 : WS_END, 0, AUX_SC1);
      STAMP(2);
    }
    if (hw >= 2) {
      const int off = WS_G + (kvh * 8 + b) * 2048 + ((hw - 2) * 64 + lane) * 16;
      if (QWAIT) __builtin_amdgcn_s_sleep(QWAIT);
      v4u gq;
      bool ok = false;
#pragma nounroll
      for (unsigned sp = 0; sp < SPIN_CAP; ++sp) {
        CLOB();
        if (!ok) { gq = LD128(rws, off, 0, AUX_SC1); ok = ((gq.z ^ tagl) | (gq.w ^ tagh)) == 0u; }
        if (__all(ok)) break;
        if (QSLEEP) __builtin_amdgcn_s_sleep(QSLEEP);
      }
      *(LAS v2u*)(L + L_Q + ((hw - 2) * 64 + lane) * 8) = v2u{gq.x, gq.y};
      lds_signal(L, FL_Q, lane, 2u);
    }
    lds_wait_ge(FL + FL_Q, 4u, 1);
    STAMP(3);
    PADPT(3);
    if (HSTOP == 3) return;
    v4u Fr[28];
    v4f cx = {0.f, 0.f, 0.f, 0.f}, cb = {0.f, 0.f, 0.f, 0.f};
#pragma unroll
    for (int i = 0; i < 28; ++i) Fr[i] = v4u{0u, 0u, 0u, 0u};
    auto stage_w2 = [&]() {
      const __amdgpu_buffer_rsrc_t r2 = __builtin_amdgcn_make_buffer_rsrc((void*)(w2 + m * 64 * D), 0, NOSTAGE ? 0 : 64 * D * 2, RSRC3);
      v4u uu[14];
#pragma unroll
      for (int i = 0; i < 14; ++i) uu[i] = LD128(r2, lane * 16, (i * 4 + hw) * 1024, W_AUX);
      lds_wait_ge(FL + FL_RAW2, 4u, 1);
#pragma unroll
      for (int i = 0; i < 14; ++i) *(LAS v4u*)(L + L_RAW + (i * 4 + hw) * 1024 + lane * 16) = uu[i];
      lds_signal(L, FL_RAW3, lane, 1u);
      lds_wait_ge(FL + FL_RAW3, 4u, 0);
#pragma unroll
      for (int i = 0; i < 14; ++i) Fr[14 + i] = frag8(L + L_RAW + ((i & 1) * 32 + g * 8) * 896 + ((hw * 7 + (i >> 1)) * 16 + (lane & 15)) * 2, 896, 4 * 896);
    };
    if (isW) {
      const __amdgpu_buffer_rsrc_t ro = __builtin_amdgcn_make_buffer_rsrc((void*)wo, 0, NOSTAGE ? 0 : NQ * D * 2, RSRC3);
      v4u tt[4];
#pragma unroll
      for (int i = 0; i < 4; ++i) tt[i] = LD128(ro, (lane >> 3) * (D * 2) + (lane & 7) * 16, (kvh * 512 + kq * 128 + hw * 32 + i * 8) * (D * 2) + t * 128, W_AUX);
      const v2u xv = __builtin_amdgcn_raw_buffer_load_b64(rx, (kvh * D + r5 * 16 + (lane & 3) * 4) * 2, 0, X_AUX);
      const v2u bv = __builtin_nontemporal_load((const GAS v2u*)(b2 + r5 * 16 + (lane & 3) * 4));
#pragma unroll
      for (int i = 0; i < 4; ++i) *(LAS v4u*)(L + L_RAW + (hw * 32 + i * 8) * 128 + lane * 16) = tt[i];
      cx = v4f{bflo(xv.x), bfhi(xv.x), bflo(xv.y), bfhi(xv.y)};
      cb = v4f{bflo(bv.x), bfhi(bv.x), bflo(bv.y), bfhi(bv.y)};
      lds_signal(L, FL_RAW, lane, 1u);
      lds_wait_ge(FL + FL_RAW, 4u, 1);
#pragma unroll
      for (int ks = 0; ks < 4; ++ks) Fr[ks] = frag8(L + L_RAW + (ks * 32 + g * 4) * 128 + (hw * 16 + (lane & 15)) * 2, 128, 16 * 128);
    } else if (isM) {
      const __amdgpu_buffer_rsrc_t r1 = __builtin_amdgcn_make_buffer_rsrc((void*)w1, 0, NOSTAGE ? 0 : D * F * 2, RSRC3);
#pragma unroll 1
      for (int it = 0; it < 7; ++it) {
        v4u tt[2];
#pragma unroll
        for (int i = 0; i < 2; ++i) tt[i] = LD128(r1, (lane >> 3) * (F * 2) + (lane & 7) * 16, ((it * 4 + hw) * 16 + i * 8) * (F * 2) + m * 128, W_AUX);
#pragma unroll
        for (int i = 0; i < 2; ++i) *(LAS v4u*)(L + L_RAW + ((it * 4 + hw) * 16 + i * 8) * 128 + lane * 16) = tt[i];
      }
      const v2u bv = __builtin_nontemporal_load((const GAS v2u*)(b1 + m * 64 + hw * 16 + g * 4));
      cb = v4f{bflo(bv.x), bfhi(bv.x), bflo(bv.y), bfhi(bv.y)};
      lds_signal(L, FL_RAW, lane, 1u);
      lds_wait_ge(FL + FL_RAW, 4u, 1);
#pragma unroll
      for (int ks = 0; ks < 14; ++ks) Fr[ks] = frag8(L + L_RAW + (ks * 32 + g * 8) * 128 + (hw * 16 + (lane & 15)) * 2, 128, 4 * 128);
#pragma unroll
      for (int ks = 0; ks < 14; ++ks) asm volatile("" : "+v"(Fr[ks]));
      lds_signal(L, FL_RAW2, lane, 1u);
      if (!LATEW2) stage_w2();
    }
    STAMP(4);
    float fw[4] = {0.f, 0.f, 0.f, 0.f};
    if (hw < 2) {
      lds_wait_ge(FL + FL_ML, 4u, 1);
      PADPT(4);
      const v2f ml = *(const LAS v2f*)(L + L_ML + ((lane & 3) * 2 + hw) * 8);
      float Ms = ml.x;
      Ms = fmaxf(Ms, dpp<XOR1>(Ms)); Ms = fmaxf(Ms, dpp<XOR2>(Ms));
      const float Msub = Ms == -INFINITY ? 0.f : Ms;
      float Lw = __builtin_amdgcn_exp2f((ml.x - Msub) * SC2) * ml.y;
      Lw += dpp<XOR1>(Lw); Lw += dpp<XOR2>(Lw);
      ST128((v4u{__float_as_uint(Ms), __float_as_uint(Lw), tagl, tagh}), rws, lane == 0 ? WS_ML + ((grp * 2 + hw) * 4 + s) * 16 : WS_END, 0, AUX_SC1);
      const int off = WS_ML + ((grp * 2 + hw) * 4 + (lane & 3)) * 16;
      v4u gm;
#pragma nounroll
      for (unsigned sp = 0; sp < SPIN_CAP; ++sp) {
        __builtin_amdgcn_s_sleep(64);
        CLOB();
        gm = LD128(rws, off, 0, AUX_SC1);
        if (__all(((gm.z ^ tagl) | (gm.w ^ tagh)) == 0u)) break;
      }
      float M = __uint_as_float(gm.x);
      M = fmaxf(M, dpp<XOR1>(M)); M = fmaxf(M, dpp<XOR2>(M));
      float Ls = __builtin_amdgcn_exp2f((__uint_as_float(gm.x) - M) * SC2) * __uint_as_float(gm.y);
      Ls += dpp<XOR1>(Ls); Ls += dpp<XOR2>(Ls);
      const float f = __builtin_amdgcn_exp2f((ml.x - M) * SC2) / Ls;
#pragma unroll
      for (int i = 0; i < 4; ++i) fw[i] = __builtin_bit_cast(float, __builtin_amdgcn_readlane(__builtin_bit_cast(int, f), i));
    }
    STAMP(5);
    if (HSTOP == 5) { asm volatile("" :: "v"(Fr[0]), "v"(Fr[7]), "v"(Fr[13]), "v"(Fr[27]), "v"(fw[0])); return; }
    const int p_p0 = WS_P0 + (grp * 4 + s) * 2048 + hw * 1024 + lane * 16;
    const int p_pw = (lane & 8) == 0 ? WS_PW + (((lane & 7) * 28 + t * 4 + hw) * 32 + kvh * 4 + kq) * 64 + g * 16 : WS_END;
    const int p_h = lane < 4 ? WS_H + (kvh * 112 + r5 * 4 + lane) * 16 : WS_END;
    const int p_pf = WS_PF + (((lane & 7) * 28 + hw * 7 + ((lane >> 3) & 1)) * 32 + m) * 64 + g * 16;
    const int p_pf3 = (lane & 8) ? WS_END : p_pf + 6 * 2048;
    const int g_p0 = WS_P0 + (((lane & 7) * 8 + kvh) * 4 + ((lane >> 3) & 1) * 2) * 2048 + (kq * 128 + hw * 32 + g * 4) * 4;
    const int g_pw = WS_PW + (kvh * 28 + r5) * 2048 + lane * 16;
    const int g_h = WS_H + (row * 112 + (c < 28 ? c : 27)) * 16;
    const int a_ln = c < 28 ? L_LN + row * LN_STR + c * 8 : L_SINK + lane * 8;
    const int g_o = lane < 4 ? (kvh * D + r5 * 16 + lane * 4) * 2 : 8 * D * 2;
    const int g_e = (w == 0 && lane == 0) ? WS_EPOCH : WS_END;
    const __amdgpu_buffer_rsrc_t rout = __builtin_amdgcn_make_buffer_rsrc((void*)out, 0, 8 * D * 2, RSRC3);
#pragma unroll 1
    for (int pass = DRYRUN ? 0 : 1; pass < (HSTOP == 6 ? 1 : 2); ++pass) {
      int real = __builtin_amdgcn_readfirstlane(pass);
      asm volatile("" : "+s"(real));
      const int oob = real ? 0 : WS_END;
      const unsigned tl = real ? tagl : 0u, th = real ? tagh : 0u;
      if (real) { lds_wait_ge(FL + FL_PRE, 4u, 1); STAMP(6); }
      lds_wait_ge(FL + FL_DONE, real ? 4u : 0u, 0);
      if (hw < 2) {
        STAMPR(7);
        PADPT(7);
        const LAS v4f* mp = (const LAS v4f*)(L + L_MRG + hw * 1024 + lane * 16);
        const v4f o = (mp[0] * fw[0] + mp[128] * fw[1]) + (mp[256] * fw[2] + mp[384] * fw[3]);
        ST128(pack24(o, tl), rws, p_p0, oob, AUX_SC1);
        STAMPR(8);
      }
      if (isW) {
        v4u pv[4];
        bool ok = false;
#pragma nounroll
        for (unsigned sp = 0; sp < SPIN_CAP; ++sp) {
          CLOB();
          if (!ok) {
#pragma unroll
            for (int i = 0; i < 4; ++i) pv[i] = LD128(rws, g_p0 + (i & 1) * 64 + (i >> 1) * 2048, oob, AUX_SC1);
            ok = (((pv[0].w ^ tl) | (pv[1].w ^ tl)) | ((pv[2].w ^ tl) | (pv[3].w ^ tl))) == 0u;
          }
          if (__all(ok)) break;
          if (PSLEEP) __builtin_amdgcn_s_sleep(PSLEEP);
        }
        STAMPR(9);
        v4f lo = unpack24(pv[0]) + unpack24(pv[2]), hi = unpack24(pv[1]) + unpack24(pv[3]);
#pragma unroll
        for (int k = 0; k < 4; ++k) { lo[k] += dpp<ROR8>(lo[k]); hi[k] += dpp<ROR8>(hi[k]); }
        *(LAS v4u*)(L + L_BF + hw * 1024 + lane * 16) = v4u{pk2(lo.x, lo.y), pk2(lo.z, lo.w), pk2(hi.x, hi.y), pk2(hi.z, hi.w)};
        if (real) BAR();
        v4u bf[4];
#pragma unroll
        for (int ks = 0; ks < 4; ++ks) bf[ks] = *(const LAS v4u*)(L + L_BF + ks * 1024 + lane * 16);
        SB();
        v4f d0 = {0.f, 0.f, 0.f, 0.f}, d1 = {0.f, 0.f, 0.f, 0.f};
        d0 = mfma(Fr[0], bf[0], d0); d1 = mfma(Fr[1], bf[1], d1); d0 = mfma(Fr[2], bf[2], d0); d1 = mfma(Fr[3], bf[3], d1);
        ST128(pack24(d0 + d1, tl), rws, p_pw, oob, AUX_SC1);
        STAMPR(10);
        if (hw == 0) {
          v4u u0, u1;
          ok = false;
#pragma nounroll
          for (unsigned sp = 0; sp < SPIN_CAP; ++sp) {
            CLOB();
            if (!ok) { u0 = LD128(rws, g_pw, oob, AUX_SC1); u1 = LD128(rws, g_pw + 1024, oob, AUX_SC1); ok = ((u0.w ^ tl) | (u1.w ^ tl)) == 0u; }
            if (__all(ok)) break;
            if (PSLEEP) __builtin_amdgcn_s_sleep(PSLEEP);
          }
          STAMPR(11);
          PADPT(11);
          v4f hv = red16(unpack24(u0) + unpack24(u1));
          const unsigned h01 = pk2(cx.x + rbf(hv.x), cx.y + rbf(hv.y)), h23 = pk2(cx.z + rbf(hv.z), cx.w + rbf(hv.w));
          ST128((v4u{h01, h23, tl, th}), rws, p_h, oob, AUX_SC1);
          STAMPR(12);
          if (real) __builtin_amdgcn_s_sleep(FWAIT);
          const bool has1 = lane < 48;
          ok = false;
#pragma nounroll
          for (unsigned sp = 0; sp < SPIN_CAP; ++sp) {
            CLOB();
            if (!ok) {
              u0 = LD128(rws, g_pw + (WS_PF - WS_PW), oob, AUX_SC1); u1 = LD128(rws, has1 ? g_pw + (WS_PF - WS_PW) + 1024 : WS_END, oob, AUX_SC1);
              ok = ((u0.w ^ tl) | (has1 ? u1.w ^ tl : 0u)) == 0u;
            }
            if (__all(ok)) break;
          }
          STAMPR(13);
          const v4f ov = red16(unpack24(u0) + unpack24(u1));
          const unsigned o01 = pk2(bflo(h01) + rbf(rbf(ov.x) + cb.x), bfhi(h01) + rbf(rbf(ov.y) + cb.y));
          const unsigned o23 = pk2(bflo(h23) + rbf(rbf(ov.z) + cb.z), bfhi(h23) + rbf(rbf(ov.w) + cb.w));
          __builtin_amdgcn_raw_buffer_store_b64(v2u{o01, o23}, rout, g_o, oob, 0);
          __builtin_amdgcn_raw_buffer_store_b64(v2u{tagl, tagh}, rws, g_e, oob, AUX_SC1);
          STAMPR(14);
          if (PADAT >= 0 && real) { while (__builtin_amdgcn_s_memrealtime() < padt + 8000) __builtin_amdgcn_s_sleep(1); }
        }
      } else if (isM) {
        if (LATEW2 && real) stage_w2();
        v4u hg[4];
        bool ok = false;
#pragma nounroll
        for (unsigned sp = 0; sp < SPIN_CAP; ++sp) {
          CLOB();
          if (!ok) {
#pragma unroll
            for (int i = 0; i < 4; ++i) hg[i] = LD128(rws, g_h + i * 448, oob, AUX_SC1);
            unsigned bad = 0u;
#pragma unroll
            for (int i = 0; i < 4; ++i) bad |= (hg[i].z ^ tl) | (hg[i].w ^ th);
            ok = bad == 0u;
          }
          if (__all(ok)) break;
          if (PSLEEP) __builtin_amdgcn_s_sleep(PSLEEP);
        }
        STAMPR(15);
        const bool act = c < 28;
        v4u o0, o1;
        ln_rows(v4u{act ? hg[0].x : 0u, act ? hg[0].y : 0u, act ? hg[1].x : 0u, act ? hg[1].y : 0u},
                v4u{act ? hg[2].x : 0u, act ? hg[2].y : 0u, act ? hg[3].x : 0u, act ? hg[3].y : 0u}, act, o0, o1);
        *(LAS v2u*)(L + a_ln) = v2u{o0.x, o0.y}; *(LAS v2u*)(L + a_ln + 224) = v2u{o0.z, o0.w};
        *(LAS v2u*)(L + a_ln + 448) = v2u{o1.x, o1.y}; *(LAS v2u*)(L + a_ln + 672) = v2u{o1.z, o1.w};
        if (real) BAR();
        const LAS unsigned char* bp = L + L_LN + (lane & 7) * LN_STR + g * 16;
        v4u bf[14];
#pragma unroll
        for (int ks = 0; ks < 14; ++ks) bf[ks] = *(const LAS v4u*)(bp + ks * 64);
        SB();
        v4f d0 = {0.f, 0.f, 0.f, 0.f}, d1 = {0.f, 0.f, 0.f, 0.f};
#pragma unroll
        for (int ks = 0; ks < 14; ks += 2) { d0 = mfma(Fr[ks], bf[ks], d0); d1 = mfma(Fr[ks + 1], bf[ks + 1], d1); }
        d0 += d1;
        float u[4];
#pragma unroll
        for (int k = 0; k < 4; ++k) { float a = rbf(rbf(d0[k]) + cb[k]); a = a > 0.f ? a : 0.f; u[k] = a * a; }
        *(LAS v2u*)(L + L_BF + (lane & 15) * 144 + (hw * 16 + g * 4) * 2) = v2u{pk2(u[0], u[1]), pk2(u[2], u[3])};
        if (real) BAR();
        const v4u h0 = *(const LAS v4u*)(L + L_BF + (lane & 15) * 144 + g * 16), h1 = *(const LAS v4u*)(L + L_BF + (lane & 15) * 144 + 64 + g * 16);
        v4f e[8];
#pragma unroll
        for (int i = 0; i < 7; ++i) e[i] = mfma(Fr[14 + 2 * i], h0, v4f{0.f, 0.f, 0.f, 0.f});
#pragma unroll
        for (int i = 0; i < 7; ++i) e[i] = mfma(Fr[14 + 2 * i + 1], h1, e[i]);
        e[7] = e[6];
        const bool up = lane & 8;
        v4u ev[4];
#pragma unroll
        for (int i = 0; i < 4; ++i) {
          const v4f ea = e[2 * i], eb = e[2 * i + 1];
          ev[i] = pack24(v4f{up ? eb.x : ea.x, up ? eb.y : ea.y, up ? eb.z : ea.z, up ? eb.w : ea.w}, tl);
        }
#pragma unroll
        for (int i = 0; i < 3; ++i) ST128(ev[i], rws, p_pf + i * 4096, oob, AUX_SC1);
        ST128(ev[3], rws, p_pf3, oob, AUX_SC1);
        STAMPR(16);
      }
    }
  } else {
    const size_t kvoff = ((size_t)grp * SK + (size_t)s * 1024) * DH;
    const __amdgpu_buffer_rsrc_t rk = __builtin_amdgcn_make_buffer_rsrc((void*)(kc + kvoff), 0, 524288, RSRC3);
    const int voff = lane * 16 + wv * 4096;
    v4u X[NEXTRA * 4][4], R[4][4];
    const unsigned tb = (unsigned)__builtin_amdgcn_s_memrealtime() + (unsigned)(T1);
#define LANDED() asm volatile("s_waitcnt vmcnt(%0)" :: "i"(4 * (LEAD - 1)) : "memory")
    STAMP(0);
    if (PURE != 3) BAR();
#pragma unroll
    for (int n = 0; n < NEXTRA * 4 + LEAD; ++n) {
      if (n >= LEAD) LANDED();
      pace(tb, n);
      issue_turn(n < NEXTRA * 4 ? X[n] : R[n - NEXTRA * 4], rk, voff, n * 16384);
    }
    const bool b0 = lane & 1, b1_ = lane & 2, b2_ = lane & 4, b3 = lane & 8;
    const int hf = lane >> 5, c = lane & 31;
    LAS float* P = (LAS float*)(L + L_P) + wv * 512;
    LAS float* Pl = P + ((lane >> 3) & 3) * 16 + hf * 8 + (lane & 7);
    STAMP(1);
    if (PURE != 3) lds_wait_ge(FL + FL_Q, 4u, 0);
    STAMP(2);
    const v4u q0 = *(const LAS v4u*)(L + L_Q + c * 16), q1 = *(const LAS v4u*)(L + L_Q + 512 + c * 16);
    float mx = -INFINITY;
#pragma unroll
    for (int g = 0; g < NEXTRA; ++g) {
      float r[4], rx[4];
#pragma unroll
      for (int d = 0; d < 4; ++d) {
        rx[d] = k_turn(X[4 * g + d], q0, q1, b0, b1_, b2_);
        SB();
        LANDED();
        pace(tb, (NEXTRA + g) * 4 + d + LEAD);
        issue_turn(R[(d + LEAD) & 3], rk, voff, ((NEXTRA + g) * 4 + d + LEAD) * 16384);
        r[d] = k_turn(R[d], q0, q1, b0, b1_, b2_);
        SB();
      }
      const float sx = k_group(rx[0], rx[1], rx[2], rx[3], b3), sc = k_group(r[0], r[1], r[2], r[3], b3);
      mx = fmaxf(mx, fmaxf(sx, sc));
      Pl[g * 64] = sx; Pl[(NEXTRA + g) * 64] = sc;
      SB();
    }
    STAMP(3);
    const __amdgpu_buffer_rsrc_t rv = __builtin_amdgcn_make_buffer_rsrc((void*)(vc + kvoff), 0, 524288, RSRC3);
#pragma unroll 1
    for (int g = 2 * NEXTRA; g < 8; ++g) {
      float r[4];
#pragma unroll
      for (int d = 0; d < 4; ++d) {
        const int nx = g * 4 + d + LEAD;
        const bool tov = (d + LEAD >= 4) && g == 7;
        const __amdgpu_buffer_rsrc_t rn = __builtin_amdgcn_make_buffer_rsrc((void*)((tov ? vc : kc) + kvoff), 0, 524288, RSRC3);
        LANDED();
        pace(tb, nx);
        issue_turn(R[(d + LEAD) & 3], rn, voff, (nx & 31) * 16384);
        r[d] = k_turn(R[d], q0, q1, b0, b1_, b2_);
        SB();
      }
      const float sc = k_group(r[0], r[1], r[2], r[3], b3);
      mx = fmaxf(mx, sc);
      Pl[g * 64] = sc;
    }
    STAMP(4);
    mx = fmaxf(mx, dpp<XOR2>(mx)); mx = fmaxf(mx, dpp<ROR4>(mx)); mx = fmaxf(mx, dpp<ROR8>(mx));
    mx = sw16_max(mx); mx = sw32_max(mx);
    const float msub = mx == -INFINITY ? 0.f : mx;
    float ls = 0.f;
    {
      float sv[8];
#pragma unroll
      for (int g = 0; g < 8; ++g) sv[g] = Pl[g * 64];
#pragma unroll
      for (int g = 0; g < 8; ++g) { const float p = __builtin_amdgcn_exp2f((sv[g] - msub) * SC2); ls += p; Pl[g * 64] = p; }
    }
    ls += dpp<XOR2>(ls); ls += dpp<ROR8>(ls); ls += dpp<ROR4>(ls);
    ls = sw16_add(ls, ls); ls = sw32_add(ls, ls);
    *(LAS v2f*)(L + (lane < 2 ? L_ML + (wv * 2 + lane) * 8 : L_SINK + 1024 + lane * 8)) = v2f{mx, ls};
    lds_signal(L, FL_ML, lane, 1u);
    v2f a0[4], a1[4];
#pragma unroll
    for (int k = 0; k < 4; ++k) { a0[k] = v2f{0.f, 0.f}; a1[k] = v2f{0.f, 0.f}; }
    const LAS v4f* Pr = (const LAS v4f*)(P + hf * 8);
    LAS v4f* mo = (LAS v4f*)(L + L_MRG + wv * 2048 + lane * 32);
#pragma unroll 1
    for (int g = 0; g < 8; ++g) {
      v4f pa[4], pb[4];
#pragma unroll
      for (int d = 0; d < 4; ++d) { pa[d] = Pr[(g * 4 + d) * 4]; pb[d] = Pr[(g * 4 + d) * 4 + 1]; }
#pragma unroll
      for (int d = 0; d < 4; ++d) {
        LANDED();
        pace(tb, 32 + g * 4 + d + LEAD);
        issue_turn<V_AUX>(R[(d + LEAD) & 3], rv, voff, (g * 4 + d + LEAD) * 16384);
        v_turn(R[d], pa[d], pb[d], a0, a1);
        SB();
        if (d == 1) lds_signal(L, FL_PRE, lane, g == 7 ? 1u : 0u);
        if (d == 3) {
          mo[0] = v4f{sw32_add(a0[0].x, a1[0].x), sw32_add(a0[0].y, a1[0].y), sw32_add(a0[1].x, a1[1].x), sw32_add(a0[1].y, a1[1].y)};
          mo[1] = v4f{sw32_add(a0[2].x, a1[2].x), sw32_add(a0[2].y, a1[2].y), sw32_add(a0[3].x, a1[3].x), sw32_add(a0[3].y, a1[3].y)};
          lds_signal(L, FL_DONE, lane, g == 7 ? 1u : 0u);
          SB();
        }
      }
    }
    STAMP(5);
  }
}
}

extern "C" void kernel_launch(void* const* d_in, const int* in_sizes, int n_in, void* d_out, int out_size, void* d_ws,
                              size_t ws_size, hipStream_t stream) {
  using namespace fk;
  (void)hipFuncSetAttribute((const void*)fused_decode, hipFuncAttributeMaxDynamicSharedMemorySize, L_END + DBG_STAMPS * 2048);
  hipLaunchKernelGGL(fused_decode, dim3(256), dim3(512), L_END + DBG_STAMPS * 2048, stream, (const u16*)d_in[0], (const u16*)d_in[1],
                     (const u16*)d_in[2], (const u16*)d_in[3], (const u16*)d_in[4], (const u16*)d_in[6], (const u16*)d_in[7],
                     (const u16*)d_in[8], (const u16*)d_in[9], (u16*)d_out, (unsigned char*)d_ws);
}
```

```cpp
#define QWAIT 40
#define QSLEEP 8
#define LATEW2 1
#define DRYRUN 0
#define PADAT 100
#define PADW (w == 223)
#include <hip/hip_runtime.h>
#include <hip/hip_bf16.h>
#include <stdint.h>

#define DBG_STAMPS 0
#define W_AUX 2
#define Q_AUX 2
#define NEXTRA 2
#define RING 4
#define PACE 2
#define PRETOUCH 1
#define TAU 0
#define T1 0
#define NOSTAGE 0
#define NOWQ 0
#define NORESET 0
#define LEAD 2
#define PURE 0
#define HSTOP 0
#define K_AUX 2
#define V_AUX 2
#define X_AUX 2
#define FWAIT 16
#define PSLEEP 0
#define PADPT(k) do { if (PADAT == (k)) padt = __builtin_amdgcn_s_memrealtime(); } while (0)
#define HPRIO 3

namespace fk {
typedef unsigned short u16;
typedef unsigned long long u64;
typedef unsigned v4u __attribute__((ext_vector_type(4)));
typedef unsigned v2u __attribute__((ext_vector_type(2)));
typedef float v4f __attribute__((ext_vector_type(4)));
typedef float v2f __attribute__((ext_vector_type(2)));
typedef __bf16 bf16x2 __attribute__((ext_vector_type(2)));
typedef __bf16 bf16x8 __attribute__((ext_vector_type(8)));
typedef short s16x4 __attribute__((ext_vector_type(4)));
#define LAS __attribute__((address_space(3)))
#define GAS __attribute__((address_space(1)))

constexpr int D = 448, F = 1792, SK = 4096, DH = 256, NQ = 4096;
constexpr unsigned SENT = 0xFFFFFFFFu;
constexpr int AUX_NT = 2, AUX_SC1 = 16;
constexpr int RSRC3 = 0x00020000;
constexpr float SC2 = 0.0625f * 1.4426950408889634f;
constexpr unsigned SPIN_CAP = 1u << 16;

constexpr int WS_EPOCH = 0;
constexpr int WS_G = 4096;
constexpr int WS_ML = WS_G + 8 * 8 * 128 * 16;
constexpr int WS_H = WS_ML + 64 * 8 * 16;
constexpr int WS_P0 = WS_H + 16384;
constexpr int WS_PW = WS_P0 + 64 * 4 * 2048;
constexpr int WS_PF = WS_PW + 8 * 28 * 2048;
constexpr int WS_END = WS_PF + 8 * 28 * 2048;
constexpr int WS_DBG = WS_END;

constexpr int LN_STR = 912;
constexpr int L_P = 0;
constexpr int L_MRG = 8192;
constexpr int L_Q = 16384;
constexpr int L_FLAG = 17408;
constexpr int L_QP = L_FLAG + 64;
constexpr int L_LN = L_QP + 2048;
constexpr int L_BF = L_LN + 8 * LN_STR;
constexpr int L_SINK = L_BF + 4096;
constexpr int L_RAW = L_SINK + 2048;
constexpr int L_END = 96 * 1024;
static_assert(L_RAW + 57344 <= L_END, "lds");
static_assert(L_QP % 16 == 0 && L_LN % 16 == 0 && L_BF % 16 == 0 && L_RAW % 16 == 0, "align");
enum { FL_LN = 0, FL_QP = 1, FL_Q = 2, FL_PRE = 3, FL_DONE = 4, FL_RAW = 5, FL_RAW2 = 6, FL_RAW3 = 7, FL_ML = 9 };
constexpr int L_ML = L_QP;

__device__ __forceinline__ float bflo(unsigned v) { return __builtin_bit_cast(float, v << 16); }
__device__ __forceinline__ float bfhi(unsigned v) { return __builtin_bit_cast(float, v & 0xffff0000u); }
__device__ __forceinline__ float rbf(float f) { return (float)(__bf16)f; }
__device__ __forceinline__ unsigned pk2(float lo, float hi) {
  bf16x2 v = {(__bf16)lo, (__bf16)hi};
  return __builtin_bit_cast(unsigned, v);
}
__device__ __forceinline__ float dot2(unsigned a, unsigned b, float c) {
  return __builtin_amdgcn_fdot2_f32_bf16(__builtin_bit_cast(bf16x2, a), __builtin_bit_cast(bf16x2, b), c, false);
}
__device__ __forceinline__ v4f mfma(v4u a, v4u b, v4f c) {
  return __builtin_amdgcn_mfma_f32_16x16x32_bf16(__builtin_bit_cast(bf16x8, a), __builtin_bit_cast(bf16x8, b), c, 0, 0, 0);
}
template <int CTRL> __device__ __forceinline__ float dpp(float x) {
  return __builtin_bit_cast(float, __builtin_amdgcn_mov_dpp(__builtin_bit_cast(int, x), CTRL, 0xf, 0xf, true));
}
constexpr int XOR1 = 0xB1, XOR2 = 0x4E, HMIR = 0x141, ROR4 = 0x124, ROR8 = 0x128;
__device__ __forceinline__ float sw16_add(float a, float b) {
  auto s = __builtin_amdgcn_permlane16_swap(__float_as_uint(a), __float_as_uint(b), false, false);
  return __uint_as_float(s[0]) + __uint_as_float(s[1]);
}
__device__ __forceinline__ float sw32_add(float a, float b) {
  auto s = __builtin_amdgcn_permlane32_swap(__float_as_uint(a), __float_as_uint(b), false, false);
  return __uint_as_float(s[0]) + __uint_as_float(s[1]);
}
__device__ __forceinline__ float sw16_max(float a) {
  auto s = __builtin_amdgcn_permlane16_swap(__float_as_uint(a), __float_as_uint(a), false, false);
  return fmaxf(__uint_as_float(s[0]), __uint_as_float(s[1]));
}
__device__ __forceinline__ float sw32_max(float a) {
  auto s = __builtin_amdgcn_permlane32_swap(__float_as_uint(a), __float_as_uint(a), false, false);
  return fmaxf(__uint_as_float(s[0]), __uint_as_float(s[1]));
}
__device__ __forceinline__ v4u pack24(v4f v, unsigned tag) {
  const unsigned a = __float_as_uint(v.x), b = __float_as_uint(v.y), c = __float_as_uint(v.z), d = __float_as_uint(v.w);
  return v4u{__builtin_amdgcn_perm(b, a, 0x05030201u), __builtin_amdgcn_perm(c, b, 0x06050302u), __builtin_amdgcn_perm(d, c, 0x07060503u), tag};
}
__device__ __forceinline__ v4f unpack24(v4u g) {
  return v4f{__uint_as_float(g.x << 8), __uint_as_float(__builtin_amdgcn_perm(g.y, g.x, 0x0504030Cu)),
             __uint_as_float(__builtin_amdgcn_perm(g.z, g.y, 0x0403020Cu)), __uint_as_float(g.z & 0xFFFFFF00u)};
}
__device__ __forceinline__ v4f asf(v4u v) { return __builtin_bit_cast(v4f, v); }
#define LGKM0() asm volatile("s_waitcnt lgkmcnt(0)" ::: "memory")
#define VM0() asm volatile("s_waitcnt vmcnt(0)" ::: "memory")
#define CLOB() asm volatile("" ::: "memory")
#define SB() __builtin_amdgcn_sched_barrier(0)
#define BAR() do { asm volatile("s_waitcnt lgkmcnt(0)" ::: "memory"); __builtin_amdgcn_s_barrier(); asm volatile("" ::: "memory"); __builtin_amdgcn_sched_barrier(0); } while (0)
#define LD128(rs, vo, so, aux) __builtin_amdgcn_raw_buffer_load_b128(rs, vo, so, aux)
#define ST128(v, rs, vo, so, aux) __builtin_amdgcn_raw_buffer_store_b128(v, rs, vo, so, aux)
#define STR2(x) #x
#define STR(x) STR2(x)

#define STAMP(i) do {} while (0)
#define STAMPR(i) do {} while (0)

__device__ __forceinline__ void lds_wait_ge(volatile LAS unsigned* f, unsigned want, int slp) {
#pragma nounroll
  for (unsigned sp = 0; sp < (SPIN_CAP << 4); ++sp) {
    if (*f >= want) break;
    if (slp) __builtin_amdgcn_s_sleep(8);
  }
  CLOB();
}
__device__ __forceinline__ void lds_signal(LAS unsigned char* L, int word, int lane, unsigned inc) {
  LGKM0();
  LAS unsigned* p = (LAS unsigned*)(L + (lane == 0 ? L_FLAG + word * 4 : L_SINK + lane * 4));
  __hip_atomic_fetch_add(p, inc, __ATOMIC_RELAXED, __HIP_MEMORY_SCOPE_WORKGROUP);
}

__device__ __forceinline__ void pace(unsigned tb, int n) {
  if (TAU) {
    const unsigned until = tb + (unsigned)(((n < 64 ? n : 0) * TAU) >> 4);
    asm volatile("8:\n\ts_memrealtime vcc\n\ts_waitcnt lgkmcnt(0)\n\ts_sub_i32 vcc_lo, vcc_lo, %0\n\ts_cmp_lt_i32 vcc_lo, 0\n\ts_cbranch_scc1 8b\n" :: "s"(until) : "scc", "vcc", "memory");
  }
}
template <int AUX = K_AUX> __device__ __forceinline__ void issue_turn(v4u* r, __amdgpu_buffer_rsrc_t rs, int voff, int soff) {
#pragma unroll
  for (int i = 0; i < 4; ++i) r[i] = LD128(rs, voff + i * 1024, soff, AUX);
  SB();
}
__device__ __forceinline__ float k_turn(const v4u* k, v4u q0, v4u q1, bool b0, bool b1, bool b2) {
  if (PURE == 1 || PURE == 3) { const v4u t = (k[0] ^ k[1]) ^ (k[2] ^ k[3]); return __uint_as_float((t.x ^ t.y ^ t.z ^ t.w) & 0x3fffffffu); }
  float x[8];
#pragma unroll
  for (int i = 0; i < 4; ++i) {
    float a0 = dot2(k[i].x, q0.x, 0.f), a1 = dot2(k[i].x, q1.x, 0.f);
    a0 = dot2(k[i].y, q0.y, a0); a1 = dot2(k[i].y, q1.y, a1);
    a0 = dot2(k[i].z, q0.z, a0); a1 = dot2(k[i].z, q1.z, a1);
    a0 = dot2(k[i].w, q0.w, a0); a1 = dot2(k[i].w, q1.w, a1);
    x[2 * i] = a0; x[2 * i + 1] = a1;
  }
  float y[4], z[2];
#pragma unroll
  for (int v = 0; v < 4; ++v) y[v] = (b2 ? x[v + 4] : x[v]) + dpp<HMIR>(b2 ? x[v] : x[v + 4]);
#pragma unroll
  for (int v = 0; v < 2; ++v) z[v] = (b1 ? y[v + 2] : y[v]) + dpp<XOR2>(b1 ? y[v] : y[v + 2]);
  return (b0 ? z[1] : z[0]) + dpp<XOR1>(b0 ? z[0] : z[1]);
}
__device__ __forceinline__ float k_group(float r0, float r1, float r2, float r3, bool b3) {
  const float u0 = (b3 ? r1 : r0) + dpp<ROR8>(b3 ? r0 : r1);
  const float u1 = (b3 ? r3 : r2) + dpp<ROR8>(b3 ? r2 : r3);
  return rbf(sw16_add(u0, u1));
}
__device__ __forceinline__ void v_turn(const v4u* v, v4f pa, v4f pb, v2f (&a0)[4], v2f (&a1)[4]) {
  if (PURE == 1 || PURE == 3) { const v4u t = (v[0] ^ v[1]) ^ (v[2] ^ v[3]); a0[0].x += __uint_as_float((t.x ^ t.y ^ t.z ^ t.w) & 0x3fffffffu) * pa.x; return; }
#pragma unroll
  for (int i = 0; i < 4; ++i) {
    const float p0 = i == 0 ? pa.x : i == 1 ? pa.z : i == 2 ? pb.x : pb.z;
    const float p1 = i == 0 ? pa.y : i == 1 ? pa.w : i == 2 ? pb.y : pb.w;
#pragma unroll
    for (int j = 0; j < 4; ++j) {
      const v2f e = {bflo(v[i][j]), bfhi(v[i][j])};
      a0[j] = __builtin_elementwise_fma(e, v2f{p0, p0}, a0[j]);
      a1[j] = __builtin_elementwise_fma(e, v2f{p1, p1}, a1[j]);
    }
  }
}
__device__ __forceinline__ float half_sum(float v) {
  v += dpp<XOR1>(v); v += dpp<XOR2>(v); v += dpp<HMIR>(v); v += dpp<ROR8>(v);
  return sw16_add(v, v);
}
__device__ __forceinline__ void ln_rows(v4u xa, v4u xb, bool act, v4u& o0, v4u& o1) {
  constexpr unsigned ONES = 0x3F803F80u;
  const float sa = dot2(xa.w, ONES, dot2(xa.z, ONES, dot2(xa.y, ONES, dot2(xa.x, ONES, 0.f))));
  const float sb = dot2(xb.w, ONES, dot2(xb.z, ONES, dot2(xb.y, ONES, dot2(xb.x, ONES, 0.f))));
  const float mean = half_sum(sa + sb) * (1.0f / D);
  v2f v[8];
#pragma unroll
  for (int k = 0; k < 4; ++k) { v[k] = v2f{bflo(xa[k]), bfhi(xa[k])}; v[4 + k] = v2f{bflo(xb[k]), bfhi(xb[k])}; }
  v2f q0 = {0.f, 0.f}, q1 = {0.f, 0.f};
#pragma unroll
  for (int k = 0; k < 8; k += 2) {
    const v2f d0 = v[k] - mean, d1 = v[k + 1] - mean;
    q0 = __builtin_elementwise_fma(d0, d0, q0); q1 = __builtin_elementwise_fma(d1, d1, q1);
  }
  q0 += q1;
  const float sq = half_sum(act ? q0.x + q0.y : 0.f);
  const float mb = rbf(mean);
#pragma unroll
  for (int k = 0; k < 8; ++k) { const v2f d = v[k] - mb; const unsigned p = pk2(d.x, d.y); v[k] = v2f{bflo(p), bfhi(p)}; }
  const float den = rbf(__builtin_amdgcn_sqrtf(rbf(rbf(sq * (1.0f / D)) + 1e-5f)));
  const float ri = __builtin_amdgcn_rcpf(den);
#pragma unroll
  for (int k = 0; k < 4; ++k) { const v2f a = v[k] * ri, c = v[4 + k] * ri; o0[k] = pk2(a.x, a.y); o1[k] = pk2(c.x, c.y); }
}
__device__ __forceinline__ v4u frag8(const LAS unsigned char* p, int str, int hi) {
  v4u r;
#pragma unroll
  for (int k = 0; k < 4; ++k) {
    const int o = (k >> 1) * hi + (k & 1) * 2 * str;
    r[k] = (unsigned)*(const LAS u16*)(p + o) | ((unsigned)*(const LAS u16*)(p + o + str) << 16);
  }
  return r;
}
__device__ __forceinline__ v4f red16(v4f t) {
#pragma unroll
  for (int k = 0; k < 4; ++k) { float a = t[k]; a += dpp<ROR8>(a); a += dpp<ROR4>(a); a = sw16_add(a, a); a = sw32_add(a, a); t[k] = a; }
  return t;
}

__global__ void __launch_bounds__(512, 2)
fused_decode(const u16* __restrict__ x, const u16* __restrict__ kc, const u16* __restrict__ vc, const u16* __restrict__ wq,
             const u16* __restrict__ wo, const u16* __restrict__ w1, const u16* __restrict__ b1, const u16* __restrict__ w2,
             const u16* __restrict__ b2, u16* __restrict__ out, unsigned char* __restrict__ ws) {
  extern __shared__ __attribute__((aligned(16))) unsigned char smem[];
  u64 pad0 = 0;
  if (PADAT == 100) asm volatile("s_memrealtime %0" : "=s"(pad0) :: "memory");
  LAS unsigned char* L = (LAS unsigned char*)smem;
  const int w = blockIdx.x, tid = threadIdx.x, lane = tid & 63, wv = __builtin_amdgcn_readfirstlane(tid >> 6);
  const int kvh = w & 7, b = (w >> 3) & 7, s = w >> 6, grp = b * 8 + kvh;
  volatile LAS unsigned* FL = (volatile LAS unsigned*)(L + L_FLAG);
  const __amdgpu_buffer_rsrc_t rws = __builtin_amdgcn_make_buffer_rsrc((void*)ws, 0, WS_END, RSRC3);
  asm volatile("" :: "s"(x), "s"(kc), "s"(vc), "s"(wq), "s"(wo), "s"(w1), "s"(b1), "s"(w2), "s"(b2), "s"(out), "s"(ws));

  if (__builtin_expect(wv >= 4, 1)) {
    const int hw = wv - 4;
    u64 padt = pad0;
    PADPT(0);
    STAMP(0);
    if (PURE) { if (tid < 256 + 16) FL[tid - 256] = 4u; if (PURE != 3) BAR(); return; }
    __builtin_amdgcn_s_setprio(HPRIO);
    const int r5 = w >> 3;
    const bool isW = r5 < 28;
    const int m = (r5 - 28) * 8 + kvh;
    const bool isM = !isW && m < 28;
    const int t = r5 >> 2, kq = r5 & 3;
    const int row = hw * 2 + (lane >> 5), c = lane & 31, g = lane >> 4;
    const __amdgpu_buffer_rsrc_t rx = __builtin_amdgcn_make_buffer_rsrc((void*)x, 0, 8 * D * 2, RSRC3);
    const int xo = c < 28 ? row * (D * 2) + c * 32 : 8 * D * 2;
    const v4u xa = LD128(rx, xo, 0, X_AUX), xb = LD128(rx, xo + 16, 0, X_AUX);
    const v2u ep = __builtin_amdgcn_raw_buffer_load_b64(rws, WS_EPOCH, 0, AUX_SC1);
    unsigned wr[16];
    {
      const __amdgpu_buffer_rsrc_t rq = __builtin_amdgcn_make_buffer_rsrc((void*)wq, 0, NOWQ ? 0 : D * NQ * 2, RSRC3);
      const int vo = (((lane >> 3) & 1) * 32 + g * 8) * (NQ * 2) + (kvh * 512 + (b * 4 + s) * 16 + (lane & 7) * 2) * 2;
#pragma unroll
      for (int i = 0; i < 16; ++i) wr[i] = __builtin_amdgcn_raw_buffer_load_b32(rq, vo, (hw * 128 + (i >> 3) * 64 + (i & 7)) * (NQ * 2), Q_AUX);
    }
    if (tid < 256 + 16) FL[tid - 256] = 0u;
    SB();
    BAR();
    {
      v4u o0, o1;
      ln_rows(xa, xb, c < 28, o0, o1);
      const int a = c < 28 ? L_LN + row * LN_STR + c * 32 : L_SINK + lane * 32;
      *(LAS v4u*)(L + a) = o0; *(LAS v4u*)(L + a + 16) = o1;
    }
    lds_signal(L, FL_LN, lane, 1u);
    PADPT(1);
    STAMP(1);
    const u64 e64 = (((u64)ep.y << 32) | ep.x) + 1ull;
    const unsigned tagl = __builtin_amdgcn_readfirstlane((unsigned)e64), tagh = __builtin_amdgcn_readfirstlane((unsigned)(e64 >> 32));
    {
      v4u be[2], bo[2];
#pragma unroll
      for (int st = 0; st < 2; ++st)
#pragma unroll
        for (int k = 0; k < 4; ++k) {
          be[st][k] = __builtin_amdgcn_perm(wr[st * 8 + 2 * k + 1], wr[st * 8 + 2 * k], 0x05040100u);
          bo[st][k] = __builtin_amdgcn_perm(wr[st * 8 + 2 * k + 1], wr[st * 8 + 2 * k], 0x07060302u);
        }
      lds_wait_ge(FL + FL_LN, 4u, 0);
      const LAS unsigned char* ap = L + L_LN + (lane & 7) * LN_STR + (((lane >> 3) & 1) * 32 + g * 8) * 2 + hw * 256;
      const v4u a0 = *(const LAS v4u*)ap;
      const v4u a1 = *(const LAS v4u*)(ap + (hw == 3 ? 0 : 128));
      v4f de = {0.f, 0.f, 0.f, 0.f}, dd = {0.f, 0.f, 0.f, 0.f};
      de = mfma(a0, be[0], de); dd = mfma(a0, bo[0], dd);
      if (hw != 3) { de = mfma(a1, be[1], de); dd = mfma(a1, bo[1], dd); }
      const bool val = ((lane >> 3) & 1) == (lane >> 5);
      const int qa = val ? L_QP + hw * 512 + (((lane >> 4) & 1) * 4) * 64 + (lane & 7) * 8 : L_SINK + lane * 8;
      LAS unsigned char* qp = L + qa + ((val && lane >= 32) ? (L_BF - L_QP) : 0);
#pragma unroll
      for (int r = 0; r < 4; ++r) *(LAS v2f*)(qp + (val ? r * 64 : 0)) = v2f{de[r], dd[r]};
      lds_signal(L, FL_QP, lane, 1u);
    }
    if (hw == 0) {
      lds_wait_ge(FL + FL_QP, 4u, 0);
      v4f a = {0.f, 0.f, 0.f, 0.f};
#pragma unroll
      for (int i = 0; i < 4; ++i) a += *(const LAS v4f*)(L + L_QP + i * 512 + c * 16) + *(const LAS v4f*)(L + L_BF + i * 512 + c * 16);
      ST128((v4u{pk2(a.x, a.y), pk2(a.z, a.w), tagl, tagh}), rws, lane < 32 ? WS_G + ((kvh * 8 + (c >> 2)) * 128 + (b * 4 + s) * 4 + (c & 3)) * 16 : WS_END, 0, AUX_SC1);
      PADPT(2);
      STAMP(2);
    }
    if (hw >= 2) {
      const int off = WS_G + (kvh * 8 + b) * 2048 + ((hw - 2) * 64 + lane) * 16;
      if (QWAIT) __builtin_amdgcn_s_sleep(QWAIT);
      v4u gq;
      bool ok = false;
#pragma nounroll
      for (unsigned sp = 0; sp < SPIN_CAP; ++sp) {
        CLOB();
        if (!ok) { gq = LD128(rws, off, 0, AUX_SC1); ok = ((gq.z ^ tagl) | (gq.w ^ tagh)) == 0u; }
        if (__all(ok)) break;
        if (QSLEEP) __builtin_amdgcn_s_sleep(QSLEEP);
      }
      *(LAS v2u*)(L + L_Q + ((hw - 2) * 64 + lane) * 8) = v2u{gq.x, gq.y};
      lds_signal(L, FL_Q, lane, 2u);
    }
    lds_wait_ge(FL + FL_Q, 4u, 1);
    STAMP(3);
    PADPT(3);
    if (HSTOP == 3) return;
    v4u Fr[28];
    v4f cx = {0.f, 0.f, 0.f, 0.f}, cb = {0.f, 0.f, 0.f, 0.f};
#pragma unroll
    for (int i = 0; i < 28; ++i) Fr[i] = v4u{0u, 0u, 0u, 0u};
    auto stage_w2 = [&]() {
      const __amdgpu_buffer_rsrc_t r2 = __builtin_amdgcn_make_buffer_rsrc((void*)(w2 + m * 64 * D), 0, NOSTAGE ? 0 : 64 * D * 2, RSRC3);
      v4u uu[14];
#pragma unroll
      for (int i = 0; i < 14; ++i) uu[i] = LD128(r2, lane * 16, (i * 4 + hw) * 1024, W_AUX);
      lds_wait_ge(FL + FL_RAW2, 4u, 1);
#pragma unroll
      for (int i = 0; i < 14; ++i) *(LAS v4u*)(L + L_RAW + (i * 4 + hw) * 1024 + lane * 16) = uu[i];
      lds_signal(L, FL_RAW3, lane, 1u);
      lds_wait_ge(FL + FL_RAW3, 4u, 0);
#pragma unroll
      for (int i = 0; i < 14; ++i) Fr[14 + i] = frag8(L + L_RAW + ((i & 1) * 32 + g * 8) * 896 + ((hw * 7 + (i >> 1)) * 16 + (lane & 15)) * 2, 896, 4 * 896);
    };
    if (isW) {
      const __amdgpu_buffer_rsrc_t ro = __builtin_amdgcn_make_buffer_rsrc((void*)wo, 0, NOSTAGE ? 0 : NQ * D * 2, RSRC3);
      v4u tt[4];
#pragma unroll
      for (int i = 0; i < 4; ++i) tt[i] = LD128(ro, (lane >> 3) * (D * 2) + (lane & 7) * 16, (kvh * 512 + kq * 128 + hw * 32 + i * 8) * (D * 2) + t * 128, W_AUX);
      const v2u xv = __builtin_amdgcn_raw_buffer_load_b64(rx, (kvh * D + r5 * 16 + (lane & 3) * 4) * 2, 0, X_AUX);
      const v2u bv = __builtin_nontemporal_load((const GAS v2u*)(b2 + r5 * 16 + (lane & 3) * 4));
#pragma unroll
      for (int i = 0; i < 4; ++i) *(LAS v4u*)(L + L_RAW + (hw * 32 + i * 8) * 128 + lane * 16) = tt[i];
      cx = v4f{bflo(xv.x), bfhi(xv.x), bflo(xv.y), bfhi(xv.y)};
      cb = v4f{bflo(bv.x), bfhi(bv.x), bflo(bv.y), bfhi(bv.y)};
      lds_signal(L, FL_RAW, lane, 1u);
      lds_wait_ge(FL + FL_RAW, 4u, 1);
#pragma unroll
      for (int ks = 0; ks < 4; ++ks) Fr[ks] = frag8(L + L_RAW + (ks * 32 + g * 4) * 128 + (hw * 16 + (lane & 15)) * 2, 128, 16 * 128);
    } else if (isM) {
      const __amdgpu_buffer_rsrc_t r1 = __builtin_amdgcn_make_buffer_rsrc((void*)w1, 0, NOSTAGE ? 0 : D * F * 2, RSRC3);
#pragma unroll 1
      for (int it = 0; it < 7; ++it) {
        v4u tt[2];
#pragma unroll
        for (int i = 0; i < 2; ++i) tt[i] = LD128(r1, (lane >> 3) * (F * 2) + (lane & 7) * 16, ((it * 4 + hw) * 16 + i * 8) * (F * 2) + m * 128, W_AUX);
#pragma unroll
        for (int i = 0; i < 2; ++i) *(LAS v4u*)(L + L_RAW + ((it * 4 + hw) * 16 + i * 8) * 128 + lane * 16) = tt[i];
      }
      const v2u bv = __builtin_nontemporal_load((const GAS v2u*)(b1 + m * 64 + hw * 16 + g * 4));
      cb = v4f{bflo(bv.x), bfhi(bv.x), bflo(bv.y), bfhi(bv.y)};
      lds_signal(L, FL_RAW, lane, 1u);
      lds_wait_ge(FL + FL_RAW, 4u, 1);
#pragma unroll
      for (int ks = 0; ks < 14; ++ks) Fr[ks] = frag8(L + L_RAW + (ks * 32 + g * 8) * 128 + (hw * 16 + (lane & 15)) * 2, 128, 4 * 128);
#pragma unroll
      for (int ks = 0; ks < 14; ++ks) asm volatile("" : "+v"(Fr[ks]));
      lds_signal(L, FL_RAW2, lane, 1u);
      if (!LATEW2) stage_w2();
    }
    STAMP(4);
    float fw[4] = {0.f, 0.f, 0.f, 0.f};
    if (hw < 2) {
      lds_wait_ge(FL + FL_ML, 4u, 1);
      PADPT(4);
      const v2f ml = *(const LAS v2f*)(L + L_ML + ((lane & 3) * 2 + hw) * 8);
      float Ms = ml.x;
      Ms = fmaxf(Ms, dpp<XOR1>(Ms)); Ms = fmaxf(Ms, dpp<XOR2>(Ms));
      const float Msub = Ms == -INFINITY ? 0.f : Ms;
      float Lw = __builtin_amdgcn_exp2f((ml.x - Msub) * SC2) * ml.y;
      Lw += dpp<XOR1>(Lw); Lw += dpp<XOR2>(Lw);
      ST128((v4u{__float_as_uint(Ms), __float_as_uint(Lw), tagl, tagh}), rws, lane == 0 ? WS_ML + ((grp * 2 + hw) * 4 + s) * 16 : WS_END, 0, AUX_SC1);
      const int off = WS_ML + ((grp * 2 + hw) * 4 + (lane & 3)) * 16;
      v4u gm;
#pragma nounroll
      for (unsigned sp = 0; sp < SPIN_CAP; ++sp) {
        __builtin_amdgcn_s_sleep(64);
        CLOB();
        gm = LD128(rws, off, 0, AUX_SC1);
        if (__all(((gm.z ^ tagl) | (gm.w ^ tagh)) == 0u)) break;
      }
      float M = __uint_as_float(gm.x);
      M = fmaxf(M, dpp<XOR1>(M)); M = fmaxf(M, dpp<XOR2>(M));
      float Ls = __builtin_amdgcn_exp2f((__uint_as_float(gm.x) - M) * SC2) * __uint_as_float(gm.y);
      Ls += dpp<XOR1>(Ls); Ls += dpp<XOR2>(Ls);
      const float f = __builtin_amdgcn_exp2f((ml.x - M) * SC2) / Ls;
#pragma unroll
      for (int i = 0; i < 4; ++i) fw[i] = __builtin_bit_cast(float, __builtin_amdgcn_readlane(__builtin_bit_cast(int, f), i));
    }
    STAMP(5);
    if (HSTOP == 5) { asm volatile("" :: "v"(Fr[0]), "v"(Fr[7]), "v"(Fr[13]), "v"(Fr[27]), "v"(fw[0])); return; }
    const int p_p0 = WS_P0 + (grp * 4 + s) * 2048 + hw * 1024 + lane * 16;
    const int p_pw = (lane & 8) == 0 ? WS_PW + (((lane & 7) * 28 + t * 4 + hw) * 32 + kvh * 4 + kq) * 64 + g * 16 : WS_END;
    const int p_h = lane < 4 ? WS_H + (kvh * 112 + r5 * 4 + lane) * 16 : WS_END;
    const int p_pf = WS_PF + (((lane & 7) * 28 + hw * 7 + ((lane >> 3) & 1)) * 32 + m) * 64 + g * 16;
    const int p_pf3 = (lane & 8) ? WS_END : p_pf + 6 * 2048;
    const int g_p0 = WS_P0 + (((lane & 7) * 8 + kvh) * 4 + ((lane >> 3) & 1) * 2) * 2048 + (kq * 128 + hw * 32 + g * 4) * 4;
    const int g_pw = WS_PW + (kvh * 28 + r5) * 2048 + lane * 16;
    const int g_h = WS_H + (row * 112 + (c < 28 ? c : 27)) * 16;
    const int a_ln = c < 28 ? L_LN + row * LN_STR + c * 8 : L_SINK + lane * 8;
    const int g_o = lane < 4 ? (kvh * D + r5 * 16 + lane * 4) * 2 : 8 * D * 2;
    const int g_e = (w == 0 && lane == 0) ? WS_EPOCH : WS_END;
    const __amdgpu_buffer_rsrc_t rout = __builtin_amdgcn_make_buffer_rsrc((void*)out, 0, 8 * D * 2, RSRC3);
#pragma unroll 1
    for (int pass = DRYRUN ? 0 : 1; pass < (HSTOP == 6 ? 1 : 2); ++pass) {
      int real = __builtin_amdgcn_readfirstlane(pass);
      asm volatile("" : "+s"(real));
      const int oob = real ? 0 : WS_END;
      const unsigned tl = real ? tagl : 0u, th = real ? tagh : 0u;
      if (real) { lds_wait_ge(FL + FL_PRE, 4u, 1); STAMP(6); }
      lds_wait_ge(FL + FL_DONE, real ? 4u : 0u, 0);
      if (hw < 2) {
        STAMPR(7);
        PADPT(7);
        const LAS v4f* mp = (const LAS v4f*)(L + L_MRG + hw * 1024 + lane * 16);
        const v4f o = (mp[0] * fw[0] + mp[128] * fw[1]) + (mp[256] * fw[2] + mp[384] * fw[3]);
        ST128(pack24(o, tl), rws, p_p0, oob, AUX_SC1);
        STAMPR(8);
      }
      if (isW) {
        v4u pv[4];
        bool ok = false;
#pragma nounroll
        for (unsigned sp = 0; sp < SPIN_CAP; ++sp) {
          CLOB();
          if (!ok) {
#pragma unroll
            for (int i = 0; i < 4; ++i) pv[i] = LD128(rws, g_p0 + (i & 1) * 64 + (i >> 1) * 2048, oob, AUX_SC1);
            ok = (((pv[0].w ^ tl) | (pv[1].w ^ tl)) | ((pv[2].w ^ tl) | (pv[3].w ^ tl))) == 0u;
          }
          if (__all(ok)) break;
          if (PSLEEP) __builtin_amdgcn_s_sleep(PSLEEP);
        }
        STAMPR(9);
        v4f lo = unpack24(pv[0]) + unpack24(pv[2]), hi = unpack24(pv[1]) + unpack24(pv[3]);
#pragma unroll
        for (int k = 0; k < 4; ++k) { lo[k] += dpp<ROR8>(lo[k]); hi[k] += dpp<ROR8>(hi[k]); }
        *(LAS v4u*)(L + L_BF + hw * 1024 + lane * 16) = v4u{pk2(lo.x, lo.y), pk2(lo.z, lo.w), pk2(hi.x, hi.y), pk2(hi.z, hi.w)};
        if (real) BAR();
        v4u bf[4];
#pragma unroll
        for (int ks = 0; ks < 4; ++ks) bf[ks] = *(const LAS v4u*)(L + L_BF + ks * 1024 + lane * 16);
        SB();
        v4f d0 = {0.f, 0.f, 0.f, 0.f}, d1 = {0.f, 0.f, 0.f, 0.f};
        d0 = mfma(Fr[0], bf[0], d0); d1 = mfma(Fr[1], bf[1], d1); d0 = mfma(Fr[2], bf[2], d0); d1 = mfma(Fr[3], bf[3], d1);
        ST128(pack24(d0 + d1, tl), rws, p_pw, oob, AUX_SC1);
        STAMPR(10);
        if (hw == 0) {
          v4u u0, u1;
          ok = false;
#pragma nounroll
          for (unsigned sp = 0; sp < SPIN_CAP; ++sp) {
            CLOB();
            if (!ok) { u0 = LD128(rws, g_pw, oob, AUX_SC1); u1 = LD128(rws, g_pw + 1024, oob, AUX_SC1); ok = ((u0.w ^ tl) | (u1.w ^ tl)) == 0u; }
            if (__all(ok)) break;
            if (PSLEEP) __builtin_amdgcn_s_sleep(PSLEEP);
          }
          STAMPR(11);
          PADPT(11);
          v4f hv = red16(unpack24(u0) + unpack24(u1));
          const unsigned h01 = pk2(cx.x + rbf(hv.x), cx.y + rbf(hv.y)), h23 = pk2(cx.z + rbf(hv.z), cx.w + rbf(hv.w));
          ST128((v4u{h01, h23, tl, th}), rws, p_h, oob, AUX_SC1);
          STAMPR(12);
          if (real) __builtin_amdgcn_s_sleep(FWAIT);
          const bool has1 = lane < 48;
          ok = false;
#pragma nounroll
          for (unsigned sp = 0; sp < SPIN_CAP; ++sp) {
            CLOB();
            if (!ok) {
              u0 = LD128(rws, g_pw + (WS_PF - WS_PW), oob, AUX_SC1); u1 = LD128(rws, has1 ? g_pw + (WS_PF - WS_PW) + 1024 : WS_END, oob, AUX_SC1);
              ok = ((u0.w ^ tl) | (has1 ? u1.w ^ tl : 0u)) == 0u;
            }
            if (__all(ok)) break;
          }
          STAMPR(13);
          const v4f ov = red16(unpack24(u0) + unpack24(u1));
          const unsigned o01 = pk2(bflo(h01) + rbf(rbf(ov.x) + cb.x), bfhi(h01) + rbf(rbf(ov.y) + cb.y));
          const unsigned o23 = pk2(bflo(h23) + rbf(rbf(ov.z) + cb.z), bfhi(h23) + rbf(rbf(ov.w) + cb.w));
          __builtin_amdgcn_raw_buffer_store_b64(v2u{o01, o23}, rout, g_o, oob, 0);
          __builtin_amdgcn_raw_buffer_store_b64(v2u{tagl, tagh}, rws, g_e, oob, AUX_SC1);
          STAMPR(14);
          if (PADAT >= 0 && real && (PADW)) { while (__builtin_amdgcn_s_memrealtime() < padt + 8000) __builtin_amdgcn_s_sleep(1); }
        }
      } else if (isM) {
        if (LATEW2 && real) stage_w2();
        v4u hg[4];
        bool ok = false;
#pragma nounroll
        for (unsigned sp = 0; sp < SPIN_CAP; ++sp) {
          CLOB();
          if (!ok) {
#pragma unroll
            for (int i = 0; i < 4; ++i) hg[i] = LD128(rws, g_h + i * 448, oob, AUX_SC1);
            unsigned bad = 0u;
#pragma unroll
            for (int i = 0; i < 4; ++i) bad |= (hg[i].z ^ tl) | (hg[i].w ^ th);
            ok = bad == 0u;
          }
          if (__all(ok)) break;
          if (PSLEEP) __builtin_amdgcn_s_sleep(PSLEEP);
        }
        STAMPR(15);
        const bool act = c < 28;
        v4u o0, o1;
        ln_rows(v4u{act ? hg[0].x : 0u, act ? hg[0].y : 0u, act ? hg[1].x : 0u, act ? hg[1].y : 0u},
                v4u{act ? hg[2].x : 0u, act ? hg[2].y : 0u, act ? hg[3].x : 0u, act ? hg[3].y : 0u}, act, o0, o1);
        *(LAS v2u*)(L + a_ln) = v2u{o0.x, o0.y}; *(LAS v2u*)(L + a_ln + 224) = v2u{o0.z, o0.w};
        *(LAS v2u*)(L + a_ln + 448) = v2u{o1.x, o1.y}; *(LAS v2u*)(L + a_ln + 672) = v2u{o1.z, o1.w};
        if (real) BAR();
        const LAS unsigned char* bp = L + L_LN + (lane & 7) * LN_STR + g * 16;
        v4u bf[14];
#pragma unroll
        for (int ks = 0; ks < 14; ++ks) bf[ks] = *(const LAS v4u*)(bp + ks * 64);
        SB();
        v4f d0 = {0.f, 0.f, 0.f, 0.f}, d1 = {0.f, 0.f, 0.f, 0.f};
#pragma unroll
        for (int ks = 0; ks < 14; ks += 2) { d0 = mfma(Fr[ks], bf[ks], d0); d1 = mfma(Fr[ks + 1], bf[ks + 1], d1); }
        d0 += d1;
        float u[4];
#pragma unroll
        for (int k = 0; k < 4; ++k) { float a = rbf(rbf(d0[k]) + cb[k]); a = a > 0.f ? a : 0.f; u[k] = a * a; }
        *(LAS v2u*)(L + L_BF + (lane & 15) * 144 + (hw * 16 + g * 4) * 2) = v2u{pk2(u[0], u[1]), pk2(u[2], u[3])};
        if (real) BAR();
        const v4u h0 = *(const LAS v4u*)(L + L_BF + (lane & 15) * 144 + g * 16), h1 = *(const LAS v4u*)(L + L_BF + (lane & 15) * 144 + 64 + g * 16);
        v4f e[8];
#pragma unroll
        for (int i = 0; i < 7; ++i) e[i] = mfma(Fr[14 + 2 * i], h0, v4f{0.f, 0.f, 0.f, 0.f});
#pragma unroll
        for (int i = 0; i < 7; ++i) e[i] = mfma(Fr[14 + 2 * i + 1], h1, e[i]);
        e[7] = e[6];
        const bool up = lane & 8;
        v4u ev[4];
#pragma unroll
        for (int i = 0; i < 4; ++i) {
          const v4f ea = e[2 * i], eb = e[2 * i + 1];
          ev[i] = pack24(v4f{up ? eb.x : ea.x, up ? eb.y : ea.y, up ? eb.z : ea.z, up ? eb.w : ea.w}, tl);
        }
#pragma unroll
        for (int i = 0; i < 3; ++i) ST128(ev[i], rws, p_pf + i * 4096, oob, AUX_SC1);
        ST128(ev[3], rws, p_pf3, oob, AUX_SC1);
        STAMPR(16);
      }
    }
  } else {
    const size_t kvoff = ((size_t)grp * SK + (size_t)s * 1024) * DH;
    const __amdgpu_buffer_rsrc_t rk = __builtin_amdgcn_make_buffer_rsrc((void*)(kc + kvoff), 0, 524288, RSRC3);
    const int voff = lane * 16 + wv * 4096;
    v4u X[NEXTRA * 4][4], R[4][4];
    const unsigned tb = (unsigned)__builtin_amdgcn_s_memrealtime() + (unsigned)(T1);
#define LANDED() asm volatile("s_waitcnt vmcnt(%0)" :: "i"(4 * (LEAD - 1)) : "memory")
    STAMP(0);
    if (PURE != 3) BAR();
#pragma unroll
    for (int n = 0; n < NEXTRA * 4 + LEAD; ++n) {
      if (n >= LEAD) LANDED();
      pace(tb, n);
      issue_turn(n < NEXTRA * 4 ? X[n] : R[n - NEXTRA * 4], rk, voff, n * 16384);
    }
    const bool b0 = lane & 1, b1_ = lane & 2, b2_ = lane & 4, b3 = lane & 8;
    const int hf = lane >> 5, c = lane & 31;
    LAS float* P = (LAS float*)(L + L_P) + wv * 512;
    LAS float* Pl = P + ((lane >> 3) & 3) * 16 + hf * 8 + (lane & 7);
    STAMP(1);
    if (PURE != 3) lds_wait_ge(FL + FL_Q, 4u, 0);
    STAMP(2);
    const v4u q0 = *(const LAS v4u*)(L + L_Q + c * 16), q1 = *(const LAS v4u*)(L + L_Q + 512 + c * 16);
    float mx = -INFINITY;
#pragma unroll
    for (int g = 0; g < NEXTRA; ++g) {
      float r[4], rx[4];
#pragma unroll
      for (int d = 0; d < 4; ++d) {
        rx[d] = k_turn(X[4 * g + d], q0, q1, b0, b1_, b2_);
        SB();
        LANDED();
        pace(tb, (NEXTRA + g) * 4 + d + LEAD);
        issue_turn(R[(d + LEAD) & 3], rk, voff, ((NEXTRA + g) * 4 + d + LEAD) * 16384);
        r[d] = k_turn(R[d], q0, q1, b0, b1_, b2_);
        SB();
      }
      const float sx = k_group(rx[0], rx[1], rx[2], rx[3], b3), sc = k_group(r[0], r[1], r[2], r[3], b3);
      mx = fmaxf(mx, fmaxf(sx, sc));
      Pl[g * 64] = sx; Pl[(NEXTRA + g) * 64] = sc;
      SB();
    }
    STAMP(3);
    const __amdgpu_buffer_rsrc_t rv = __builtin_amdgcn_make_buffer_rsrc((void*)(vc + kvoff), 0, 524288, RSRC3);
#pragma unroll 1
    for (int g = 2 * NEXTRA; g < 8; ++g) {
      float r[4];
#pragma unroll
      for (int d = 0; d < 4; ++d) {
        const int nx = g * 4 + d + LEAD;
        const bool tov = (d + LEAD >= 4) && g == 7;
        const __amdgpu_buffer_rsrc_t rn = __builtin_amdgcn_make_buffer_rsrc((void*)((tov ? vc : kc) + kvoff), 0, 524288, RSRC3);
        LANDED();
        pace(tb, nx);
        issue_turn(R[(d + LEAD) & 3], rn, voff, (nx & 31) * 16384);
        r[d] = k_turn(R[d], q0, q1, b0, b1_, b2_);
        SB();
      }
      const float sc = k_group(r[0], r[1], r[2], r[3], b3);
      mx = fmaxf(mx, sc);
      Pl[g * 64] = sc;
    }
    STAMP(4);
    mx = fmaxf(mx, dpp<XOR2>(mx)); mx = fmaxf(mx, dpp<ROR4>(mx)); mx = fmaxf(mx, dpp<ROR8>(mx));
    mx = sw16_max(mx); mx = sw32_max(mx);
    const float msub = mx == -INFINITY ? 0.f : mx;
    float ls = 0.f;
    {
      float sv[8];
#pragma unroll
      for (int g = 0; g < 8; ++g) sv[g] = Pl[g * 64];
#pragma unroll
      for (int g = 0; g < 8; ++g) { const float p = __builtin_amdgcn_exp2f((sv[g] - msub) * SC2); ls += p; Pl[g * 64] = p; }
    }
    ls += dpp<XOR2>(ls); ls += dpp<ROR8>(ls); ls += dpp<ROR4>(ls);
    ls = sw16_add(ls, ls); ls = sw32_add(ls, ls);
    *(LAS v2f*)(L + (lane < 2 ? L_ML + (wv * 2 + lane) * 8 : L_SINK + 1024 + lane * 8)) = v2f{mx, ls};
    lds_signal(L, FL_ML, lane, 1u);
    v2f a0[4], a1[4];
#pragma unroll
    for (int k = 0; k < 4; ++k) { a0[k] = v2f{0.f, 0.f}; a1[k] = v2f{0.f, 0.f}; }
    const LAS v4f* Pr = (const LAS v4f*)(P + hf * 8);
    LAS v4f* mo = (LAS v4f*)(L + L_MRG + wv * 2048 + lane * 32);
#pragma unroll 1
    for (int g = 0; g < 8; ++g) {
      v4f pa[4], pb[4];
#pragma unroll
      for (int d = 0; d < 4; ++d) { pa[d] = Pr[(g * 4 + d) * 4]; pb[d] = Pr[(g * 4 + d) * 4 + 1]; }
#pragma unroll
      for (int d = 0; d < 4; ++d) {
        LANDED();
        pace(tb, 32 + g * 4 + d + LEAD);
        issue_turn<V_AUX>(R[(d + LEAD) & 3], rv, voff, (g * 4 + d + LEAD) * 16384);
        v_turn(R[d], pa[d], pb[d], a0, a1);
        SB();
        if (d == 1) lds_signal(L, FL_PRE, lane, g == 7 ? 1u : 0u);
        if (d == 3) {
          mo[0] = v4f{sw32_add(a0[0].x, a1[0].x), sw32_add(a0[0].y, a1[0].y), sw32_add(a0[1].x, a1[1].x), sw32_add(a0[1].y, a1[1].y)};
          mo[1] = v4f{sw32_add(a0[2].x, a1[2].x), sw32_add(a0[2].y, a1[2].y), sw32_add(a0[3].x, a1[3].x), sw32_add(a0[3].y, a1[3].y)};
          lds_signal(L, FL_DONE, lane, g == 7 ? 1u : 0u);
          SB();
        }
      }
    }
    STAMP(5);
  }
}
}

extern "C" void kernel_launch(void* const* d_in, const int* in_sizes, int n_in, void* d_out, int out_size, void* d_ws,
                              size_t ws_size, hipStream_t stream) {
  using namespace fk;
  (void)hipFuncSetAttribute((const void*)fused_decode, hipFuncAttributeMaxDynamicSharedMemorySize, L_END + DBG_STAMPS * 2048);
  hipLaunchKernelGGL(fused_decode, dim3(256), dim3(512), L_END + DBG_STAMPS * 2048, stream, (const u16*)d_in[0], (const u16*)d_in[1],
                     (const u16*)d_in[2], (const u16*)d_in[3], (const u16*)d_in[4], (const u16*)d_in[6], (const u16*)d_in[7],
                     (const u16*)d_in[8], (const u16*)d_in[9], (u16*)d_out, (unsigned char*)d_ws);
}
```

```cpp
#define WDELAY 20
#define RDELAY 12
#define LATEW2 1
#define ILV 1
#define TAU0 1100
#define TAU 912
#include <hip/hip_runtime.h>
#include <hip/hip_bf16.h>
#include <stdint.h>

#define DBG_STAMPS 0
#define NEXTRA 2
#define LEAD 2
#define T1 0
#define TJ 0
#define PN0 16
#define PN1 64
#define LATEALL 0
#define PRED 1
#define STAG 0
#define QT 350
#define QSLEEP 8
#define CODEPF 1
#define CODE_BYTES 25344
#define PRETOUCH 1
#define MDELAY 0
#define FWAIT 16
#define PREG 6

namespace fk {
typedef unsigned short u16;
typedef unsigned long long u64;
typedef unsigned v4u __attribute__((ext_vector_type(4)));
typedef unsigned v2u __attribute__((ext_vector_type(2)));
typedef float v4f __attribute__((ext_vector_type(4)));
typedef float v2f __attribute__((ext_vector_type(2)));
typedef __bf16 bf16x2 __attribute__((ext_vector_type(2)));
typedef __bf16 bf16x8 __attribute__((ext_vector_type(8)));
typedef short s16x4 __attribute__((ext_vector_type(4)));
#define LAS __attribute__((address_space(3)))
#define GAS __attribute__((address_space(1)))

constexpr int D = 448, F = 1792, SK = 4096, DH = 256, NQ = 4096;
constexpr int AUX_NT = 2, AUX_SC1 = 16;
constexpr int RSRC3 = 0x00020000;
constexpr float SC2 = 0.0625f * 1.4426950408889634f;
constexpr unsigned SPIN_CAP = 1u << 16;

constexpr int WS_EPOCH = 0;
constexpr int WS_G = 4096;
constexpr int WS_ML = WS_G + 8 * 8 * 128 * 16;
constexpr int WS_H = WS_ML + 64 * 8 * 16;
constexpr int WS_P0 = WS_H + 16384;
constexpr int WS_PW = WS_P0 + 64 * 4 * 2048;
constexpr int WS_PF = WS_PW + 32 * 28 * 512;
constexpr int WS_END = WS_PF + 28 * 28 * 512;
constexpr int WS_DBG = WS_END;

constexpr int LN_STR = 912;
constexpr int L_P = 0;
constexpr int L_MRG = 8192;
constexpr int L_Q = 16384;
constexpr int L_FLAG = 17408;
constexpr int L_ML = L_FLAG + 64;
constexpr int L_LN = L_ML + 64;
constexpr int L_BF = L_LN + 8 * LN_STR;
constexpr int L_SINK = L_BF + 4096;
constexpr int L_RAW = L_SINK + 2048;
constexpr int L_END = 96 * 1024;
static_assert(L_RAW + 57344 <= L_END, "lds");
static_assert(L_LN % 16 == 0 && L_BF % 16 == 0 && L_RAW % 16 == 0, "align");
enum { FL_LN = 0, FL_QP = 1, FL_Q = 2, FL_PRE = 3, FL_DONE = 4, FL_RAW = 5, FL_RAW2 = 6, FL_RAW3 = 7, FL_ML = 9, FL_TAGL = 10, FL_TAGH = 11 };

__device__ __forceinline__ float bflo(unsigned v) { return __builtin_bit_cast(float, v << 16); }
__device__ __forceinline__ float bfhi(unsigned v) { return __builtin_bit_cast(float, v & 0xffff0000u); }
__device__ __forceinline__ float rbf(float f) { return (float)(__bf16)f; }
__device__ __forceinline__ unsigned pk2(float lo, float hi) {
  bf16x2 v = {(__bf16)lo, (__bf16)hi};
  return __builtin_bit_cast(unsigned, v);
}
__device__ __forceinline__ unsigned pk2o(float lo, float hi) {
  unsigned p = pk2(lo, hi);
  asm("" : "+v"(p));
  return p;
}
__device__ __forceinline__ float dot2(unsigned a, unsigned b, float c) {
  return __builtin_amdgcn_fdot2_f32_bf16(__builtin_bit_cast(bf16x2, a), __builtin_bit_cast(bf16x2, b), c, false);
}
__device__ __forceinline__ v4f mfma(v4u a, v4u b, v4f c) {
  return __builtin_amdgcn_mfma_f32_16x16x32_bf16(__builtin_bit_cast(bf16x8, a), __builtin_bit_cast(bf16x8, b), c, 0, 0, 0);
}
template <int CTRL> __device__ __forceinline__ float dpp(float x) {
  return __builtin_bit_cast(float, __builtin_amdgcn_mov_dpp(__builtin_bit_cast(int, x), CTRL, 0xf, 0xf, true));
}
constexpr int XOR1 = 0xB1, XOR2 = 0x4E, HMIR = 0x141, ROR4 = 0x124, ROR8 = 0x128;
__device__ __forceinline__ float sw16_add(float a, float b) {
  auto s = __builtin_amdgcn_permlane16_swap(__float_as_uint(a), __float_as_uint(b), false, false);
  return __uint_as_float(s[0]) + __uint_as_float(s[1]);
}
__device__ __forceinline__ float sw32_add(float a, float b) {
  auto s = __builtin_amdgcn_permlane32_swap(__float_as_uint(a), __float_as_uint(b), false, false);
  return __uint_as_float(s[0]) + __uint_as_float(s[1]);
}
__device__ __forceinline__ float sw16_max(float a) {
  auto s = __builtin_amdgcn_permlane16_swap(__float_as_uint(a), __float_as_uint(a), false, false);
  return fmaxf(__uint_as_float(s[0]), __uint_as_float(s[1]));
}
__device__ __forceinline__ float sw32_max(float a) {
  auto s = __builtin_amdgcn_permlane32_swap(__float_as_uint(a), __float_as_uint(a), false, false);
  return fmaxf(__uint_as_float(s[0]), __uint_as_float(s[1]));
}
__device__ __forceinline__ v4u pack24(v4f v, unsigned tag) {
  const unsigned a = __float_as_uint(v.x), b = __float_as_uint(v.y), c = __float_as_uint(v.z), d = __float_as_uint(v.w);
  return v4u{__builtin_amdgcn_perm(b, a, 0x05030201u), __builtin_amdgcn_perm(c, b, 0x06050302u), __builtin_amdgcn_perm(d, c, 0x07060503u), tag};
}
__device__ __forceinline__ v4f unpack24(v4u g) {
  return v4f{__uint_as_float(g.x << 8), __uint_as_float(__builtin_amdgcn_perm(g.y, g.x, 0x0504030Cu)),
             __uint_as_float(__builtin_amdgcn_perm(g.z, g.y, 0x0403020Cu)), __uint_as_float(g.z & 0xFFFFFF00u)};
}
#define LGKM0() asm volatile("s_waitcnt lgkmcnt(0)" ::: "memory")
#define CLOB() asm volatile("" ::: "memory")
#define SB() __builtin_amdgcn_sched_barrier(0)
#define BAR() do { asm volatile("s_waitcnt lgkmcnt(0)" ::: "memory"); __builtin_amdgcn_s_barrier(); asm volatile("" ::: "memory"); __builtin_amdgcn_sched_barrier(0); } while (0)
#define LD128(rs, vo, so, aux) __builtin_amdgcn_raw_buffer_load_b128(rs, vo, so, aux)
#define ST128(v, rs, vo, so, aux) __builtin_amdgcn_raw_buffer_store_b128(v, rs, vo, so, aux)

#define STAMP(i) do {} while (0)

__device__ __forceinline__ void lds_wait_ge(volatile LAS unsigned* f, unsigned want, int slp) {
#pragma nounroll
  for (unsigned sp = 0; sp < (SPIN_CAP << 4); ++sp) {
    if (*f >= want) break;
    if (slp) __builtin_amdgcn_s_sleep(8);
  }
  CLOB();
}
__device__ __forceinline__ void lds_signal(LAS unsigned char* L, int word, int lane, unsigned inc) {
  LGKM0();
  LAS unsigned* p = (LAS unsigned*)(L + (lane == 0 ? L_FLAG + word * 4 : L_SINK + lane * 4));
  __hip_atomic_fetch_add(p, inc, __ATOMIC_RELAXED, __HIP_MEMORY_SCOPE_WORKGROUP);
}
template <int N, int STR, bool WIDE>
__device__ __forceinline__ void gather(v4u (&v)[N], __amdgpu_buffer_rsrc_t rs, int off, unsigned tl, unsigned th, unsigned skip_last) {
#pragma unroll
  for (int i = 0; i < N; ++i) v[i] = LD128(rs, off + i * STR, 0, AUX_SC1);
#pragma nounroll
  for (unsigned sp = 0; sp < SPIN_CAP; ++sp) {
    unsigned bad[N], any = 0u;
#pragma unroll
    for (int i = 0; i < N; ++i) {
      bad[i] = WIDE ? ((v[i].z ^ tl) | (v[i].w ^ th)) : (v[i].w ^ tl);
      if (i == N - 1) bad[i] &= skip_last;
      any |= bad[i];
    }
    if (__builtin_amdgcn_ballot_w64(any != 0u) == 0ull) break;
    CLOB();
#pragma unroll
    for (int i = 0; i < N; ++i) if (bad[i] != 0u) v[i] = LD128(rs, off + i * STR, 0, AUX_SC1);
  }
}
__device__ __forceinline__ void pace(unsigned tb, int n) {
  const unsigned until = tb + (unsigned)((n >= PN1 ? -65536 : n < PN0 ? n * TAU0 : PN0 * TAU0 + TJ * 16 + (n - PN0) * TAU) >> 4);
  asm volatile("8:\n\ts_memrealtime vcc\n\ts_waitcnt lgkmcnt(0)\n\ts_sub_i32 vcc_lo, vcc_lo, %0\n\ts_cmp_lt_i32 vcc_lo, 0\n\ts_cbranch_scc1 8b\n" :: "s"(until) : "scc", "vcc", "memory");
}
__device__ __forceinline__ void issue_turn(v4u* r, __amdgpu_buffer_rsrc_t rs, int voff, int soff) {
#pragma unroll
  for (int i = 0; i < 4; ++i) r[i] = LD128(rs, voff + i * 1024, soff, AUX_NT);
  SB();
}
__device__ __forceinline__ float k_turn(const v4u* k, v4u q0, v4u q1, bool b0, bool b1, bool b2) {
  float x[8];
#pragma unroll
  for (int i = 0; i < 4; ++i) {
    float a0 = dot2(k[i].x, q0.x, 0.f), a1 = dot2(k[i].x, q1.x, 0.f);
    a0 = dot2(k[i].y, q0.y, a0); a1 = dot2(k[i].y, q1.y, a1);
    a0 = dot2(k[i].z, q0.z, a0); a1 = dot2(k[i].z, q1.z, a1);
    a0 = dot2(k[i].w, q0.w, a0); a1 = dot2(k[i].w, q1.w, a1);
    x[2 * i] = a0; x[2 * i + 1] = a1;
  }
  float y[4], z[2];
#pragma unroll
  for (int v = 0; v < 4; ++v) y[v] = (b2 ? x[v + 4] : x[v]) + dpp<HMIR>(b2 ? x[v] : x[v + 4]);
#pragma unroll
  for (int v = 0; v < 2; ++v) z[v] = (b1 ? y[v + 2] : y[v]) + dpp<XOR2>(b1 ? y[v] : y[v + 2]);
  return (b0 ? z[1] : z[0]) + dpp<XOR1>(b0 ? z[0] : z[1]);
}
__device__ __forceinline__ float k_group(float r0, float r1, float r2, float r3, bool b3) {
  const float u0 = (b3 ? r1 : r0) + dpp<ROR8>(b3 ? r0 : r1);
  const float u1 = (b3 ? r3 : r2) + dpp<ROR8>(b3 ? r2 : r3);
  return rbf(sw16_add(u0, u1));
}
__device__ __forceinline__ void v_turn(const v4u* v, v4f pa, v4f pb, v2f (&a0)[4], v2f (&a1)[4]) {
#pragma unroll
  for (int i = 0; i < 4; ++i) {
    const float p0 = i == 0 ? pa.x : i == 1 ? pa.z : i == 2 ? pb.x : pb.z;
    const float p1 = i == 0 ? pa.y : i == 1 ? pa.w : i == 2 ? pb.y : pb.w;
#pragma unroll
    for (int j = 0; j < 4; ++j) {
      const v2f e = {bflo(v[i][j]), bfhi(v[i][j])};
      a0[j] = __builtin_elementwise_fma(e, v2f{p0, p0}, a0[j]);
      a1[j] = __builtin_elementwise_fma(e, v2f{p1, p1}, a1[j]);
    }
  }
}
template <bool FULL> __device__ __forceinline__ float row_sum(float v) {
  v += dpp<XOR1>(v); v += dpp<XOR2>(v); v += dpp<HMIR>(v); v += dpp<ROR8>(v);
  v = sw16_add(v, v);
  return FULL ? sw32_add(v, v) : v;
}
template <int NW> __device__ __forceinline__ void ln_row(unsigned (&x)[NW], bool act) {
  constexpr unsigned ONES = 0x3F803F80u;
  float sa = 0.f, sb = 0.f;
#pragma unroll
  for (int k = 0; k < NW; k += 2) { sa = dot2(x[k], ONES, sa); sb = dot2(x[k + 1], ONES, sb); }
  const float mean = row_sum<NW == 4>(sa + sb) * (1.0f / D);
  v2f v[NW];
#pragma unroll
  for (int k = 0; k < NW; ++k) v[k] = v2f{bflo(x[k]), bfhi(x[k])};
  v2f q0 = {0.f, 0.f}, q1 = {0.f, 0.f};
#pragma unroll
  for (int k = 0; k < NW; k += 2) {
    const v2f d0 = v[k] - mean, d1 = v[k + 1] - mean;
    q0 = __builtin_elementwise_fma(d0, d0, q0); q1 = __builtin_elementwise_fma(d1, d1, q1);
  }
  q0 += q1;
  const float sq = row_sum<NW == 4>(act ? q0.x + q0.y : 0.f);
  const float mb = rbf(mean);
#pragma unroll
  for (int k = 0; k < NW; ++k) { const v2f d = v[k] - mb; const unsigned p = pk2o(d.x, d.y); v[k] = v2f{bflo(p), bfhi(p)}; }
  const float den = rbf(__builtin_amdgcn_sqrtf(rbf(rbf(sq * (1.0f / D)) + 1e-5f)));
  const float ri = __builtin_amdgcn_rcpf(den);
#pragma unroll
  for (int k = 0; k < NW; ++k) { const v2f a = v[k] * ri; x[k] = pk2(a.x, a.y); }
}
__device__ __forceinline__ v4u frag_tr(const LAS unsigned char* p, int hi) {
#if defined(__HIP_DEVICE_COMPILE__)
  const v2u a = __builtin_bit_cast(v2u, __builtin_amdgcn_ds_read_tr16_b64_v4i16((LAS s16x4*)p));
  const v2u b = __builtin_bit_cast(v2u, __builtin_amdgcn_ds_read_tr16_b64_v4i16((LAS s16x4*)(p + hi)));
  return v4u{a.x, a.y, b.x, b.y};
#else
  return v4u{0u, 0u, 0u, 0u};
#endif
}
__device__ __forceinline__ v4f red16(v4f t) {
#pragma unroll
  for (int k = 0; k < 4; ++k) { float a = t[k]; a += dpp<ROR8>(a); a += dpp<ROR4>(a); a = sw16_add(a, a); a = sw32_add(a, a); t[k] = a; }
  return t;
}
__device__ __forceinline__ void mlp_ln(LAS unsigned char* L, __amdgpu_buffer_rsrc_t rws, int row, int lane, unsigned tl, unsigned th) {
  const bool act = lane < 56;
  const int a = act ? L_LN + row * LN_STR + lane * 8 : L_SINK + lane * 8;
  v4u hg[2];
  if (MDELAY) __builtin_amdgcn_s_sleep(MDELAY);
  gather<2, 896, true>(hg, rws, WS_H + (row * 112 + (act ? lane : 55)) * 16, tl, th, ~0u);
  unsigned x[4] = {act ? hg[0].x : 0u, act ? hg[0].y : 0u, act ? hg[1].x : 0u, act ? hg[1].y : 0u};
  ln_row<4>(x, act);
  *(LAS v2u*)(L + a) = v2u{x[0], x[1]}; *(LAS v2u*)(L + a + 448) = v2u{x[2], x[3]};
  BAR();
}

__global__ void __launch_bounds__(512, 2)
fused_decode(const u16* __restrict__ x, const u16* __restrict__ kc, const u16* __restrict__ vc, const u16* __restrict__ wq,
             const u16* __restrict__ wo, const u16* __restrict__ w1, const u16* __restrict__ b1, const u16* __restrict__ w2,
             const u16* __restrict__ b2, u16* __restrict__ out, unsigned char* __restrict__ ws) {
  extern __shared__ __attribute__((aligned(16))) unsigned char smem[];
  LAS unsigned char* L = (LAS unsigned char*)smem;
  const u64 pc0 = __builtin_amdgcn_s_getpc() & ~0xFFull;
  const int w = blockIdx.x, tid = threadIdx.x, lane = tid & 63, wv = __builtin_amdgcn_readfirstlane(tid >> 6);
  const int kvh = w & 7, r5 = w >> 3, b = r5 & 7, s = r5 >> 3, grp = b * 8 + kvh;
  const int m = (r5 - 28) * 8 + kvh;
  const bool isM = r5 >= 28 && m < 28;
  volatile LAS unsigned* FL = (volatile LAS unsigned*)(L + L_FLAG);
  const __amdgpu_buffer_rsrc_t rws = __builtin_amdgcn_make_buffer_rsrc((void*)ws, 0, WS_END, RSRC3);
  auto code_prefetch = [&](int part) {
    const __amdgpu_buffer_rsrc_t rc = __builtin_amdgcn_make_buffer_rsrc((void*)pc0, 0, CODE_BYTES, RSRC3);
    return __builtin_amdgcn_raw_buffer_load_b32(rc, lane * 128, part * 8192, 0);
  };
  unsigned cpf = 0;
  if (CODEPF && (r5 >= 28 || r5 < 8)) cpf = code_prefetch(wv & 3);
  SB();
  asm volatile("" :: "s"(x), "s"(kc), "s"(vc), "s"(wq), "s"(wo), "s"(w1), "s"(b1), "s"(w2), "s"(b2), "s"(out), "s"(ws));

  if (__builtin_expect(wv >= 4, 1)) {
    const int hw = wv - 4;
    STAMP(0);
    __builtin_amdgcn_s_setprio(3);
    const unsigned tstart = (unsigned)__builtin_amdgcn_s_memrealtime();
    const bool isW = r5 < 28;
    const bool isQ = r5 < 8;
    const int t = r5 >> 2, kq = r5 & 3;
    const int g = lane >> 4;
    const LAS unsigned char* ap = L + L_RAW + (g * 8 + ((lane & 15) >> 2)) * 128 + hw * 32 + (lane & 3) * 8;
    const LAS unsigned char* bp = L + L_LN + (lane & 7) * LN_STR + g * 16;
    const __amdgpu_buffer_rsrc_t rx = __builtin_amdgcn_make_buffer_rsrc((void*)x, 0, 8 * D * 2, RSRC3);
    const v2u ep = __builtin_amdgcn_raw_buffer_load_b64(rws, WS_EPOCH, 0, AUX_SC1);
    unsigned tagl, tagh;
#define MAKE_TAGS() do { const u64 e64 = (((u64)ep.y << 32) | ep.x) + 1ull; \
      tagl = __builtin_amdgcn_readfirstlane((unsigned)e64); tagh = __builtin_amdgcn_readfirstlane((unsigned)(e64 >> 32)); } while (0)
    if (tid < 256 + 16) FL[tid - 256] = 0u;
    if (__builtin_expect(isQ, 1)) {
      const int row = hw * 2 + (lane >> 5), c = lane & 31;
      const int xo = c < 28 ? row * (D * 2) + c * 32 : 8 * D * 2;
      const v4u xa = LD128(rx, xo, 0, AUX_NT), xb = LD128(rx, xo + 16, 0, AUX_NT);
      const __amdgpu_buffer_rsrc_t rq = __builtin_amdgcn_make_buffer_rsrc((void*)wq, 0, D * NQ * 2, RSRC3);
      v4u tt[14];
#pragma unroll
      for (int i = 0; i < 14; ++i) tt[i] = LD128(rq, (lane >> 3) * (NQ * 2) + (lane & 7) * 16, (i * 4 + hw) * 8 * (NQ * 2) + (kvh * 8 + r5) * 128, AUX_NT);
      SB();
      BAR();
      {
        unsigned xr[8] = {xa.x, xa.y, xa.z, xa.w, xb.x, xb.y, xb.z, xb.w};
        ln_row<8>(xr, c < 28);
        const int a = c < 28 ? L_LN + row * LN_STR + c * 32 : L_SINK + lane * 32;
        *(LAS v4u*)(L + a) = v4u{xr[0], xr[1], xr[2], xr[3]}; *(LAS v4u*)(L + a + 16) = v4u{xr[4], xr[5], xr[6], xr[7]};
      }
      MAKE_TAGS();
      STAMP(1);
#pragma unroll
      for (int i = 0; i < 14; ++i) *(LAS v4u*)(L + L_RAW + (i * 4 + hw) * 1024 + lane * 16) = tt[i];
      lds_signal(L, FL_LN, lane, 1u);
      lds_wait_ge(FL + FL_LN, 4u, 0);
      v4u af[14], bf[14];
#pragma unroll
      for (int ks = 0; ks < 14; ++ks) { af[ks] = frag_tr(ap + ks * 4096, 512); bf[ks] = *(const LAS v4u*)(bp + ks * 64); }
      SB();
      v4f d0 = {0.f, 0.f, 0.f, 0.f}, d1 = {0.f, 0.f, 0.f, 0.f};
#pragma unroll
      for (int ks = 0; ks < 14; ks += 2) { d0 = mfma(af[ks], bf[ks], d0); d1 = mfma(af[ks + 1], bf[ks + 1], d1); }
      d0 += d1;
      ST128((v4u{pk2(d0.x, d0.y), pk2(d0.z, d0.w), tagl, tagh}), rws,
            (lane & 8) == 0 ? WS_G + ((kvh * 8 + (lane & 7)) * 128 + r5 * 16 + hw * 4 + g) * 16 : WS_END, 0, AUX_SC1);
      lds_signal(L, FL_QP, lane, 1u);
      STAMP(2);
    } else {
      BAR();
      MAKE_TAGS();
    }
    auto q_gather = [&]() {
      const int off = WS_G + (kvh * 8 + b) * 2048 + ((hw - 2) * 64 + lane) * 16;
#pragma nounroll
      while ((int)((unsigned)__builtin_amdgcn_s_memrealtime() - tstart) < QT) __builtin_amdgcn_s_sleep(2);
      v4u gq;
      bool ok = false;
#pragma nounroll
      for (unsigned sp = 0; sp < SPIN_CAP; ++sp) {
        CLOB();
        if (!ok) { gq = LD128(rws, off, 0, AUX_SC1); ok = ((gq.z ^ tagl) | (gq.w ^ tagh)) == 0u; }
        if (__all(ok)) break;
        __builtin_amdgcn_s_sleep(QSLEEP);
      }
      *(LAS v2u*)(L + L_Q + ((hw - 2) * 64 + lane) * 8) = v2u{gq.x, gq.y};
      FL[FL_TAGL] = tagl; FL[FL_TAGH] = tagh;
      lds_signal(L, FL_Q, lane, 2u);
      STAMP(3);
    };
    if (isQ || LATEALL) { if (hw >= 2) q_gather(); lds_wait_ge(FL + FL_Q, 4u, 1); }
    v4u Fr[28];
    v4f cx, cb;
    {
      const v2u bv = __builtin_nontemporal_load((const GAS v2u*)(isW ? b2 + r5 * 16 + (lane & 3) * 4 : b1 + (isM ? m : 0) * 64 + hw * 16 + g * 4));
      const v2u xv = __builtin_amdgcn_raw_buffer_load_b64(rx, (kvh * D + r5 * 16 + (lane & 3) * 4) * 2, 0, AUX_NT);
      cb = v4f{bflo(bv.x), bfhi(bv.x), bflo(bv.y), bfhi(bv.y)};
      cx = v4f{bflo(xv.x), bfhi(xv.x), bflo(xv.y), bfhi(xv.y)};
    }
    auto stage_wo = [&]() {
      const __amdgpu_buffer_rsrc_t rw = __builtin_amdgcn_make_buffer_rsrc((void*)(wo + (kvh * 512 + kq * 128) * D + t * 64), 0, 127 * (D * 2) + 128, RSRC3);
      v4u tt[4];
#pragma unroll
      for (int i = 0; i < 4; ++i) tt[i] = LD128(rw, (lane >> 3) * (D * 2) + (lane & 7) * 16, (i * 4 + hw) * 8 * (D * 2), AUX_NT);
#pragma unroll
      for (int i = 0; i < 4; ++i) *(LAS v4u*)(L + L_RAW + (i * 4 + hw) * 1024 + lane * 16) = tt[i];
      lds_signal(L, FL_RAW, lane, 1u);
    };
    auto frag_wo = [&]() {
      lds_wait_ge(FL + FL_RAW, 4u, 0);
#pragma unroll
      for (int ks = 0; ks < 4; ++ks) Fr[ks] = frag_tr(ap + ks * 4096, 512);
    };
    auto stage_w1 = [&]() {
      const __amdgpu_buffer_rsrc_t rw = __builtin_amdgcn_make_buffer_rsrc((void*)(w1 + m * 64), 0, 447 * (F * 2) + 128, RSRC3);
      v4u tt[14];
#pragma unroll
      for (int i = 0; i < 14; ++i) tt[i] = LD128(rw, (lane >> 3) * (F * 2) + (lane & 7) * 16, (i * 4 + hw) * 8 * (F * 2), AUX_NT);
#pragma unroll
      for (int i = 0; i < 14; ++i) *(LAS v4u*)(L + L_RAW + (i * 4 + hw) * 1024 + lane * 16) = tt[i];
      lds_signal(L, FL_RAW, lane, 1u);
      lds_wait_ge(FL + FL_RAW, 4u, 0);
#pragma unroll
      for (int ks = 0; ks < 14; ++ks) Fr[ks] = frag_tr(ap + ks * 4096, 512);
      lds_signal(L, FL_RAW2, lane, 1u);
    };
#pragma unroll
    for (int i = 0; i < 28; ++i) Fr[i] = v4u{0u, 0u, 0u, 0u};
    if (!(LATEALL & 1) && isW) { stage_wo(); frag_wo(); }
    if (!(LATEALL & 2) && isM) stage_w1();
    asm volatile("" :: "v"(cpf));
    STAMP(4);
    if (!LATEALL && !isQ && hw >= 2) q_gather();
    auto stage_w2 = [&]() {
      const __amdgpu_buffer_rsrc_t r2 = __builtin_amdgcn_make_buffer_rsrc((void*)(w2 + m * 64 * D), 0, 64 * D * 2, RSRC3);
      v4u uu[14];
#pragma unroll
      for (int i = 0; i < 14; ++i) uu[i] = LD128(r2, lane * 16, (i * 4 + hw) * 1024, AUX_NT);
      lds_wait_ge(FL + FL_RAW2, 4u, 0);
#pragma unroll
      for (int i = 0; i < 14; ++i) *(LAS v4u*)(L + L_RAW + (i * 4 + hw) * 1024 + lane * 16) = uu[i];
      lds_signal(L, FL_RAW3, lane, 1u);
      lds_wait_ge(FL + FL_RAW3, 4u, 0);
      const LAS unsigned char* a2 = L + L_RAW + (g * 8 + ((lane & 15) >> 2)) * 896 + hw * 224 + (lane & 3) * 8;
#pragma unroll
      for (int i = 0; i < 14; ++i) Fr[14 + i] = frag_tr(a2 + (i & 1) * 32 * 896 + (i >> 1) * 32, 4 * 896);
    };
    if (!LATEW2 && isM) { lds_wait_ge(FL + FL_Q, 4u, 1); stage_w2(); }
    float fw[4] = {0.f, 0.f, 0.f, 0.f};
    if (hw < 2) {
      lds_wait_ge(FL + FL_ML, 4u, 1);
      const v2f ml = *(const LAS v2f*)(L + L_ML + ((lane & 3) * 2 + hw) * 8);
      float Ms = ml.x;
      Ms = fmaxf(Ms, dpp<XOR1>(Ms)); Ms = fmaxf(Ms, dpp<XOR2>(Ms));
      const float Msub = Ms == -INFINITY ? 0.f : Ms;
      float Lw = __builtin_amdgcn_exp2f((ml.x - Msub) * SC2) * ml.y;
      Lw += dpp<XOR1>(Lw); Lw += dpp<XOR2>(Lw);
      ST128((v4u{__float_as_uint(Ms), __float_as_uint(Lw), tagl, tagh}), rws, lane == 0 ? WS_ML + ((grp * 2 + hw) * 4 + s) * 16 : WS_END, 0, AUX_SC1);
      const int off = WS_ML + ((grp * 2 + hw) * 4 + (lane & 3)) * 16;
      v4u gm;
#pragma nounroll
      for (unsigned sp = 0; sp < SPIN_CAP; ++sp) {
        __builtin_amdgcn_s_sleep(64);
        CLOB();
        gm = LD128(rws, off, 0, AUX_SC1);
        if (__all(((gm.z ^ tagl) | (gm.w ^ tagh)) == 0u)) break;
      }
      float M = __uint_as_float(gm.x);
      M = fmaxf(M, dpp<XOR1>(M)); M = fmaxf(M, dpp<XOR2>(M));
      float Ls = __builtin_amdgcn_exp2f((__uint_as_float(gm.x) - M) * SC2) * __uint_as_float(gm.y);
      Ls += dpp<XOR1>(Ls); Ls += dpp<XOR2>(Ls);
      const float f = __builtin_amdgcn_exp2f((ml.x - M) * SC2) / Ls;
#pragma unroll
      for (int i = 0; i < 4; ++i) fw[i] = __builtin_bit_cast(float, __builtin_amdgcn_readlane(__builtin_bit_cast(int, f), i));
    }
    STAMP(5);
    int p_p0 = WS_P0 + ((kvh * 8 + b) * 4 + s) * 2048 + (hw & 1) * 1024 + lane * 16;
    int p_st = isW ? ((lane & 8) == 0 ? WS_PW + (((kvh * 4 + kq) * 28 + t * 4 + hw) * 8 + (lane & 7)) * 64 + g * 16 : WS_END)
                   : WS_PF + ((m * 28 + hw * 7) * 8 + (lane & 15)) * 64 + g * 16;
    int p_h = lane < 4 ? WS_H + (kvh * 112 + r5 * 4 + lane) * 16 : WS_END;
    int g_p0 = WS_P0 + (kvh * 8 + (lane >> 3)) * 8192 + (kq * 32 + hw * 8 + (lane & 7)) * 16;
    int g_pw = WS_PW + (((lane >> 2) * 28 + r5) * 8 + kvh) * 64 + (lane & 3) * 16;
    int g_o = lane < 4 ? (kvh * D + r5 * 16 + lane * 4) * 2 : 8 * D * 2;
    int g_e = (w == 0 && lane == 0) ? WS_EPOCH : WS_END;
    int a_bw = isW ? L_BF + hw * 512 + lane * 8 : L_BF + (lane & 15) * 144 + (hw * 16 + g * 4) * 2;
    int a_br = isW ? L_BF + (lane & 7) * 64 + g * 16 : L_BF + (lane & 15) * 144 + g * 16;
    asm volatile("" : "+v"(p_p0), "+v"(p_st), "+v"(p_h), "+v"(g_p0), "+v"(g_pw), "+v"(g_o), "+v"(g_e), "+v"(a_bw), "+v"(a_br));
    const __amdgpu_buffer_rsrc_t rout = __builtin_amdgcn_make_buffer_rsrc((void*)out, 0, 8 * D * 2, RSRC3);
    lds_wait_ge(FL + FL_PRE, 4u, 1);
    STAMP(6);
    if (CODEPF && r5 >= 28) { const unsigned z = code_prefetch(hw); asm volatile("" :: "v"(z)); }
    {
      const int k0 = lane == 0 ? 0 : WS_END;
      const unsigned t0 = __builtin_amdgcn_raw_buffer_load_b32(rws, (isW ? g_p0 : WS_H) + k0, 0, AUX_SC1), t1 = __builtin_amdgcn_raw_buffer_load_b32(rws, p_st + k0, 0, AUX_SC1);
      const unsigned t2 = __builtin_amdgcn_raw_buffer_load_b32(rws, p_h + k0, 0, AUX_SC1), t3 = __builtin_amdgcn_raw_buffer_load_b32(rws, p_p0 + k0, 0, AUX_SC1);
      const unsigned t4 = __builtin_amdgcn_raw_buffer_load_b32(rout, g_o + k0, 0, 0);
      asm volatile("" :: "v"(t0), "v"(t1), "v"(t2), "v"(t3), "v"(t4));
    }
    if ((LATEALL & 1) && isW) stage_wo();
    lds_wait_ge(FL + FL_DONE, 4u, 0);
    if (hw < 2) {
      STAMP(7);
      const LAS v4f* mp = (const LAS v4f*)(L + L_MRG + hw * 1024 + lane * 16);
      const v4f o = (mp[0] * fw[0] + mp[128] * fw[1]) + (mp[256] * fw[2] + mp[384] * fw[3]);
      ST128(pack24(o, tagl), rws, p_p0, 0, AUX_SC1);
      STAMP(8);
    }
    if (isW) {
      if (LATEALL & 1) frag_wo();
      if (WDELAY) __builtin_amdgcn_s_sleep(WDELAY);
      v4u pv[4];
      gather<4, 2048, false>(pv, rws, g_p0, tagl, 0u, ~0u);
      STAMP(9);
      const v4f o = (unpack24(pv[0]) + unpack24(pv[1])) + (unpack24(pv[2]) + unpack24(pv[3]));
      *(LAS v2u*)(L + a_bw) = v2u{pk2(o.x, o.y), pk2(o.z, o.w)};
      BAR();
      v4u bf[4];
#pragma unroll
      for (int ks = 0; ks < 4; ++ks) bf[ks] = *(const LAS v4u*)(L + a_br + ks * 512);
      SB();
      v4f d0 = {0.f, 0.f, 0.f, 0.f}, d1 = {0.f, 0.f, 0.f, 0.f};
      d0 = mfma(Fr[0], bf[0], d0); d1 = mfma(Fr[1], bf[1], d1); d0 = mfma(Fr[2], bf[2], d0); d1 = mfma(Fr[3], bf[3], d1);
      ST128(pack24(d0 + d1, tagl), rws, p_st, 0, AUX_SC1);
      STAMP(10);
      if (hw == 0) {
        v4u u[2];
        if (RDELAY) __builtin_amdgcn_s_sleep(RDELAY);
        gather<2, 16 * 28 * 512, false>(u, rws, g_pw, tagl, 0u, ~0u);
        STAMP(11);
        const v4f hv = red16(unpack24(u[0]) + unpack24(u[1]));
        const unsigned h01 = pk2o(cx.x + rbf(hv.x), cx.y + rbf(hv.y)), h23 = pk2o(cx.z + rbf(hv.z), cx.w + rbf(hv.w));
        ST128((v4u{h01, h23, tagl, tagh}), rws, p_h, 0, AUX_SC1);
        STAMP(12);
        __builtin_amdgcn_s_sleep(FWAIT);
        gather<2, 16 * 28 * 512, false>(u, rws, g_pw + (WS_PF - WS_PW), tagl, 0u, lane < 48 ? ~0u : 0u);
        STAMP(13);
        const v4f ov = red16(unpack24(u[0]) + unpack24(u[1]));
        const unsigned o01 = pk2(bflo(h01) + rbf(rbf(ov.x) + cb.x), bfhi(h01) + rbf(rbf(ov.y) + cb.y));
        const unsigned o23 = pk2(bflo(h23) + rbf(rbf(ov.z) + cb.z), bfhi(h23) + rbf(rbf(ov.w) + cb.w));
        __builtin_amdgcn_raw_buffer_store_b64(v2u{o01, o23}, rout, g_o, 0, 0);
        __builtin_amdgcn_raw_buffer_store_b64(v2u{tagl, tagh}, rws, g_e, 0, AUX_SC1);
        STAMP(14);
      }
    } else if (isM) {
      if (LATEALL & 2) stage_w1();
      if (LATEW2) stage_w2();
      mlp_ln(L, rws, wv, lane, tagl, tagh);
      STAMP(15);
      v4u bf[14];
#pragma unroll
      for (int ks = 0; ks < 14; ++ks) bf[ks] = *(const LAS v4u*)(bp + ks * 64);
      SB();
      v4f d0 = {0.f, 0.f, 0.f, 0.f}, d1 = {0.f, 0.f, 0.f, 0.f};
#pragma unroll
      for (int ks = 0; ks < 14; ks += 2) { d0 = mfma(Fr[ks], bf[ks], d0); d1 = mfma(Fr[ks + 1], bf[ks + 1], d1); }
      d0 += d1;
      float u[4];
#pragma unroll
      for (int k = 0; k < 4; ++k) { float a = rbf(rbf(d0[k]) + cb[k]); a = a > 0.f ? a : 0.f; u[k] = a * a; }
      *(LAS v2u*)(L + a_bw) = v2u{pk2(u[0], u[1]), pk2(u[2], u[3])};
      BAR();
      const v4u h0 = *(const LAS v4u*)(L + a_br), h1 = *(const LAS v4u*)(L + a_br + 64);
      const bool up = lane & 8;
#pragma unroll
      for (int i = 0; i < 4; ++i) {
        v4f ea = mfma(Fr[14 + 4 * i], h0, v4f{0.f, 0.f, 0.f, 0.f}), eb = ea;
        if (i < 3) eb = mfma(Fr[14 + 4 * i + 2], h0, v4f{0.f, 0.f, 0.f, 0.f});
        ea = mfma(Fr[14 + 4 * i + 1], h1, ea);
        if (i < 3) eb = mfma(Fr[14 + 4 * i + 3], h1, eb);
        const v4u ev = pack24(v4f{up ? eb.x : ea.x, up ? eb.y : ea.y, up ? eb.z : ea.z, up ? eb.w : ea.w}, tagl);
        ST128(ev, rws, (i == 3 && up) ? WS_END : p_st + i * 1024, 0, AUX_SC1);
      }
      STAMP(16);
    }
  } else {
    const size_t kvoff = ((size_t)grp * SK + (size_t)s * (ILV ? 32 : 1024)) * DH;
    constexpr int TSTR = ILV ? 65536 : 16384, NREC = ILV ? 31 * 65536 + 16384 : 524288;
    const __amdgpu_buffer_rsrc_t rk = __builtin_amdgcn_make_buffer_rsrc((void*)(kc + kvoff), 0, NREC, RSRC3);
    const int voff = lane * 16 + wv * 4096;
    v4u X[NEXTRA * 4][4], R[4][4];
    const unsigned tb = (unsigned)__builtin_amdgcn_s_memrealtime() + (unsigned)(T1) - (unsigned)(STAG == 1 ? ((wv * 4 + (r5 & 3)) * TAU) >> 8 : STAG == 2 ? ((r5 & 15) * TAU) >> 8 : 0);
#define LANDED() asm volatile("s_waitcnt vmcnt(%0)" :: "i"(4 * (LEAD - 1)) : "memory")
    STAMP(0);
    BAR();
#pragma unroll
    for (int n = 0; n < NEXTRA * 4 + LEAD; ++n) {
      if (n >= LEAD) LANDED();
      if (n >= LEAD) pace(tb, n);
      issue_turn(n < NEXTRA * 4 ? X[n] : R[n - NEXTRA * 4], rk, voff, n * TSTR);
    }
    const bool b0 = lane & 1, b1_ = lane & 2, b2_ = lane & 4, b3 = lane & 8;
    const int hf = lane >> 5, c = lane & 31;
    LAS float* P = (LAS float*)(L + L_P) + wv * 512;
    LAS float* Pl = P + ((lane >> 3) & 3) * 16 + hf * 8 + (lane & 7);
    asm volatile("" :: "v"(cpf));
    STAMP(1);
    lds_wait_ge(FL + FL_Q, 4u, 0);
    STAMP(2);
    const v4u q0 = *(const LAS v4u*)(L + L_Q + c * 16), q1 = *(const LAS v4u*)(L + L_Q + 512 + c * 16);
    float mx = -INFINITY;
#pragma unroll
    for (int g = 0; g < NEXTRA; ++g) {
      float r[4], rx[4];
#pragma unroll
      for (int d = 0; d < 4; ++d) {
        rx[d] = k_turn(X[4 * g + d], q0, q1, b0, b1_, b2_);
        SB();
        LANDED();
        pace(tb, (NEXTRA + g) * 4 + d + LEAD);
        issue_turn(R[(d + LEAD) & 3], rk, voff, ((NEXTRA + g) * 4 + d + LEAD) * TSTR);
        r[d] = k_turn(R[d], q0, q1, b0, b1_, b2_);
        SB();
      }
      const float sx = k_group(rx[0], rx[1], rx[2], rx[3], b3), sc = k_group(r[0], r[1], r[2], r[3], b3);
      mx = fmaxf(mx, fmaxf(sx, sc));
      Pl[g * 64] = sx; Pl[(NEXTRA + g) * 64] = sc;
      SB();
    }
    STAMP(3);
    const __amdgpu_buffer_rsrc_t rv = __builtin_amdgcn_make_buffer_rsrc((void*)(vc + kvoff), 0, NREC, RSRC3);
#pragma unroll 1
    for (int g = 2 * NEXTRA; g < 8; ++g) {
      float r[4];
#pragma unroll
      for (int d = 0; d < 4; ++d) {
        const int nx = g * 4 + d + LEAD;
        const bool tov = (d + LEAD >= 4) && g == 7;
        const __amdgpu_buffer_rsrc_t rn = __builtin_amdgcn_make_buffer_rsrc((void*)((tov ? vc : kc) + kvoff), 0, NREC, RSRC3);
        LANDED();
        pace(tb, nx);
        issue_turn(R[(d + LEAD) & 3], rn, voff, (nx & 31) * TSTR);
        r[d] = k_turn(R[d], q0, q1, b0, b1_, b2_);
        SB();
      }
      const float sc = k_group(r[0], r[1], r[2], r[3], b3);
      mx = fmaxf(mx, sc);
      Pl[g * 64] = sc;
    }
    STAMP(4);
    mx = fmaxf(mx, dpp<XOR2>(mx)); mx = fmaxf(mx, dpp<ROR4>(mx)); mx = fmaxf(mx, dpp<ROR8>(mx));
    mx = sw16_max(mx); mx = sw32_max(mx);
    const float msub = mx == -INFINITY ? 0.f : mx;
    float ls = 0.f;
    {
      float sv[8];
#pragma unroll
      for (int g = 0; g < 8; ++g) sv[g] = Pl[g * 64];
#pragma unroll
      for (int g = 0; g < 8; ++g) { const float p = __builtin_amdgcn_exp2f((sv[g] - msub) * SC2); ls += p; Pl[g * 64] = p; }
    }
    ls += dpp<XOR2>(ls); ls += dpp<ROR8>(ls); ls += dpp<ROR4>(ls);
    ls = sw16_add(ls, ls); ls = sw32_add(ls, ls);
    *(LAS v2f*)(L + (lane < 2 ? L_ML + (wv * 2 + lane) * 8 : L_SINK + 1024 + lane * 8)) = v2f{mx, ls};
    lds_signal(L, FL_ML, lane, 1u);
    v2f a0[4], a1[4];
#pragma unroll
    for (int k = 0; k < 4; ++k) { a0[k] = v2f{0.f, 0.f}; a1[k] = v2f{0.f, 0.f}; }
    const LAS v4f* Pr = (const LAS v4f*)(P + hf * 8);
    LAS v4f* mo = (LAS v4f*)(L + L_MRG + wv * 2048 + lane * 32);
#pragma unroll 1
    for (int g = 0; g < 8; ++g) {
      v4f pa[4], pb[4];
#pragma unroll
      for (int d = 0; d < 4; ++d) { pa[d] = Pr[(g * 4 + d) * 4]; pb[d] = Pr[(g * 4 + d) * 4 + 1]; }
#pragma unroll
      for (int d = 0; d < 4; ++d) {
        LANDED();
        pace(tb, 32 + g * 4 + d + LEAD);
        issue_turn(R[(d + LEAD) & 3], rv, voff, (g * 4 + d + LEAD) * TSTR);
        v_turn(R[d], pa[d], pb[d], a0, a1);
        SB();
        if (d == PRED) lds_signal(L, FL_PRE, lane, g == PREG ? 1u : 0u);
        if (d == 3) {
          mo[0] = v4f{sw32_add(a0[0].x, a1[0].x), sw32_add(a0[0].y, a1[0].y), sw32_add(a0[1].x, a1[1].x), sw32_add(a0[1].y, a1[1].y)};
          mo[1] = v4f{sw32_add(a0[2].x, a1[2].x), sw32_add(a0[2].y, a1[2].y), sw32_add(a0[3].x, a1[3].x), sw32_add(a0[3].y, a1[3].y)};
          lds_signal(L, FL_DONE, lane, g == 7 ? 1u : 0u);
          SB();
        }
      }
    }
    STAMP(5);
    if (isM) mlp_ln(L, rws, wv, lane, __builtin_amdgcn_readfirstlane(FL[FL_TAGL]), __builtin_amdgcn_readfirstlane(FL[FL_TAGH]));
  }
}
}

extern "C" void kernel_launch(void* const* d_in, const int* in_sizes, int n_in, void* d_out, int out_size, void* d_ws,
                              size_t ws_size, hipStream_t stream) {
  using namespace fk;
  (void)hipFuncSetAttribute((const void*)fused_decode, hipFuncAttributeMaxDynamicSharedMemorySize, L_END + DBG_STAMPS * 2048);
  hipLaunchKernelGGL(fused_decode, dim3(256), dim3(512), L_END + DBG_STAMPS * 2048, stream, (const u16*)d_in[0], (const u16*)d_in[1],
                     (const u16*)d_in[2], (const u16*)d_in[3], (const u16*)d_in[4], (const u16*)d_in[6], (const u16*)d_in[7],
                     (const u16*)d_in[8], (const u16*)d_in[9], (u16*)d_out, (unsigned char*)d_ws);
}
```

```cpp
#include <hip/hip_runtime.h>
#include <hip/hip_bf16.h>
#include <stdint.h>

#define DBG_STAMPS 0
#define NEXTRA 2
#define LEAD 2
#define TAU 896
#define T1 0
#define TAU0 1100
#define TJ 0
#define PN0 16
#define PN1 64
#define LATEALL 0
#define PRED 1
#define STAG 0
#define ILV 1
#define QT 350
#define QSLEEP 8
#define LATEW2 1
#define CODEPF 1
#define CODE_BYTES 27648
#define PRETOUCH 0
#define WDELAY 20
#define RDELAY 12
#define MDELAY 0
#define FWAIT 16
#define PREG 6

namespace fk {
typedef unsigned short u16;
typedef unsigned long long u64;
typedef unsigned v4u __attribute__((ext_vector_type(4)));
typedef unsigned v2u __attribute__((ext_vector_type(2)));
typedef float v4f __attribute__((ext_vector_type(4)));
typedef float v2f __attribute__((ext_vector_type(2)));
typedef __bf16 bf16x2 __attribute__((ext_vector_type(2)));
typedef __bf16 bf16x8 __attribute__((ext_vector_type(8)));
typedef short s16x4 __attribute__((ext_vector_type(4)));
#define LAS __attribute__((address_space(3)))
#define GAS __attribute__((address_space(1)))

constexpr int D = 448, F = 1792, SK = 4096, DH = 256, NQ = 4096;
constexpr int AUX_NT = 2, AUX_SC1 = 16;
constexpr int RSRC3 = 0x00020000;
constexpr float SC2 = 0.0625f * 1.4426950408889634f;
struct Spin {
  unsigned n = 0, t0 = 0;
  __device__ __forceinline__ bool expired() {
    if ((++n & 4095u) != 0u) return false;
    const unsigned t = (unsigned)__builtin_amdgcn_s_memrealtime();
    if (n == 4096u) t0 = t;
    return t - t0 > (1u << 27);
  }
};

constexpr int WS_EPOCH = 0;
constexpr int WS_G = 4096;
constexpr int WS_ML = WS_G + 8 * 8 * 128 * 16;
constexpr int WS_H = WS_ML + 64 * 8 * 16;
constexpr int WS_P0 = WS_H + 16384;
constexpr int WS_PW = WS_P0 + 64 * 4 * 2048;
constexpr int WS_PF = WS_PW + 32 * 28 * 512;
constexpr int WS_END = WS_PF + 28 * 28 * 512;
constexpr int WS_DBG = WS_END;

constexpr int LN_STR = 912;
constexpr int L_P = 0;
constexpr int L_MRG = 8192;
constexpr int L_Q = 16384;
constexpr int L_FLAG = 17408;
constexpr int L_ML = L_FLAG + 64;
constexpr int L_LN = L_ML + 64;
constexpr int L_BF = L_LN + 8 * LN_STR;
constexpr int L_SINK = L_BF + 4096;
constexpr int L_RAW = L_SINK + 2048;
constexpr int L_END = 96 * 1024;
static_assert(L_RAW + 57344 <= L_END, "lds");
static_assert(L_LN % 16 == 0 && L_BF % 16 == 0 && L_RAW % 16 == 0, "align");
enum { FL_LN = 0, FL_QP = 1, FL_Q = 2, FL_PRE = 3, FL_DONE = 4, FL_RAW = 5, FL_RAW2 = 6, FL_RAW3 = 7, FL_ML = 9, FL_TAGL = 10, FL_TAGH = 11 };

__device__ __forceinline__ float bflo(unsigned v) { return __builtin_bit_cast(float, v << 16); }
__device__ __forceinline__ float bfhi(unsigned v) { return __builtin_bit_cast(float, v & 0xffff0000u); }
__device__ __forceinline__ float rbf(float f) { return (float)(__bf16)f; }
__device__ __forceinline__ unsigned pk2(float lo, float hi) {
  bf16x2 v = {(__bf16)lo, (__bf16)hi};
  return __builtin_bit_cast(unsigned, v);
}
__device__ __forceinline__ unsigned pk2o(float lo, float hi) {
  unsigned p = pk2(lo, hi);
  asm("" : "+v"(p));
  return p;
}
__device__ __forceinline__ float dot2(unsigned a, unsigned b, float c) {
  return __builtin_amdgcn_fdot2_f32_bf16(__builtin_bit_cast(bf16x2, a), __builtin_bit_cast(bf16x2, b), c, false);
}
__device__ __forceinline__ v4f mfma(v4u a, v4u b, v4f c) {
  return __builtin_amdgcn_mfma_f32_16x16x32_bf16(__builtin_bit_cast(bf16x8, a), __builtin_bit_cast(bf16x8, b), c, 0, 0, 0);
}
template <int CTRL> __device__ __forceinline__ float dpp(float x) {
  return __builtin_bit_cast(float, __builtin_amdgcn_mov_dpp(__builtin_bit_cast(int, x), CTRL, 0xf, 0xf, true));
}
constexpr int XOR1 = 0xB1, XOR2 = 0x4E, HMIR = 0x141, ROR4 = 0x124, ROR8 = 0x128;
__device__ __forceinline__ float sw16_add(float a, float b) {
  auto s = __builtin_amdgcn_permlane16_swap(__float_as_uint(a), __float_as_uint(b), false, false);
  return __uint_as_float(s[0]) + __uint_as_float(s[1]);
}
__device__ __forceinline__ float sw32_add(float a, float b) {
  auto s = __builtin_amdgcn_permlane32_swap(__float_as_uint(a), __float_as_uint(b), false, false);
  return __uint_as_float(s[0]) + __uint_as_float(s[1]);
}
__device__ __forceinline__ float sw16_max(float a) {
  auto s = __builtin_amdgcn_permlane16_swap(__float_as_uint(a), __float_as_uint(a), false, false);
  return fmaxf(__uint_as_float(s[0]), __uint_as_float(s[1]));
}
__device__ __forceinline__ float sw32_max(float a) {
  auto s = __builtin_amdgcn_permlane32_swap(__float_as_uint(a), __float_as_uint(a), false, false);
  return fmaxf(__uint_as_float(s[0]), __uint_as_float(s[1]));
}
__device__ __forceinline__ v4u pack24(v4f v, unsigned tag) {
  const unsigned a = __float_as_uint(v.x), b = __float_as_uint(v.y), c = __float_as_uint(v.z), d = __float_as_uint(v.w);
  return v4u{__builtin_amdgcn_perm(b, a, 0x05030201u), __builtin_amdgcn_perm(c, b, 0x06050302u), __builtin_amdgcn_perm(d, c, 0x07060503u), tag};
}
__device__ __forceinline__ v4f unpack24(v4u g) {
  return v4f{__uint_as_float(g.x << 8), __uint_as_float(__builtin_amdgcn_perm(g.y, g.x, 0x0504030Cu)),
             __uint_as_float(__builtin_amdgcn_perm(g.z, g.y, 0x0403020Cu)), __uint_as_float(g.z & 0xFFFFFF00u)};
}
#define LGKM0() asm volatile("s_waitcnt lgkmcnt(0)" ::: "memory")
#define CLOB() asm volatile("" ::: "memory")
#define SB() __builtin_amdgcn_sched_barrier(0)
#define BAR() do { asm volatile("s_waitcnt lgkmcnt(0)" ::: "memory"); __builtin_amdgcn_s_barrier(); asm volatile("" ::: "memory"); __builtin_amdgcn_sched_barrier(0); } while (0)
#define LD128(rs, vo, so, aux) __builtin_amdgcn_raw_buffer_load_b128(rs, vo, so, aux)
#define ST128(v, rs, vo, so, aux) __builtin_amdgcn_raw_buffer_store_b128(v, rs, vo, so, aux)

#define STAMP(i) do {} while (0)

__device__ __forceinline__ void lds_wait_ge(volatile LAS unsigned* f, unsigned want, int slp) {
  Spin sp;
#pragma nounroll
  while (*f < want) {
    if (slp) __builtin_amdgcn_s_sleep(8);
    if (sp.expired()) break;
  }
  CLOB();
}
__device__ __forceinline__ void lds_signal(LAS unsigned char* L, int word, int lane, unsigned inc) {
  LGKM0();
  LAS unsigned* p = (LAS unsigned*)(L + (lane == 0 ? L_FLAG + word * 4 : L_SINK + lane * 4));
  __hip_atomic_fetch_add(p, inc, __ATOMIC_RELAXED, __HIP_MEMORY_SCOPE_WORKGROUP);
}
template <int N, int STR, bool WIDE, int SLP>
__device__ __forceinline__ void gather(v4u (&v)[N], __amdgpu_buffer_rsrc_t rs, int off, unsigned tl, unsigned th, u64 last_mask) {
  Spin sp;
#pragma nounroll
  for (;;) {
    CLOB();
#pragma unroll
    for (int i = 0; i < N; ++i) v[i] = LD128(rs, off + i * STR, 0, AUX_SC1);
    u64 any = 0ull;
#pragma unroll
    for (int i = 0; i < N; ++i) {
      const u64 bad = __builtin_amdgcn_ballot_w64(WIDE ? ((v[i].z ^ tl) | (v[i].w ^ th)) != 0u : v[i].w != tl);
      any |= i == N - 1 ? bad & last_mask : bad;
    }
    if (any == 0ull || sp.expired()) break;
    if (SLP) __builtin_amdgcn_s_sleep(SLP);
  }
}
__device__ __forceinline__ void pace(unsigned tb, int n) {
  const unsigned until = tb + (unsigned)((n >= PN1 ? -65536 : n < PN0 ? n * TAU0 : PN0 * TAU0 + TJ * 16 + (n - PN0) * TAU) >> 4);
  asm volatile("8:\n\ts_memrealtime vcc\n\ts_waitcnt lgkmcnt(0)\n\ts_sub_i32 vcc_lo, vcc_lo, %0\n\ts_cmp_lt_i32 vcc_lo, 0\n\ts_cbranch_scc1 8b\n" :: "s"(until) : "scc", "vcc", "memory");
}
__device__ __forceinline__ void issue_turn(v4u* r, __amdgpu_buffer_rsrc_t rs, int voff, int soff) {
#pragma unroll
  for (int i = 0; i < 4; ++i) r[i] = LD128(rs, voff + i * 1024, soff, AUX_NT);
  SB();
}
__device__ __forceinline__ float k_turn(const v4u* k, v4u q0, v4u q1, bool b0, bool b1, bool b2) {
  float x[8];
#pragma unroll
  for (int i = 0; i < 4; ++i) {
    float a0 = dot2(k[i].x, q0.x, 0.f), a1 = dot2(k[i].x, q1.x, 0.f);
    a0 = dot2(k[i].y, q0.y, a0); a1 = dot2(k[i].y, q1.y, a1);
    a0 = dot2(k[i].z, q0.z, a0); a1 = dot2(k[i].z, q1.z, a1);
    a0 = dot2(k[i].w, q0.w, a0); a1 = dot2(k[i].w, q1.w, a1);
    x[2 * i] = a0; x[2 * i + 1] = a1;
  }
  float y[4], z[2];
#pragma unroll
  for (int v = 0; v < 4; ++v) y[v] = (b2 ? x[v + 4] : x[v]) + dpp<HMIR>(b2 ? x[v] : x[v + 4]);
#pragma unroll
  for (int v = 0; v < 2; ++v) z[v] = (b1 ? y[v + 2] : y[v]) + dpp<XOR2>(b1 ? y[v] : y[v + 2]);
  return (b0 ? z[1] : z[0]) + dpp<XOR1>(b0 ? z[0] : z[1]);
}
__device__ __forceinline__ float k_group(float r0, float r1, float r2, float r3, bool b3) {
  const float u0 = (b3 ? r1 : r0) + dpp<ROR8>(b3 ? r0 : r1);
  const float u1 = (b3 ? r3 : r2) + dpp<ROR8>(b3 ? r2 : r3);
  return rbf(sw16_add(u0, u1));
}
__device__ __forceinline__ void v_turn(const v4u* v, v4f pa, v4f pb, v2f (&a0)[4], v2f (&a1)[4]) {
#pragma unroll
  for (int i = 0; i < 4; ++i) {
    const float p0 = i == 0 ? pa.x : i == 1 ? pa.z : i == 2 ? pb.x : pb.z;
    const float p1 = i == 0 ? pa.y : i == 1 ? pa.w : i == 2 ? pb.y : pb.w;
#pragma unroll
    for (int j = 0; j < 4; ++j) {
      const v2f e = {bflo(v[i][j]), bfhi(v[i][j])};
      a0[j] = __builtin_elementwise_fma(e, v2f{p0, p0}, a0[j]);
      a1[j] = __builtin_elementwise_fma(e, v2f{p1, p1}, a1[j]);
    }
  }
}
template <bool FULL> __device__ __forceinline__ float row_sum(float v) {
  v += dpp<XOR1>(v); v += dpp<XOR2>(v); v += dpp<HMIR>(v); v += dpp<ROR8>(v);
  v = sw16_add(v, v);
  return FULL ? sw32_add(v, v) : v;
}
template <int NW> __device__ __forceinline__ void ln_row(unsigned (&x)[NW], bool act) {
  constexpr unsigned ONES = 0x3F803F80u;
  float sa = 0.f, sb = 0.f;
#pragma unroll
  for (int k = 0; k < NW; k += 2) { sa = dot2(x[k], ONES, sa); sb = dot2(x[k + 1], ONES, sb); }
  const float mean = row_sum<NW == 4>(sa + sb) * (1.0f / D);
  v2f v[NW];
#pragma unroll
  for (int k = 0; k < NW; ++k) v[k] = v2f{bflo(x[k]), bfhi(x[k])};
  v2f q0 = {0.f, 0.f}, q1 = {0.f, 0.f};
#pragma unroll
  for (int k = 0; k < NW; k += 2) {
    const v2f d0 = v[k] - mean, d1 = v[k + 1] - mean;
    q0 = __builtin_elementwise_fma(d0, d0, q0); q1 = __builtin_elementwise_fma(d1, d1, q1);
  }
  q0 += q1;
  const float sq = row_sum<NW == 4>(act ? q0.x + q0.y : 0.f);
  const float mb = rbf(mean);
#pragma unroll
  for (int k = 0; k < NW; ++k) { const v2f d = v[k] - mb; const unsigned p = pk2o(d.x, d.y); v[k] = v2f{bflo(p), bfhi(p)}; }
  const float den = rbf(__builtin_amdgcn_sqrtf(rbf(rbf(sq * (1.0f / D)) + 1e-5f)));
  const float ri = __builtin_amdgcn_rcpf(den);
#pragma unroll
  for (int k = 0; k < NW; ++k) { const v2f a = v[k] * ri; x[k] = pk2(a.x, a.y); }
}
__device__ __forceinline__ v4u frag_tr(const LAS unsigned char* p, int hi) {
#if defined(__HIP_DEVICE_COMPILE__)
  const v2u a = __builtin_bit_cast(v2u, __builtin_amdgcn_ds_read_tr16_b64_v4i16((LAS s16x4*)p));
  const v2u b = __builtin_bit_cast(v2u, __builtin_amdgcn_ds_read_tr16_b64_v4i16((LAS s16x4*)(p + hi)));
  return v4u{a.x, a.y, b.x, b.y};
#else
  return v4u{0u, 0u, 0u, 0u};
#endif
}
__device__ __forceinline__ v4f red16(v4f t) {
#pragma unroll
  for (int k = 0; k < 4; ++k) { float a = t[k]; a += dpp<ROR8>(a); a += dpp<ROR4>(a); a = sw16_add(a, a); a = sw32_add(a, a); t[k] = a; }
  return t;
}
__device__ __forceinline__ void mlp_ln(LAS unsigned char* L, __amdgpu_buffer_rsrc_t rws, int row, int lane, unsigned tl, unsigned th) {
  const bool act = lane < 56;
  const int a = act ? L_LN + row * LN_STR + lane * 8 : L_SINK + lane * 8;
  v4u hg[2];
  if (MDELAY) __builtin_amdgcn_s_sleep(MDELAY);
  gather<2, 896, true, 0>(hg, rws, WS_H + (row * 112 + (act ? lane : 55)) * 16, tl, th, ~0ull);
  { const int wv = row; (void)wv; STAMP(17); }
  unsigned x[4] = {act ? hg[0].x : 0u, act ? hg[0].y : 0u, act ? hg[1].x : 0u, act ? hg[1].y : 0u};
  ln_row<4>(x, act);
  *(LAS v2u*)(L + a) = v2u{x[0], x[1]}; *(LAS v2u*)(L + a + 448) = v2u{x[2], x[3]};
  BAR();
}

__global__ void __launch_bounds__(512, 2)
fused_decode(const u16* __restrict__ x, const u16* __restrict__ kc, const u16* __restrict__ vc, const u16* __restrict__ wq,
             const u16* __restrict__ wo, const u16* __restrict__ w1, const u16* __restrict__ b1, const u16* __restrict__ w2,
             const u16* __restrict__ b2, u16* __restrict__ out, unsigned char* __restrict__ ws) {
  extern __shared__ __attribute__((aligned(16))) unsigned char smem[];
  LAS unsigned char* L = (LAS unsigned char*)smem;
  const u64 pc0 = __builtin_amdgcn_s_getpc() & ~0xFFull;
  const int w = blockIdx.x, tid = threadIdx.x, lane = tid & 63, wv = __builtin_amdgcn_readfirstlane(tid >> 6);
  const int kvh = w & 7, r5 = w >> 3, b = r5 & 7, s = r5 >> 3, grp = b * 8 + kvh;
  const int m = (r5 - 28) * 8 + kvh;
  const bool isM = r5 >= 28 && m < 28;
  volatile LAS unsigned* FL = (volatile LAS unsigned*)(L + L_FLAG);
  const __amdgpu_buffer_rsrc_t rws = __builtin_amdgcn_make_buffer_rsrc((void*)ws, 0, WS_END, RSRC3);
  auto code_prefetch = [&](int part) {
    const __amdgpu_buffer_rsrc_t rc = __builtin_amdgcn_make_buffer_rsrc((void*)pc0, 0, CODE_BYTES, RSRC3);
    return __builtin_amdgcn_raw_buffer_load_b32(rc, lane * 128, part * 8192, 0);
  };
  unsigned cpf = 0;
  if (CODEPF && (r5 >= 28 || r5 < 8)) cpf = code_prefetch(wv & 3);
  SB();
  asm volatile("" :: "s"(x), "s"(kc), "s"(vc), "s"(wq), "s"(wo), "s"(w1), "s"(b1), "s"(w2), "s"(b2), "s"(out), "s"(ws));

  if (__builtin_expect(wv >= 4, 1)) {
    const int hw = wv - 4;
    STAMP(0);
    __builtin_amdgcn_s_setprio(3);
    const unsigned tstart = (unsigned)__builtin_amdgcn_s_memrealtime();
    const bool isW = r5 < 28;
    const bool isQ = r5 < 8;
    const int t = r5 >> 2, kq = r5 & 3;
    const int g = lane >> 4;
    const LAS unsigned char* ap = L + L_RAW + (g * 8 + ((lane & 15) >> 2)) * 128 + hw * 32 + (lane & 3) * 8;
    const LAS unsigned char* bp = L + L_LN + (lane & 7) * LN_STR + g * 16;
    const __amdgpu_buffer_rsrc_t rx = __builtin_amdgcn_make_buffer_rsrc((void*)x, 0, 8 * D * 2, RSRC3);
    const v2u ep = __builtin_amdgcn_raw_buffer_load_b64(rws, WS_EPOCH, 0, AUX_SC1);
    unsigned tagl, tagh;
#define MAKE_TAGS() do { const unsigned el = __builtin_amdgcn_readfirstlane(ep.x), eh = __builtin_amdgcn_readfirstlane(ep.y); \
      u64 z = (((u64)eh << 32) | el) + 0x9E3779B97F4A7C15ull; \
      z = (z ^ (z >> 30)) * 0xBF58476D1CE4E5B9ull; z = (z ^ (z >> 27)) * 0x94D049BB133111EBull; z ^= z >> 31; \
      tagl = (unsigned)z == el ? ~el : (unsigned)z; tagh = (unsigned)(z >> 32); } while (0)
    if (tid < 256 + 16) FL[tid - 256] = 0u;
    if (__builtin_expect(isQ, 1)) {
      const int row = hw * 2 + (lane >> 5), c = lane & 31;
      const int xo = c < 28 ? row * (D * 2) + c * 32 : 8 * D * 2;
      const v4u xa = LD128(rx, xo, 0, AUX_NT), xb = LD128(rx, xo + 16, 0, AUX_NT);
      const __amdgpu_buffer_rsrc_t rq = __builtin_amdgcn_make_buffer_rsrc((void*)wq, 0, D * NQ * 2, RSRC3);
      v4u tt[14];
#pragma unroll
      for (int i = 0; i < 14; ++i) tt[i] = LD128(rq, (lane >> 3) * (NQ * 2) + (lane & 7) * 16, (i * 4 + hw) * 8 * (NQ * 2) + (kvh * 8 + r5) * 128, AUX_NT);
      SB();
      BAR();
      {
        unsigned xr[8] = {xa.x, xa.y, xa.z, xa.w, xb.x, xb.y, xb.z, xb.w};
        ln_row<8>(xr, c < 28);
        const int a = c < 28 ? L_LN + row * LN_STR + c * 32 : L_SINK + lane * 32;
        *(LAS v4u*)(L + a) = v4u{xr[0], xr[1], xr[2], xr[3]}; *(LAS v4u*)(L + a + 16) = v4u{xr[4], xr[5], xr[6], xr[7]};
      }
      MAKE_TAGS();
      STAMP(1);
#pragma unroll
      for (int i = 0; i < 14; ++i) *(LAS v4u*)(L + L_RAW + (i * 4 + hw) * 1024 + lane * 16) = tt[i];
      lds_signal(L, FL_LN, lane, 1u);
      lds_wait_ge(FL + FL_LN, 4u, 0);
      v4u af[14], bf[14];
#pragma unroll
      for (int ks = 0; ks < 14; ++ks) { af[ks] = frag_tr(ap + ks * 4096, 512); bf[ks] = *(const LAS v4u*)(bp + ks * 64); }
      SB();
      v4f d0 = {0.f, 0.f, 0.f, 0.f}, d1 = {0.f, 0.f, 0.f, 0.f};
#pragma unroll
      for (int ks = 0; ks < 14; ks += 2) { d0 = mfma(af[ks], bf[ks], d0); d1 = mfma(af[ks + 1], bf[ks + 1], d1); }
      d0 += d1;
      ST128((v4u{pk2(d0.x, d0.y), pk2(d0.z, d0.w), tagl, tagh}), rws,
            (lane & 8) == 0 ? WS_G + ((kvh * 8 + (lane & 7)) * 128 + r5 * 16 + hw * 4 + g) * 16 : WS_END, 0, AUX_SC1);
      lds_signal(L, FL_QP, lane, 1u);
      STAMP(2);
    } else {
      BAR();
      MAKE_TAGS();
    }
    auto q_gather = [&]() {
      const int off = WS_G + (kvh * 8 + b) * 2048 + ((hw - 2) * 64 + lane) * 16;
#pragma nounroll
      while ((int)((unsigned)__builtin_amdgcn_s_memrealtime() - tstart) < QT) __builtin_amdgcn_s_sleep(2);
      v4u gq[1];
      gather<1, 0, true, QSLEEP>(gq, rws, off, tagl, tagh, ~0ull);
      *(LAS v2u*)(L + L_Q + ((hw - 2) * 64 + lane) * 8) = v2u{gq[0].x, gq[0].y};
      FL[FL_TAGL] = tagl; FL[FL_TAGH] = tagh;
      lds_signal(L, FL_Q, lane, 2u);
      STAMP(3);
    };
    if (isQ || LATEALL) { if (hw >= 2) q_gather(); lds_wait_ge(FL + FL_Q, 4u, 1); }
    v4u Fr[28];
    v4f cx, cb;
    {
      const v2u bv = __builtin_nontemporal_load((const GAS v2u*)(isW ? b2 + r5 * 16 + (lane & 3) * 4 : b1 + (isM ? m : 0) * 64 + hw * 16 + g * 4));
      const v2u xv = __builtin_amdgcn_raw_buffer_load_b64(rx, (kvh * D + r5 * 16 + (lane & 3) * 4) * 2, 0, AUX_NT);
      cb = v4f{bflo(bv.x), bfhi(bv.x), bflo(bv.y), bfhi(bv.y)};
      cx = v4f{bflo(xv.x), bfhi(xv.x), bflo(xv.y), bfhi(xv.y)};
    }
    auto stage_wo = [&]() {
      const __amdgpu_buffer_rsrc_t rw = __builtin_amdgcn_make_buffer_rsrc((void*)(wo + (kvh * 512 + kq * 128) * D + t * 64), 0, 127 * (D * 2) + 128, RSRC3);
      v4u tt[4];
#pragma unroll
      for (int i = 0; i < 4; ++i) tt[i] = LD128(rw, (lane >> 3) * (D * 2) + (lane & 7) * 16, (i * 4 + hw) * 8 * (D * 2), AUX_NT);
#pragma unroll
      for (int i = 0; i < 4; ++i) *(LAS v4u*)(L + L_RAW + (i * 4 + hw) * 1024 + lane * 16) = tt[i];
      lds_signal(L, FL_RAW, lane, 1u);
    };
    auto frag_wo = [&]() {
      lds_wait_ge(FL + FL_RAW, 4u, 0);
#pragma unroll
      for (int ks = 0; ks < 4; ++ks) Fr[ks] = frag_tr(ap + ks * 4096, 512);
    };
    auto stage_w1 = [&]() {
      const __amdgpu_buffer_rsrc_t rw = __builtin_amdgcn_make_buffer_rsrc((void*)(w1 + m * 64), 0, 447 * (F * 2) + 128, RSRC3);
      v4u tt[14];
#pragma unroll
      for (int i = 0; i < 14; ++i) tt[i] = LD128(rw, (lane >> 3) * (F * 2) + (lane & 7) * 16, (i * 4 + hw) * 8 * (F * 2), AUX_NT);
#pragma unroll
      for (int i = 0; i < 14; ++i) *(LAS v4u*)(L + L_RAW + (i * 4 + hw) * 1024 + lane * 16) = tt[i];
      lds_signal(L, FL_RAW, lane, 1u);
      lds_wait_ge(FL + FL_RAW, 4u, 0);
#pragma unroll
      for (int ks = 0; ks < 14; ++ks) Fr[ks] = frag_tr(ap + ks * 4096, 512);
      lds_signal(L, FL_RAW2, lane, 1u);
    };
#pragma unroll
    for (int i = 0; i < 28; ++i) Fr[i] = v4u{0u, 0u, 0u, 0u};
    if (!(LATEALL & 1) && isW) { stage_wo(); frag_wo(); }
    if (!(LATEALL & 2) && isM) stage_w1();
    asm volatile("" :: "v"(cpf));
    STAMP(4);
    if (!LATEALL && !isQ && hw >= 2) q_gather();
    auto stage_w2 = [&]() {
      const __amdgpu_buffer_rsrc_t r2 = __builtin_amdgcn_make_buffer_rsrc((void*)(w2 + m * 64 * D), 0, 64 * D * 2, RSRC3);
      v4u uu[14];
#pragma unroll
      for (int i = 0; i < 14; ++i) uu[i] = LD128(r2, lane * 16, (i * 4 + hw) * 1024, AUX_NT);
      lds_wait_ge(FL + FL_RAW2, 4u, 0);
#pragma unroll
      for (int i = 0; i < 14; ++i) *(LAS v4u*)(L + L_RAW + (i * 4 + hw) * 1024 + lane * 16) = uu[i];
      lds_signal(L, FL_RAW3, lane, 1u);
      lds_wait_ge(FL + FL_RAW3, 4u, 0);
      const LAS unsigned char* a2 = L + L_RAW + (g * 8 + ((lane & 15) >> 2)) * 896 + hw * 224 + (lane & 3) * 8;
#pragma unroll
      for (int i = 0; i < 14; ++i) Fr[14 + i] = frag_tr(a2 + (i & 1) * 32 * 896 + (i >> 1) * 32, 4 * 896);
    };
    if (!LATEW2 && isM) { lds_wait_ge(FL + FL_Q, 4u, 1); stage_w2(); }
    float fw[4] = {0.f, 0.f, 0.f, 0.f};
    if (hw < 2) {
      lds_wait_ge(FL + FL_ML, 4u, 1);
      const v2f ml = *(const LAS v2f*)(L + L_ML + ((lane & 3) * 2 + hw) * 8);
      float Ms = ml.x;
      Ms = fmaxf(Ms, dpp<XOR1>(Ms)); Ms = fmaxf(Ms, dpp<XOR2>(Ms));
      const float Msub = Ms == -INFINITY ? 0.f : Ms;
      float Lw = __builtin_amdgcn_exp2f((ml.x - Msub) * SC2) * ml.y;
      Lw += dpp<XOR1>(Lw); Lw += dpp<XOR2>(Lw);
      ST128((v4u{__float_as_uint(Ms), __float_as_uint(Lw), tagl, tagh}), rws, lane == 0 ? WS_ML + ((grp * 2 + hw) * 4 + s) * 16 : WS_END, 0, AUX_SC1);
      const int off = WS_ML + ((grp * 2 + hw) * 4 + (lane & 3)) * 16;
      v4u gm1[1];
      __builtin_amdgcn_s_sleep(64);
      gather<1, 0, true, 64>(gm1, rws, off, tagl, tagh, ~0ull);
      const v4u gm = gm1[0];
      float M = __uint_as_float(gm.x);
      M = fmaxf(M, dpp<XOR1>(M)); M = fmaxf(M, dpp<XOR2>(M));
      float Ls = __builtin_amdgcn_exp2f((__uint_as_float(gm.x) - M) * SC2) * __uint_as_float(gm.y);
      Ls += dpp<XOR1>(Ls); Ls += dpp<XOR2>(Ls);
      const float f = __builtin_amdgcn_exp2f((ml.x - M) * SC2) / Ls;
#pragma unroll
      for (int i = 0; i < 4; ++i) fw[i] = __builtin_bit_cast(float, __builtin_amdgcn_readlane(__builtin_bit_cast(int, f), i));
    }
    STAMP(5);
    int p_p0 = WS_P0 + ((kvh * 8 + b) * 4 + s) * 2048 + (hw & 1) * 1024 + lane * 16;
    int p_st = isW ? ((lane & 8) == 0 ? WS_PW + (((kvh * 4 + kq) * 28 + t * 4 + hw) * 8 + (lane & 7)) * 64 + g * 16 : WS_END)
                   : WS_PF + ((m * 28 + hw * 7) * 8 + (lane & 15)) * 64 + g * 16;
    int p_h = lane < 4 ? WS_H + (kvh * 112 + r5 * 4 + lane) * 16 : WS_END;
    int g_p0 = WS_P0 + (kvh * 8 + (lane >> 3)) * 8192 + (kq * 32 + hw * 8 + (lane & 7)) * 16;
    int g_pw = WS_PW + (((lane >> 2) * 28 + r5) * 8 + kvh) * 64 + (lane & 3) * 16;
    int g_o = lane < 4 ? (kvh * D + r5 * 16 + lane * 4) * 2 : 8 * D * 2;
    int g_e = (w == 0 && lane == 0) ? WS_EPOCH : WS_END;
    int a_bw = isW ? L_BF + hw * 512 + lane * 8 : L_BF + (lane & 15) * 144 + (hw * 16 + g * 4) * 2;
    int a_br = isW ? L_BF + (lane & 7) * 64 + g * 16 : L_BF + (lane & 15) * 144 + g * 16;
    asm volatile("" : "+v"(p_p0), "+v"(p_st), "+v"(p_h), "+v"(g_p0), "+v"(g_pw), "+v"(g_o), "+v"(g_e), "+v"(a_bw), "+v"(a_br));
    const __amdgpu_buffer_rsrc_t rout = __builtin_amdgcn_make_buffer_rsrc((void*)out, 0, 8 * D * 2, RSRC3);
    lds_wait_ge(FL + FL_PRE, 4u, 1);
    STAMP(6);
    if (CODEPF && r5 >= 28) { const unsigned z = code_prefetch(hw); asm volatile("" :: "v"(z)); }
    if ((LATEALL & 1) && isW) stage_wo();
    lds_wait_ge(FL + FL_DONE, 4u, 0);
    if (hw < 2) {
      STAMP(7);
      const LAS v4f* mp = (const LAS v4f*)(L + L_MRG + hw * 1024 + lane * 16);
      const v4f o = (mp[0] * fw[0] + mp[128] * fw[1]) + (mp[256] * fw[2] + mp[384] * fw[3]);
      ST128(pack24(o, tagl), rws, p_p0, 0, AUX_SC1);
      STAMP(8);
    }
    if (isW) {
      if (LATEALL & 1) frag_wo();
      if (WDELAY) __builtin_amdgcn_s_sleep(WDELAY);
      v4u pv[4];
      gather<4, 2048, false, 0>(pv, rws, g_p0, tagl, 0u, ~0ull);
      STAMP(9);
      const v4f o = (unpack24(pv[0]) + unpack24(pv[1])) + (unpack24(pv[2]) + unpack24(pv[3]));
      *(LAS v2u*)(L + a_bw) = v2u{pk2(o.x, o.y), pk2(o.z, o.w)};
      BAR();
      v4u bf[4];
#pragma unroll
      for (int ks = 0; ks < 4; ++ks) bf[ks] = *(const LAS v4u*)(L + a_br + ks * 512);
      SB();
      v4f d0 = {0.f, 0.f, 0.f, 0.f}, d1 = {0.f, 0.f, 0.f, 0.f};
      d0 = mfma(Fr[0], bf[0], d0); d1 = mfma(Fr[1], bf[1], d1); d0 = mfma(Fr[2], bf[2], d0); d1 = mfma(Fr[3], bf[3], d1);
      ST128(pack24(d0 + d1, tagl), rws, p_st, 0, AUX_SC1);
      STAMP(10);
      if (hw == 0) {
        v4u u[2];
        if (RDELAY) __builtin_amdgcn_s_sleep(RDELAY);
        gather<2, 16 * 28 * 512, false, 0>(u, rws, g_pw, tagl, 0u, ~0ull);
        STAMP(11);
        const v4f hv = red16(unpack24(u[0]) + unpack24(u[1]));
        const unsigned h01 = pk2o(cx.x + rbf(hv.x), cx.y + rbf(hv.y)), h23 = pk2o(cx.z + rbf(hv.z), cx.w + rbf(hv.w));
        ST128((v4u{h01, h23, tagl, tagh}), rws, p_h, 0, AUX_SC1);
        STAMP(12);
        __builtin_amdgcn_s_sleep(FWAIT);
        gather<2, 16 * 28 * 512, false, 0>(u, rws, g_pw + (WS_PF - WS_PW), tagl, 0u, (1ull << 48) - 1ull);
        STAMP(13);
        const v4f ov = red16(unpack24(u[0]) + unpack24(u[1]));
        const unsigned o01 = pk2(bflo(h01) + rbf(rbf(ov.x) + cb.x), bfhi(h01) + rbf(rbf(ov.y) + cb.y));
        const unsigned o23 = pk2(bflo(h23) + rbf(rbf(ov.z) + cb.z), bfhi(h23) + rbf(rbf(ov.w) + cb.w));
        __builtin_amdgcn_raw_buffer_store_b64(v2u{o01, o23}, rout, g_o, 0, 0);
        __builtin_amdgcn_raw_buffer_store_b64(v2u{tagl, tagh}, rws, g_e, 0, AUX_SC1);
        STAMP(14);
      }
    } else if (isM) {
      if (LATEALL & 2) stage_w1();
      if (LATEW2) stage_w2();
      STAMP(19);
      mlp_ln(L, rws, wv, lane, tagl, tagh);
      STAMP(15);
      v4u bf[14];
#pragma unroll
      for (int ks = 0; ks < 14; ++ks) bf[ks] = *(const LAS v4u*)(bp + ks * 64);
      SB();
      v4f d0 = {0.f, 0.f, 0.f, 0.f}, d1 = {0.f, 0.f, 0.f, 0.f};
#pragma unroll
      for (int ks = 0; ks < 14; ks += 2) { d0 = mfma(Fr[ks], bf[ks], d0); d1 = mfma(Fr[ks + 1], bf[ks + 1], d1); }
      d0 += d1;
      float u[4];
#pragma unroll
      for (int k = 0; k < 4; ++k) { float a = rbf(rbf(d0[k]) + cb[k]); a = a > 0.f ? a : 0.f; u[k] = a * a; }
      *(LAS v2u*)(L + a_bw) = v2u{pk2(u[0], u[1]), pk2(u[2], u[3])};
      BAR();
      STAMP(18);
      const v4u h0 = *(const LAS v4u*)(L + a_br), h1 = *(const LAS v4u*)(L + a_br + 64);
      const bool up = lane & 8;
#pragma unroll
      for (int i = 0; i < 4; ++i) {
        v4f ea = mfma(Fr[14 + 4 * i], h0, v4f{0.f, 0.f, 0.f, 0.f}), eb = ea;
        if (i < 3) eb = mfma(Fr[14 + 4 * i + 2], h0, v4f{0.f, 0.f, 0.f, 0.f});
        ea = mfma(Fr[14 + 4 * i + 1], h1, ea);
        if (i < 3) eb = mfma(Fr[14 + 4 * i + 3], h1, eb);
        const v4u ev = pack24(v4f{up ? eb.x : ea.x, up ? eb.y : ea.y, up ? eb.z : ea.z, up ? eb.w : ea.w}, tagl);
        ST128(ev, rws, (i == 3 && up) ? WS_END : p_st + i * 1024, 0, AUX_SC1);
      }
      STAMP(16);
    }
  } else {
    const size_t kvoff = ((size_t)grp * SK + (size_t)s * (ILV ? 32 : 1024)) * DH;
    constexpr int TSTR = ILV ? 65536 : 16384, NREC = ILV ? 31 * 65536 + 16384 : 524288;
    const __amdgpu_buffer_rsrc_t rk = __builtin_amdgcn_make_buffer_rsrc((void*)(kc + kvoff), 0, NREC, RSRC3);
    const int voff = lane * 16 + wv * 4096;
    v4u X[NEXTRA * 4][4], R[4][4];
    const unsigned tb = (unsigned)__builtin_amdgcn_s_memrealtime() + (unsigned)(T1) - (unsigned)(STAG == 1 ? ((wv * 4 + (r5 & 3)) * TAU) >> 8 : STAG == 2 ? ((r5 & 15) * TAU) >> 8 : 0);
#define LANDED() asm volatile("s_waitcnt vmcnt(%0)" :: "i"(4 * (LEAD - 1)) : "memory")
    STAMP(0);
    BAR();
#pragma unroll
    for (int n = 0; n < NEXTRA * 4 + LEAD; ++n) {
      if (n >= LEAD) LANDED();
      if (n >= LEAD) pace(tb, n);
      issue_turn(n < NEXTRA * 4 ? X[n] : R[n - NEXTRA * 4], rk, voff, n * TSTR);
    }
    const bool b0 = lane & 1, b1_ = lane & 2, b2_ = lane & 4, b3 = lane & 8;
    const int hf = lane >> 5, c = lane & 31;
    LAS float* P = (LAS float*)(L + L_P) + wv * 512;
    LAS float* Pl = P + ((lane >> 3) & 3) * 16 + hf * 8 + (lane & 7);
    asm volatile("" :: "v"(cpf));
    STAMP(1);
    lds_wait_ge(FL + FL_Q, 4u, 0);
    STAMP(2);
    const v4u q0 = *(const LAS v4u*)(L + L_Q + c * 16), q1 = *(const LAS v4u*)(L + L_Q + 512 + c * 16);
    float mx = -INFINITY;
#pragma unroll
    for (int g = 0; g < NEXTRA; ++g) {
      float r[4], rx[4];
#pragma unroll
      for (int d = 0; d < 4; ++d) {
        rx[d] = k_turn(X[4 * g + d], q0, q1, b0, b1_, b2_);
        SB();
        LANDED();
        pace(tb, (NEXTRA + g) * 4 + d + LEAD);
        issue_turn(R[(d + LEAD) & 3], rk, voff, ((NEXTRA + g) * 4 + d + LEAD) * TSTR);
        r[d] = k_turn(R[d], q0, q1, b0, b1_, b2_);
        SB();
      }
      const float sx = k_group(rx[0], rx[1], rx[2], rx[3], b3), sc = k_group(r[0], r[1], r[2], r[3], b3);
      mx = fmaxf(mx, fmaxf(sx, sc));
      Pl[g * 64] = sx; Pl[(NEXTRA + g) * 64] = sc;
      SB();
    }
    STAMP(3);
    const __amdgpu_buffer_rsrc_t rv = __builtin_amdgcn_make_buffer_rsrc((void*)(vc + kvoff), 0, NREC, RSRC3);
#pragma unroll 1
    for (int g = 2 * NEXTRA; g < 8; ++g) {
      float r[4];
#pragma unroll
      for (int d = 0; d < 4; ++d) {
        const int nx = g * 4 + d + LEAD;
        const bool tov = (d + LEAD >= 4) && g == 7;
        const __amdgpu_buffer_rsrc_t rn = __builtin_amdgcn_make_buffer_rsrc((void*)((tov ? vc : kc) + kvoff), 0, NREC, RSRC3);
        LANDED();
        pace(tb, nx);
        issue_turn(R[(d + LEAD) & 3], rn, voff, (nx & 31) * TSTR);
        r[d] = k_turn(R[d], q0, q1, b0, b1_, b2_);
        SB();
      }
      const float sc = k_group(r[0], r[1], r[2], r[3], b3);
      mx = fmaxf(mx, sc);
      Pl[g * 64] = sc;
    }
    STAMP(4);
    mx = fmaxf(mx, dpp<XOR2>(mx)); mx = fmaxf(mx, dpp<ROR4>(mx)); mx = fmaxf(mx, dpp<ROR8>(mx));
    mx = sw16_max(mx); mx = sw32_max(mx);
    const float msub = mx == -INFINITY ? 0.f : mx;
    float ls = 0.f;
    {
      float sv[8];
#pragma unroll
      for (int g = 0; g < 8; ++g) sv[g] = Pl[g * 64];
#pragma unroll
      for (int g = 0; g < 8; ++g) { const float p = __builtin_amdgcn_exp2f((sv[g] - msub) * SC2); ls += p; Pl[g * 64] = p; }
    }
    ls += dpp<XOR2>(ls); ls += dpp<ROR8>(ls); ls += dpp<ROR4>(ls);
    ls = sw16_add(ls, ls); ls = sw32_add(ls, ls);
    *(LAS v2f*)(L + (lane < 2 ? L_ML + (wv * 2 + lane) * 8 : L_SINK + 1024 + lane * 8)) = v2f{mx, ls};
    lds_signal(L, FL_ML, lane, 1u);
    v2f a0[4], a1[4];
#pragma unroll
    for (int k = 0; k < 4; ++k) { a0[k] = v2f{0.f, 0.f}; a1[k] = v2f{0.f, 0.f}; }
    const LAS v4f* Pr = (const LAS v4f*)(P + hf * 8);
    LAS v4f* mo = (LAS v4f*)(L + L_MRG + wv * 2048 + lane * 32);
#pragma unroll 1
    for (int g = 0; g < 8; ++g) {
      v4f pa[4], pb[4];
#pragma unroll
      for (int d = 0; d < 4; ++d) { pa[d] = Pr[(g * 4 + d) * 4]; pb[d] = Pr[(g * 4 + d) * 4 + 1]; }
#pragma unroll
      for (int d = 0; d < 4; ++d) {
        LANDED();
        pace(tb, 32 + g * 4 + d + LEAD);
        issue_turn(R[(d + LEAD) & 3], rv, voff, (g * 4 + d + LEAD) * TSTR);
        v_turn(R[d], pa[d], pb[d], a0, a1);
        SB();
        if (d == PRED) lds_signal(L, FL_PRE, lane, g == PREG ? 1u : 0u);
        if (d == 3) {
          mo[0] = v4f{sw32_add(a0[0].x, a1[0].x), sw32_add(a0[0].y, a1[0].y), sw32_add(a0[1].x, a1[1].x), sw32_add(a0[1].y, a1[1].y)};
          mo[1] = v4f{sw32_add(a0[2].x, a1[2].x), sw32_add(a0[2].y, a1[2].y), sw32_add(a0[3].x, a1[3].x), sw32_add(a0[3].y, a1[3].y)};
          lds_signal(L, FL_DONE, lane, g == 7 ? 1u : 0u);
          SB();
        }
      }
    }
    STAMP(5);
    if (isM) mlp_ln(L, rws, wv, lane, __builtin_amdgcn_readfirstlane(FL[FL_TAGL]), __builtin_amdgcn_readfirstlane(FL[FL_TAGH]));
  }
}
}

extern "C" void kernel_launch(void* const* d_in, const int* in_sizes, int n_in, void* d_out, int out_size, void* d_ws,
                              size_t ws_size, hipStream_t stream) {
  using namespace fk;
  (void)hipFuncSetAttribute((const void*)fused_decode, hipFuncAttributeMaxDynamicSharedMemorySize, L_END + DBG_STAMPS * 2048);
  hipLaunchKernelGGL(fused_decode, dim3(256), dim3(512), L_END + DBG_STAMPS * 2048, stream, (const u16*)d_in[0], (const u16*)d_in[1],
                     (const u16*)d_in[2], (const u16*)d_in[3], (const u16*)d_in[4], (const u16*)d_in[6], (const u16*)d_in[7],
                     (const u16*)d_in[8], (const u16*)d_in[9], (u16*)d_out, (unsigned char*)d_ws);
}
```

```cpp
#define WT 190
#define G1 90
#define G2 84
#define G3 165
#include <hip/hip_runtime.h>
#include <hip/hip_bf16.h>
#include <stdint.h>

#define DBG_STAMPS 0
#define NEXTRA 3
#define LEAD 2
#define TAU 904
#define T1 0
#define TAU0 1000
#define TJ 0
#define PN0 16
#define PN1 64
#define LATEALL 0
#define PRED 1
#define STAG 0
#define ILV 1
#define KW 1
#define KR 1
#define KM 1
#define GAPW 4
#define GAPR 4
#define GAPM 4
#define MT (RT + G2)
#define SLPM 0
#define SLPW 0
#define SLPR 0
#define MLW 3
#define EARLY 1
#define SFIRST 1
#define GBASE 1
#define GOFF 20
#define RT (WT + G1)
#define FT (MT + G3)
#define W1B 1
#define W1S 16
#define WOT 900
#define WOS 40
#define AXW 16
#define AXR 16
#define AXM 16
#define QT 500
#define QSLEEP 24
#define LATEW2 1
#define CODEPF 1
#define CODE_BYTES 33792
#define PRETOUCH 0
#define WDELAY 6
#define RDELAY 8
#define MDELAY 0
#define FWAIT 64
#define PREG 6

static_assert(PN0 <= 8 * NEXTRA, "the loops use the second slope only");
namespace fk {
typedef unsigned short u16;
typedef unsigned long long u64;
typedef unsigned v4u __attribute__((ext_vector_type(4)));
typedef unsigned v2u __attribute__((ext_vector_type(2)));
typedef float v4f __attribute__((ext_vector_type(4)));
typedef float v2f __attribute__((ext_vector_type(2)));
typedef __bf16 bf16x2 __attribute__((ext_vector_type(2)));
typedef __bf16 bf16x8 __attribute__((ext_vector_type(8)));
typedef short s16x4 __attribute__((ext_vector_type(4)));
#define LAS __attribute__((address_space(3)))
#define GAS __attribute__((address_space(1)))

constexpr int D = 448, F = 1792, SK = 4096, DH = 256, NQ = 4096;
constexpr int AUX_NT = 2, AUX_SC1 = 16;
constexpr int RSRC3 = 0x00020000;
constexpr float SC2 = 0.0625f * 1.4426950408889634f;
struct Spin {
  unsigned n = 0, t0 = 0;
  __device__ __forceinline__ bool expired() {
    if ((++n & 4095u) != 0u) return false;
    const unsigned t = (unsigned)__builtin_amdgcn_s_memrealtime();
    if (n == 4096u) t0 = t;
    return t - t0 > (1u << 27);
  }
};

constexpr int WS_EPOCH = 0;
constexpr int WS_T = 2048;
constexpr int WS_G = 4096;
constexpr int WS_ML = WS_G + 8 * 8 * 4 * 128 * 16;
constexpr int WS_H = WS_ML + 64 * 8 * 16;
constexpr int WS_P0 = WS_H + 16384;
constexpr int WS_PW = WS_P0 + 64 * 4 * 2048;
constexpr int WS_PF = WS_PW + 32 * 28 * 512;
constexpr int WS_END = WS_PF + 28 * 28 * 512;
constexpr int WS_DBG = WS_END;

constexpr int LN_STR = 912;
constexpr int L_P = 0;
constexpr int L_MRG = 8192;
constexpr int L_Q = 16384;
constexpr int L_FLAG = 17408;
constexpr int L_ML = L_FLAG + 64;
constexpr int L_LN = L_ML + 64;
constexpr int L_BF = L_LN + 8 * LN_STR;
constexpr int L_SINK = L_BF + 4096;
constexpr int L_RAW = L_SINK + 2048;
constexpr int L_END = 96 * 1024;
static_assert(L_RAW + 57344 <= L_END, "lds");
static_assert(L_LN % 16 == 0 && L_BF % 16 == 0 && L_RAW % 16 == 0, "align");
enum { FL_LN = 0, FL_QP = 1, FL_Q = 2, FL_PRE = 3, FL_DONE = 4, FL_RAW = 5, FL_RAW2 = 6, FL_RAW3 = 7, FL_ML = 9, FL_TAGL = 10, FL_TAGH = 11, FL_TG = 12 };

__device__ __forceinline__ float bflo(unsigned v) { return __builtin_bit_cast(float, v << 16); }
__device__ __forceinline__ float bfhi(unsigned v) { return __builtin_bit_cast(float, v & 0xffff0000u); }
__device__ __forceinline__ float rbf(float f) { return (float)(__bf16)f; }
__device__ __forceinline__ unsigned pk2(float lo, float hi) {
  bf16x2 v = {(__bf16)lo, (__bf16)hi};
  return __builtin_bit_cast(unsigned, v);
}
__device__ __forceinline__ unsigned pk2o(float lo, float hi) {
  unsigned p = pk2(lo, hi);
  asm("" : "+v"(p));
  return p;
}
__device__ __forceinline__ float dot2(unsigned a, unsigned b, float c) {
  return __builtin_amdgcn_fdot2_f32_bf16(__builtin_bit_cast(bf16x2, a), __builtin_bit_cast(bf16x2, b), c, false);
}
__device__ __forceinline__ v4f mfma(v4u a, v4u b, v4f c) {
  return __builtin_amdgcn_mfma_f32_16x16x32_bf16(__builtin_bit_cast(bf16x8, a), __builtin_bit_cast(bf16x8, b), c, 0, 0, 0);
}
template <int CTRL> __device__ __forceinline__ float dpp(float x) {
  return __builtin_bit_cast(float, __builtin_amdgcn_mov_dpp(__builtin_bit_cast(int, x), CTRL, 0xf, 0xf, true));
}
constexpr int XOR1 = 0xB1, XOR2 = 0x4E, HMIR = 0x141, ROR4 = 0x124, ROR8 = 0x128;
__device__ __forceinline__ float sw16_add(float a, float b) {
  auto s = __builtin_amdgcn_permlane16_swap(__float_as_uint(a), __float_as_uint(b), false, false);
  return __uint_as_float(s[0]) + __uint_as_float(s[1]);
}
__device__ __forceinline__ float sw32_add(float a, float b) {
  auto s = __builtin_amdgcn_permlane32_swap(__float_as_uint(a), __float_as_uint(b), false, false);
  return __uint_as_float(s[0]) + __uint_as_float(s[1]);
}
__device__ __forceinline__ float sw16_max(float a) {
  auto s = __builtin_amdgcn_permlane16_swap(__float_as_uint(a), __float_as_uint(a), false, false);
  return fmaxf(__uint_as_float(s[0]), __uint_as_float(s[1]));
}
__device__ __forceinline__ float sw32_max(float a) {
  auto s = __builtin_amdgcn_permlane32_swap(__float_as_uint(a), __float_as_uint(a), false, false);
  return fmaxf(__uint_as_float(s[0]), __uint_as_float(s[1]));
}
__device__ __forceinline__ v4u pack24(v4f v, unsigned tag) {
  const unsigned a = __float_as_uint(v.x), b = __float_as_uint(v.y), c = __float_as_uint(v.z), d = __float_as_uint(v.w);
  return v4u{__builtin_amdgcn_perm(b, a, 0x05030201u), __builtin_amdgcn_perm(c, b, 0x06050302u), __builtin_amdgcn_perm(d, c, 0x07060503u), tag};
}
__device__ __forceinline__ v4f unpack24(v4u g) {
  return v4f{__uint_as_float(g.x << 8), __uint_as_float(__builtin_amdgcn_perm(g.y, g.x, 0x0504030Cu)),
             __uint_as_float(__builtin_amdgcn_perm(g.z, g.y, 0x0403020Cu)), __uint_as_float(g.z & 0xFFFFFF00u)};
}
__device__ __forceinline__ v4u pack28(v4f v, unsigned tag) {
  return v4u{(__float_as_uint(v.x) & ~3u) | (tag & 3u), (__float_as_uint(v.y) & ~3u) | ((tag >> 2) & 3u),
             (__float_as_uint(v.z) & ~3u) | ((tag >> 4) & 3u), (__float_as_uint(v.w) & ~3u) | ((tag >> 6) & 3u)};
}
__device__ __forceinline__ v4f unpack28(v4u g) { return __builtin_bit_cast(v4f, g & ~3u); }
__device__ __forceinline__ bool bad28(v4u g, unsigned tag) {
  return (((g.x ^ tag) | (g.y ^ (tag >> 2)) | (g.z ^ (tag >> 4)) | (g.w ^ (tag >> 6))) & 3u) != 0u;
}
#define LGKM0() asm volatile("s_waitcnt lgkmcnt(0)" ::: "memory")
#define CLOB() asm volatile("" ::: "memory")
#define SB() __builtin_amdgcn_sched_barrier(0)
#define BAR() do { asm volatile("s_waitcnt lgkmcnt(0)" ::: "memory"); __builtin_amdgcn_s_barrier(); asm volatile("" ::: "memory"); __builtin_amdgcn_sched_barrier(0); } while (0)
#define LD128(rs, vo, so, aux) __builtin_amdgcn_raw_buffer_load_b128(rs, vo, so, aux)
#define ST128(v, rs, vo, so, aux) __builtin_amdgcn_raw_buffer_store_b128(v, rs, vo, so, aux)

#define STAMP(i) do {} while (0)

__device__ __forceinline__ void lds_wait_ge(volatile LAS unsigned* f, unsigned want, int slp) {
  Spin sp;
#pragma nounroll
  while (*f < want) {
    if (slp) __builtin_amdgcn_s_sleep(8);
    if (sp.expired()) break;
  }
  CLOB();
}
__device__ __forceinline__ void lds_signal(LAS unsigned char* L, int word, int lane, unsigned inc) {
  LGKM0();
  LAS unsigned* p = (LAS unsigned*)(L + (lane == 0 ? L_FLAG + word * 4 : L_SINK + lane * 4));
  __hip_atomic_fetch_add(p, inc, __ATOMIC_RELAXED, __HIP_MEMORY_SCOPE_WORKGROUP);
}
template <int N, int STR, int WIDE, int SLP, int AUX = AUX_SC1>
__device__ __forceinline__ void gather(v4u (&v)[N], __amdgpu_buffer_rsrc_t rs, int off, unsigned tl, unsigned th, u64 last_mask) {
  Spin sp;
#pragma nounroll
  for (;;) {
    CLOB();
#pragma unroll
    for (int i = 0; i < N; ++i) v[i] = LD128(rs, off + i * STR, 0, AUX);
    u64 any = 0ull;
#pragma unroll
    for (int i = 0; i < N; ++i) {
      const u64 bad = __builtin_amdgcn_ballot_w64(WIDE == 2 ? bad28(v[i], tl) : WIDE ? ((v[i].z ^ tl) | (v[i].w ^ th)) != 0u : v[i].w != tl);
      any |= i == N - 1 ? bad & last_mask : bad;
    }
    if (any == 0ull || sp.expired()) break;
    if (SLP) __builtin_amdgcn_s_sleep(SLP);
  }
}
template <int N> struct Poll3 {
  v4u a[3][N];
  __device__ __forceinline__ void load(int k, v4u rs, const int (&off)[N]) {
#pragma unroll
    for (int i = 0; i < N; ++i) asm volatile("buffer_load_dwordx4 %0, %1, %2, 0 offen sc1" : "+v"(a[k][i]) : "v"(off[i]), "s"(rs) : "memory");
  }
  __device__ __forceinline__ bool good(int k, unsigned tl, u64 last_mask) {
    if constexpr (N == 2) asm volatile("s_waitcnt vmcnt(4)" : "+v"(a[k][0]), "+v"(a[k][1]));
    else asm volatile("s_waitcnt vmcnt(8)" : "+v"(a[k][0]), "+v"(a[k][1]), "+v"(a[k][N - 2]), "+v"(a[k][N - 1]));
    u64 any = 0ull;
#pragma unroll
    for (int i = 0; i < N; ++i) {
      const u64 bad = __builtin_amdgcn_ballot_w64(a[k][i].w != tl);
      any |= i == N - 1 ? bad & last_mask : bad;
    }
    return any == 0ull;
  }
  template <int GAP> __device__ __forceinline__ void run(v4u (&v)[N], v4u rs, const int (&off)[N], unsigned tl, u64 last_mask) {
#pragma unroll
    for (int k = 0; k < 3; ++k)
#pragma unroll
      for (int i = 0; i < N; ++i) asm volatile("" : "=v"(a[k][i]));
    load(0, rs, off); __builtin_amdgcn_s_sleep(GAP); load(1, rs, off); __builtin_amdgcn_s_sleep(GAP); load(2, rs, off);
    Spin sp;
    unsigned m0 = 0u, m1 = 0u;
#pragma nounroll
    for (;;) {
      if (good(0, tl, last_mask)) { m0 = ~0u; break; }
      load(0, rs, off);
      if (good(1, tl, last_mask)) { m1 = ~0u; break; }
      load(1, rs, off);
      if (good(2, tl, last_mask) || sp.expired()) break;
      load(2, rs, off);
    }
    asm volatile("" : "+s"(m0), "+s"(m1));
#pragma unroll
    for (int i = 0; i < N; ++i)
#pragma unroll
      for (int c = 0; c < 3; ++c) { const unsigned t = (a[1][i][c] & m1) | (a[2][i][c] & ~m1); v[i][c] = (a[0][i][c] & m0) | (t & ~m0); }
  }
  __device__ __forceinline__ void keep() {
#pragma unroll
    for (int k = 0; k < 3; ++k)
#pragma unroll
      for (int i = 0; i < N; ++i) asm volatile("" :: "v"(a[k][i]));
    if (DBG_STAMPS) asm volatile("s_waitcnt vmcnt(0)" ::: "memory");
  }
  __device__ __forceinline__ void drain() {
#pragma unroll
    for (int k = 0; k < 3; ++k)
#pragma unroll
      for (int i = 0; i < N; ++i) asm volatile("s_waitcnt vmcnt(0)" : "+v"(a[k][i]) :: "memory");
  }
};
__device__ __forceinline__ void wait_until(unsigned until) {
  asm volatile("8:\n\ts_memrealtime vcc\n\ts_waitcnt lgkmcnt(0)\n\ts_sub_i32 vcc_lo, vcc_lo, %0\n\ts_cmp_lt_i32 vcc_lo, 0\n\ts_cbranch_scc1 8b\n" :: "s"(until) : "scc", "vcc", "memory");
}
constexpr int SCHED63 = (PN0 * TAU0 + (63 - PN0) * TAU) >> 4;
__device__ __forceinline__ void pace(unsigned tb, int n) {
  const unsigned until = tb + (unsigned)((n >= PN1 ? -65536 : n < PN0 ? n * TAU0 : PN0 * TAU0 + TJ * 16 + (n - PN0) * TAU) >> 4);
  asm volatile("8:\n\ts_memrealtime vcc\n\ts_waitcnt lgkmcnt(0)\n\ts_sub_i32 vcc_lo, vcc_lo, %0\n\ts_cmp_lt_i32 vcc_lo, 0\n\ts_cbranch_scc1 8b\n" :: "s"(until) : "scc", "vcc", "memory");
}
__device__ __forceinline__ void pace2(unsigned tb, int n) { wait_until(tb + (unsigned)((PN0 * TAU0 + (n - PN0) * TAU) >> 4)); }
template <bool B> struct BoolC { static constexpr bool value = B; };
__device__ __forceinline__ void issue_turn(v4u* r, __amdgpu_buffer_rsrc_t rs, int voff, int soff) {
#pragma unroll
  for (int i = 0; i < 4; ++i) r[i] = LD128(rs, voff + i * 1024, soff, AUX_NT);
  SB();
}
__device__ __forceinline__ float k_turn(const v4u* k, v4u q0, v4u q1, bool b0, bool b1, bool b2) {
  float x[8];
#pragma unroll
  for (int i = 0; i < 4; ++i) {
    float a0 = dot2(k[i].x, q0.x, 0.f), a1 = dot2(k[i].x, q1.x, 0.f);
    a0 = dot2(k[i].y, q0.y, a0); a1 = dot2(k[i].y, q1.y, a1);
    a0 = dot2(k[i].z, q0.z, a0); a1 = dot2(k[i].z, q1.z, a1);
    a0 = dot2(k[i].w, q0.w, a0); a1 = dot2(k[i].w, q1.w, a1);
    x[2 * i] = a0; x[2 * i + 1] = a1;
  }
  float y[4], z[2];
#pragma unroll
  for (int v = 0; v < 4; ++v) y[v] = (b2 ? x[v + 4] : x[v]) + dpp<HMIR>(b2 ? x[v] : x[v + 4]);
#pragma unroll
  for (int v = 0; v < 2; ++v) z[v] = (b1 ? y[v + 2] : y[v]) + dpp<XOR2>(b1 ? y[v] : y[v + 2]);
  return (b0 ? z[1] : z[0]) + dpp<XOR1>(b0 ? z[0] : z[1]);
}
__device__ __forceinline__ float k_group(float r0, float r1, float r2, float r3, bool b3) {
  const float u0 = (b3 ? r1 : r0) + dpp<ROR8>(b3 ? r0 : r1);
  const float u1 = (b3 ? r3 : r2) + dpp<ROR8>(b3 ? r2 : r3);
  return rbf(sw16_add(u0, u1));
}
__device__ __forceinline__ void v_turn(const v4u* v, v4f pa, v4f pb, v2f (&a0)[4], v2f (&a1)[4]) {
#pragma unroll
  for (int i = 0; i < 4; ++i) {
    const float p0 = i == 0 ? pa.x : i == 1 ? pa.z : i == 2 ? pb.x : pb.z;
    const float p1 = i == 0 ? pa.y : i == 1 ? pa.w : i == 2 ? pb.y : pb.w;
#pragma unroll
    for (int j = 0; j < 4; ++j) {
      const v2f e = {bflo(v[i][j]), bfhi(v[i][j])};
      a0[j] = __builtin_elementwise_fma(e, v2f{p0, p0}, a0[j]);
      a1[j] = __builtin_elementwise_fma(e, v2f{p1, p1}, a1[j]);
    }
  }
}
template <bool FULL> __device__ __forceinline__ float row_sum(float v) {
  v += dpp<XOR1>(v); v += dpp<XOR2>(v); v += dpp<HMIR>(v); v += dpp<ROR8>(v);
  v = sw16_add(v, v);
  return FULL ? sw32_add(v, v) : v;
}
template <int NW> __device__ __forceinline__ void ln_row(unsigned (&x)[NW], bool act) {
  v2f v[NW];
#pragma unroll
  for (int k = 0; k < NW; ++k) v[k] = v2f{bflo(x[k]), bfhi(x[k])};
  float mean, var;
  if constexpr (NW == 4) {
    const float c = bflo(__builtin_amdgcn_readfirstlane(x[0]));
    v2f s1 = {0.f, 0.f}, s2 = {0.f, 0.f};
#pragma unroll
    for (int k = 0; k < NW; ++k) { const v2f d = v[k] - c; s1 += d; s2 = __builtin_elementwise_fma(d, d, s2); }
    float t = sw32_add(act ? s1.x + s1.y : 0.f, act ? s2.x + s2.y : 0.f);
    t = row_sum<false>(t) * (1.0f / D);
    const float m1 = __builtin_bit_cast(float, __builtin_amdgcn_readlane(__builtin_bit_cast(int, t), 0));
    const float m2 = __builtin_bit_cast(float, __builtin_amdgcn_readlane(__builtin_bit_cast(int, t), 32));
    mean = c + m1;
    var = fmaxf(fmaf(-m1, m1, m2), m2 - m2);
  } else {
    constexpr unsigned ONES = 0x3F803F80u;
    float sa = 0.f, sb = 0.f;
#pragma unroll
    for (int k = 0; k < NW; k += 2) { sa = dot2(x[k], ONES, sa); sb = dot2(x[k + 1], ONES, sb); }
    mean = row_sum<false>(sa + sb) * (1.0f / D);
    v2f q0 = {0.f, 0.f}, q1 = {0.f, 0.f};
#pragma unroll
    for (int k = 0; k < NW; k += 2) {
      const v2f d0 = v[k] - mean, d1 = v[k + 1] - mean;
      q0 = __builtin_elementwise_fma(d0, d0, q0); q1 = __builtin_elementwise_fma(d1, d1, q1);
    }
    q0 += q1;
    var = row_sum<false>(act ? q0.x + q0.y : 0.f) * (1.0f / D);
  }
  const float mb = rbf(mean);
#pragma unroll
  for (int k = 0; k < NW; ++k) { const v2f d = v[k] - mb; const unsigned p = pk2o(d.x, d.y); v[k] = v2f{bflo(p), bfhi(p)}; }
  const float den = rbf(__builtin_amdgcn_sqrtf(rbf(rbf(var) + 1e-5f)));
  const float ri = __builtin_amdgcn_rcpf(den);
#pragma unroll
  for (int k = 0; k < NW; ++k) { const v2f a = v[k] * ri; x[k] = pk2(a.x, a.y); }
}
__device__ __forceinline__ v4u frag_tr(const LAS unsigned char* p, int hi) {
#if defined(__HIP_DEVICE_COMPILE__)
  const v2u a = __builtin_bit_cast(v2u, __builtin_amdgcn_ds_read_tr16_b64_v4i16((LAS s16x4*)p));
  const v2u b = __builtin_bit_cast(v2u, __builtin_amdgcn_ds_read_tr16_b64_v4i16((LAS s16x4*)(p + hi)));
  return v4u{a.x, a.y, b.x, b.y};
#else
  return v4u{0u, 0u, 0u, 0u};
#endif
}
__device__ __forceinline__ v4f red16(v4f t) {
#pragma unroll
  for (int k = 0; k < 4; ++k) { float a = t[k]; a += dpp<ROR8>(a); a += dpp<ROR4>(a); a = sw16_add(a, a); a = sw32_add(a, a); t[k] = a; }
  return t;
}
__device__ __forceinline__ void mlp_ln(LAS unsigned char* L, __amdgpu_buffer_rsrc_t rws, v4u rwv, Poll3<2>& pl, int row, int lane, unsigned tl, unsigned t0) {
  const bool act = lane < 56;
  const int a = act ? L_LN + row * LN_STR + lane * 8 : L_SINK + lane * 8;
  v4u hg[2];
  if (MT) wait_until(t0 + SCHED63 + MT);
  const int o0 = WS_H + (row * 112 + (act ? lane : 55)) * 16;
  if (KM > 1) { const int of[2] = {o0, o0 + 896}; pl.run<GAPM>(hg, rwv, of, tl, ~0ull); }
  else gather<2, 896, false, SLPM, AXM>(hg, rws, o0, tl, 0u, ~0ull);
  { const int wv = row; (void)wv; STAMP(17); }
  unsigned x[4] = {hg[0].x, hg[0].y, hg[1].x, hg[1].y};
  ln_row<4>(x, act);
  *(LAS v2u*)(L + a) = v2u{x[0], x[1]}; *(LAS v2u*)(L + a + 448) = v2u{x[2], x[3]};
  BAR();
}

__global__ void __launch_bounds__(512, 2)
fused_decode(const u16* __restrict__ x, const u16* __restrict__ kc, const u16* __restrict__ vc, const u16* __restrict__ wq,
             const u16* __restrict__ wo, const u16* __restrict__ w1, const u16* __restrict__ b1, const u16* __restrict__ w2,
             const u16* __restrict__ b2, u16* __restrict__ out, unsigned char* __restrict__ ws) {
  extern __shared__ __attribute__((aligned(16))) unsigned char smem[];
  LAS unsigned char* L = (LAS unsigned char*)smem;
  const u64 pc0 = __builtin_amdgcn_s_getpc() & ~0xFFull;
  const int w = blockIdx.x, tid = threadIdx.x, lane = tid & 63, wv = __builtin_amdgcn_readfirstlane(tid >> 6);
  const int kvh = w & 7, r5 = w >> 3, b = r5 & 7, s = r5 >> 3, grp = b * 8 + kvh;
  const int m = (r5 - 28) * 8 + kvh;
  const bool isM = r5 >= 28 && m < 28;
  volatile LAS unsigned* FL = (volatile LAS unsigned*)(L + L_FLAG);
  const __amdgpu_buffer_rsrc_t rws = __builtin_amdgcn_make_buffer_rsrc((void*)ws, 0, WS_END, RSRC3);
  const v4u rwv = {(unsigned)(u64)ws, (unsigned)((u64)ws >> 32) & 0xffffu, (unsigned)WS_END, (unsigned)RSRC3};
  auto code_prefetch = [&](int part) {
    const __amdgpu_buffer_rsrc_t rc = __builtin_amdgcn_make_buffer_rsrc((void*)pc0, 0, CODE_BYTES, RSRC3);
    return __builtin_amdgcn_raw_buffer_load_b32(rc, lane * 128, part * 8192, 0);
  };
  unsigned cpf = 0;
  if (CODEPF && (r5 >= 28 || r5 < 4)) cpf = code_prefetch((wv + 4) & 7);
  SB();
  asm volatile("" :: "s"(x), "s"(kc), "s"(vc), "s"(wq), "s"(wo), "s"(w1), "s"(b1), "s"(w2), "s"(b2), "s"(out), "s"(ws));

  if (__builtin_expect(wv >= 4, !SFIRST)) {
    const int hw = wv - 4;
    STAMP(0);
    __builtin_amdgcn_s_setprio(3);
    const unsigned tstart = (unsigned)__builtin_amdgcn_s_memrealtime();
    const bool isW = r5 < 28;
    const int t = r5 >> 2, kq = r5 & 3;
    const int g = lane >> 4;
    const LAS unsigned char* ap = L + L_RAW + (g * 8 + ((lane & 15) >> 2)) * 128 + hw * 32 + (lane & 3) * 8;
    const LAS unsigned char* bp = L + L_LN + (lane & 7) * LN_STR + g * 16;
    const __amdgpu_buffer_rsrc_t rx = __builtin_amdgcn_make_buffer_rsrc((void*)x, 0, 8 * D * 2, RSRC3);
    const v2u ep = __builtin_amdgcn_raw_buffer_load_b64(rws, WS_EPOCH, 0, AUX_SC1);
    unsigned tagl, tagh;
#define MAKE_TAGS() do { const unsigned el = __builtin_amdgcn_readfirstlane(ep.x), eh = __builtin_amdgcn_readfirstlane(ep.y); \
      u64 z = (((u64)eh << 32) | el) + 0x9E3779B97F4A7C15ull; \
      z = (z ^ (z >> 30)) * 0xBF58476D1CE4E5B9ull; z = (z ^ (z >> 27)) * 0x94D049BB133111EBull; z ^= z >> 31; \
      tagl = (((unsigned)z ^ el) & 3u) == 0u ? (unsigned)z ^ 1u : (unsigned)z; tagh = (unsigned)(z >> 32); } while (0)
    if (tid < 256 + 16) FL[tid - 256] = 0u;
    {
      const int row = hw * 2 + (lane >> 5), c = lane & 31;
      const int ks0 = s < 3 ? s * 4 : 11, nks = s < 2 ? 4 : 3;
      const int xo = c < 28 ? row * (D * 2) + c * 32 : 8 * D * 2;
      const v4u xa = LD128(rx, xo, 0, AUX_NT), xb = LD128(rx, xo + 16, 0, AUX_NT);
      const __amdgpu_buffer_rsrc_t rq = __builtin_amdgcn_make_buffer_rsrc((void*)(wq + ks0 * 32 * NQ + (kvh * 8 + b) * 64), 0, (nks * 32 - 1) * (NQ * 2) + 128, RSRC3);
      v4u tt[4];
#pragma unroll
      for (int i = 0; i < 4; ++i) tt[i] = LD128(rq, (lane >> 3) * (NQ * 2) + (lane & 7) * 16, (i * 4 + hw) * 8 * (NQ * 2), AUX_NT);
      SB();
      BAR();
      {
        unsigned xr[8] = {xa.x, xa.y, xa.z, xa.w, xb.x, xb.y, xb.z, xb.w};
        ln_row<8>(xr, c < 28);
        const int a = c < 28 ? L_LN + row * LN_STR + c * 32 : L_SINK + lane * 32;
        *(LAS v4u*)(L + a) = v4u{xr[0], xr[1], xr[2], xr[3]}; *(LAS v4u*)(L + a + 16) = v4u{xr[4], xr[5], xr[6], xr[7]};
      }
      MAKE_TAGS();
      if (GBASE) ST128((v4u{*(volatile LAS unsigned*)(L + L_ML), 0u, tagl, tagh}), rws, (w == 0 && tid == 256) ? WS_T : WS_END, 0, AUX_SC1);
      STAMP(1);
#pragma unroll
      for (int i = 0; i < 4; ++i) *(LAS v4u*)(L + L_RAW + (i * 4 + hw) * 1024 + lane * 16) = tt[i];
      lds_signal(L, FL_LN, lane, 1u);
      lds_wait_ge(FL + FL_LN, 4u, 0);
      v4f d0 = {0.f, 0.f, 0.f, 0.f};
#pragma unroll
      for (int ks = 0; ks < 3; ++ks) d0 = mfma(frag_tr(ap + ks * 4096, 512), *(const LAS v4u*)(bp + (ks0 + ks) * 64), d0);
      if (s < 2) d0 = mfma(frag_tr(ap + 3 * 4096, 512), *(const LAS v4u*)(bp + (ks0 + 3) * 64), d0);
      ST128(pack28(d0, tagl), rws, (lane & 8) == 0 ? WS_G + (((kvh * 8 + (lane & 7)) * 4 + s) * 128 + b * 16 + hw * 4 + g) * 16 : WS_END, 0, AUX_SC1);
      lds_signal(L, FL_QP, lane, 1u);
      STAMP(2);
    }
    auto q_gather = [&]() {
      const int off = WS_G + (kvh * 8 + b) * 8192 + ((hw - 2) * 64 + lane) * 16;
      wait_until(tstart + QT);
      v4u gq[4];
      const v4u gt = LD128(rws, WS_T, 0, AUX_SC1);
      gather<4, 2048, 2, QSLEEP>(gq, rws, off, tagl, 0u, ~0ull);
      FL[FL_TG] = (GBASE && gt.z == tagl && gt.w == tagh) ? gt.x + (unsigned)(GOFF) : tstart + (unsigned)(GOFF);
      const v4f qv = (unpack28(gq[0]) + unpack28(gq[1])) + (unpack28(gq[2]) + unpack28(gq[3]));
      *(LAS v2u*)(L + L_Q + ((hw - 2) * 64 + lane) * 8) = v2u{pk2(qv.x, qv.y), pk2(qv.z, qv.w)};
      FL[FL_TAGL] = tagl;
      lds_signal(L, FL_Q, lane, 2u);
      STAMP(3);
    };
    if (hw >= 2) q_gather();
    lds_wait_ge(FL + FL_Q, 4u, 1);
    lds_wait_ge(FL + FL_QP, 4u, 1);
    v4u Fr[28];
    v4f cx, cb;
    {
      const v2u bv = __builtin_nontemporal_load((const GAS v2u*)(isW ? b2 + r5 * 16 + (lane & 3) * 4 : b1 + (isM ? m : 0) * 64 + hw * 16 + g * 4));
      const v2u xv = __builtin_amdgcn_raw_buffer_load_b64(rx, (kvh * D + r5 * 16 + (lane & 3) * 4) * 2, 0, AUX_NT);
      cb = v4f{bflo(bv.x), bfhi(bv.x), bflo(bv.y), bfhi(bv.y)};
      cx = v4f{bflo(xv.x), bfhi(xv.x), bflo(xv.y), bfhi(xv.y)};
    }
    auto stage_wo = [&]() {
      const __amdgpu_buffer_rsrc_t rw = __builtin_amdgcn_make_buffer_rsrc((void*)(wo + (kvh * 512 + kq * 128) * D + t * 64), 0, 127 * (D * 2) + 128, RSRC3);
      v4u tt[4];
#pragma unroll
      for (int i = 0; i < 4; ++i) tt[i] = LD128(rw, (lane >> 3) * (D * 2) + (lane & 7) * 16, (i * 4 + hw) * 8 * (D * 2), AUX_NT);
#pragma unroll
      for (int i = 0; i < 4; ++i) *(LAS v4u*)(L + L_RAW + (i * 4 + hw) * 1024 + lane * 16) = tt[i];
      lds_signal(L, FL_RAW, lane, 1u);
    };
    auto frag_wo = [&]() {
      lds_wait_ge(FL + FL_RAW, 4u, 0);
#pragma unroll
      for (int ks = 0; ks < 4; ++ks) Fr[ks] = frag_tr(ap + ks * 4096, 512);
    };
    auto stage_w1 = [&]() {
      const __amdgpu_buffer_rsrc_t rw = __builtin_amdgcn_make_buffer_rsrc((void*)(w1 + m * 64), 0, 447 * (F * 2) + 128, RSRC3);
#pragma unroll 1
      for (int it = 0; it < 14; it += W1B) {
        v4u tt[W1B];
#pragma unroll
        for (int i = 0; i < W1B; ++i) tt[i] = LD128(rw, (lane >> 3) * (F * 2) + (lane & 7) * 16, ((it + i) * 4 + hw) * 8 * (F * 2), AUX_NT);
#pragma unroll
        for (int i = 0; i < W1B; ++i) *(LAS v4u*)(L + L_RAW + ((it + i) * 4 + hw) * 1024 + lane * 16) = tt[i];
        if (W1S) __builtin_amdgcn_s_sleep(W1S);
      }
      lds_signal(L, FL_RAW, lane, 1u);
      lds_wait_ge(FL + FL_RAW, 4u, 1);
#pragma unroll
      for (int ks = 0; ks < 14; ++ks) Fr[ks] = frag_tr(ap + ks * 4096, 512);
      lds_signal(L, FL_RAW2, lane, 1u);
    };
#pragma unroll
    for (int i = 0; i < 28; ++i) Fr[i] = v4u{0u, 0u, 0u, 0u};
    if (!(LATEALL & 1) && isW) { if (WOT) wait_until(tstart + WOT + r5 * WOS); stage_wo(); frag_wo(); }
    if (!(LATEALL & 2) && isM) stage_w1();
    asm volatile("" :: "v"(cpf));
    STAMP(4);
    auto stage_w2 = [&]() {
      const __amdgpu_buffer_rsrc_t r2 = __builtin_amdgcn_make_buffer_rsrc((void*)(w2 + m * 64 * D), 0, 64 * D * 2, RSRC3);
      v4u uu[14];
#pragma unroll
      for (int i = 0; i < 14; ++i) uu[i] = LD128(r2, lane * 16, (i * 4 + hw) * 1024, AUX_NT);
      lds_wait_ge(FL + FL_RAW2, 4u, 0);
#pragma unroll
      for (int i = 0; i < 14; ++i) *(LAS v4u*)(L + L_RAW + (i * 4 + hw) * 1024 + lane * 16) = uu[i];
      lds_signal(L, FL_RAW3, lane, 1u);
      lds_wait_ge(FL + FL_RAW3, 4u, 0);
      const LAS unsigned char* a2 = L + L_RAW + (g * 8 + ((lane & 15) >> 2)) * 896 + hw * 224 + (lane & 3) * 8;
#pragma unroll
      for (int i = 0; i < 14; ++i) Fr[14 + i] = frag_tr(a2 + (i & 1) * 32 * 896 + (i >> 1) * 32, 4 * 896);
    };
    if (!LATEW2 && isM) { lds_wait_ge(FL + FL_Q, 4u, 1); stage_w2(); }
    float fw[4] = {0.f, 0.f, 0.f, 0.f};
    if (hw < 2) {
      lds_wait_ge(FL + FL_ML, 4u, 1);
      const v2f ml = *(const LAS v2f*)(L + L_ML + ((lane & 3) * 2 + hw) * 8);
      float Ms = ml.x;
      Ms = fmaxf(Ms, dpp<XOR1>(Ms)); Ms = fmaxf(Ms, dpp<XOR2>(Ms));
      const float Msub = Ms == -INFINITY ? 0.f : Ms;
      float Lw = __builtin_amdgcn_exp2f((ml.x - Msub) * SC2) * ml.y;
      Lw += dpp<XOR1>(Lw); Lw += dpp<XOR2>(Lw);
      ST128((v4u{__float_as_uint(Ms), __float_as_uint(Lw), tagl, tagh}), rws, lane == 0 ? WS_ML + ((grp * 2 + hw) * 4 + s) * 16 : WS_END, 0, AUX_SC1);
      const int off = WS_ML + ((grp * 2 + hw) * 4 + (lane & 3)) * 16;
      v4u gm1[1];
#pragma unroll
      for (int i = 0; i < MLW; ++i) __builtin_amdgcn_s_sleep(64);
      gather<1, 0, true, 64>(gm1, rws, off, tagl, tagh, ~0ull);
      const v4u gm = gm1[0];
      float M = __uint_as_float(gm.x);
      M = fmaxf(M, dpp<XOR1>(M)); M = fmaxf(M, dpp<XOR2>(M));
      float Ls = __builtin_amdgcn_exp2f((__uint_as_float(gm.x) - M) * SC2) * __uint_as_float(gm.y);
      Ls += dpp<XOR1>(Ls); Ls += dpp<XOR2>(Ls);
      const float f = __builtin_amdgcn_exp2f((ml.x - M) * SC2) / Ls;
#pragma unroll
      for (int i = 0; i < 4; ++i) fw[i] = __builtin_bit_cast(float, __builtin_amdgcn_readlane(__builtin_bit_cast(int, f), i));
    }
    STAMP(5);
    const unsigned tg = __builtin_amdgcn_readfirstlane(FL[FL_TG]);
    asm volatile("" : "+v"(cx), "+v"(cb));
    int p_p0 = WS_P0 + ((kvh * 8 + b) * 4 + s) * 2048 + (hw & 1) * 1024 + lane * 16;
    int p_st = isW ? ((lane & 8) == 0 ? WS_PW + (((kvh * 4 + kq) * 28 + t * 4 + hw) * 8 + (lane & 7)) * 64 + g * 16 : WS_END)
                   : WS_PF + ((m * 28 + hw * 7) * 8 + (lane & 15)) * 64 + g * 16;
    int p_h = lane < 4 ? WS_H + (kvh * 112 + r5 * 4 + lane) * 16 : WS_END;
    int g_p0 = WS_P0 + (kvh * 8 + (lane >> 3)) * 8192 + (kq * 32 + hw * 8 + (lane & 7)) * 16;
    int g_pw = WS_PW + (((lane >> 2) * 28 + r5) * 8 + kvh) * 64 + (lane & 3) * 16;
    int g_o = lane < 4 ? (kvh * D + r5 * 16 + lane * 4) * 2 : 8 * D * 2;
    int g_e = (w == 0 && lane == 0) ? WS_EPOCH : WS_END;
    int a_bw = isW ? L_BF + hw * 512 + lane * 8 : L_BF + (lane & 15) * 144 + (hw * 16 + g * 4) * 2;
    int a_br = isW ? L_BF + (lane & 7) * 64 + g * 16 : L_BF + (lane & 15) * 144 + g * 16;
    asm volatile("" : "+v"(p_p0), "+v"(p_st), "+v"(p_h), "+v"(g_p0), "+v"(g_pw), "+v"(g_o), "+v"(g_e), "+v"(a_bw), "+v"(a_br));
    const __amdgpu_buffer_rsrc_t rout = __builtin_amdgcn_make_buffer_rsrc((void*)out, 0, 8 * D * 2, RSRC3);
    lds_wait_ge(FL + FL_PRE, 4u, 1);
    STAMP(6);
    if (CODEPF && r5 >= 28) { const unsigned z = code_prefetch(hw + 1); asm volatile("" :: "v"(z)); }
    if ((LATEALL & 1) && isW) stage_wo();
    lds_wait_ge(FL + FL_DONE, 4u, 0);
    if (hw < 2) {
      STAMP(7);
      const LAS v4f* mp = (const LAS v4f*)(L + L_MRG + hw * 1024 + lane * 16);
      const v4f o = (mp[0] * fw[0] + mp[128] * fw[1]) + (mp[256] * fw[2] + mp[384] * fw[3]);
      ST128(pack24(o, tagl), rws, p_p0, 0, AUX_SC1);
      STAMP(8);
    }
    if (isW) {
      if (LATEALL & 1) frag_wo();
      if (WDELAY) __builtin_amdgcn_s_sleep(WDELAY);
      if (WT) wait_until(tg + SCHED63 + WT);
      v4u pv[4];
      Poll3<4> pw;
      if (KW > 1) { const int of[4] = {g_p0, g_p0 + 2048, g_p0 + 4096, g_p0 + 6144}; pw.run<GAPW>(pv, rwv, of, tagl, ~0ull); }
      else gather<4, 2048, false, SLPW, AXW>(pv, rws, g_p0, tagl, 0u, ~0ull);
      STAMP(9);
      const v4f o = (unpack24(pv[0]) + unpack24(pv[1])) + (unpack24(pv[2]) + unpack24(pv[3]));
      *(LAS v2u*)(L + a_bw) = v2u{pk2(o.x, o.y), pk2(o.z, o.w)};
      BAR();
      v4u bf[4];
#pragma unroll
      for (int ks = 0; ks < 4; ++ks) bf[ks] = *(const LAS v4u*)(L + a_br + ks * 512);
      SB();
      v4f d0 = {0.f, 0.f, 0.f, 0.f}, d1 = {0.f, 0.f, 0.f, 0.f};
      d0 = mfma(Fr[0], bf[0], d0); d1 = mfma(Fr[1], bf[1], d1); d0 = mfma(Fr[2], bf[2], d0); d1 = mfma(Fr[3], bf[3], d1);
      ST128(pack24(d0 + d1, tagl), rws, p_st, 0, AUX_SC1);
      STAMP(10);
      if (KW > 1) pw.drain();
      if (hw == 0) {
        v4u u[2];
        if (RDELAY) __builtin_amdgcn_s_sleep(RDELAY);
        if (RT) wait_until(tg + SCHED63 + RT);
        Poll3<2> pr;
        if (KR > 1) { const int of[2] = {g_pw, g_pw + 16 * 28 * 512}; pr.run<GAPR>(u, rwv, of, tagl, ~0ull); }
        else gather<2, 16 * 28 * 512, false, SLPR, AXR>(u, rws, g_pw, tagl, 0u, ~0ull);
        STAMP(11);
        const v4f hv = red16(unpack24(u[0]) + unpack24(u[1]));
        const unsigned h01 = pk2o(cx.x + rbf(hv.x), cx.y + rbf(hv.y)), h23 = pk2o(cx.z + rbf(hv.z), cx.w + rbf(hv.w));
        ST128((v4u{h01, h23, tagh, tagl}), rws, p_h, 0, AUX_SC1);
        STAMP(12);
        if (KR > 1) pr.drain();
        __builtin_amdgcn_s_sleep(FWAIT);
        if (FT) wait_until(tg + SCHED63 + FT);
        if (KR > 1) { const int of[2] = {g_pw + (WS_PF - WS_PW), g_pw + (WS_PF - WS_PW) + 16 * 28 * 512}; pr.run<GAPR>(u, rwv, of, tagl, (1ull << 48) - 1ull); }
        else gather<2, 16 * 28 * 512, false, SLPR, AXR>(u, rws, g_pw + (WS_PF - WS_PW), tagl, 0u, (1ull << 48) - 1ull);
        STAMP(13);
        const v4f ov = red16(unpack24(u[0]) + unpack24(u[1]));
        const unsigned o01 = pk2(bflo(h01) + rbf(rbf(ov.x) + cb.x), bfhi(h01) + rbf(rbf(ov.y) + cb.y));
        const unsigned o23 = pk2(bflo(h23) + rbf(rbf(ov.z) + cb.z), bfhi(h23) + rbf(rbf(ov.w) + cb.w));
        __builtin_amdgcn_raw_buffer_store_b64(v2u{o01, o23}, rout, g_o, 0, 0);
        __builtin_amdgcn_raw_buffer_store_b64(v2u{tagl, tagh}, rws, g_e, 0, AUX_SC1);
        STAMP(14);
        if (KR > 1) pr.keep();
      }
    } else if (isM) {
      if (LATEALL & 2) stage_w1();
      if (LATEW2) stage_w2();
      STAMP(19);
      Poll3<2> pm;
      mlp_ln(L, rws, rwv, pm, wv, lane, tagl, tg);
      STAMP(15);
      v4u bf[14];
#pragma unroll
      for (int ks = 0; ks < 14; ++ks) bf[ks] = *(const LAS v4u*)(bp + ks * 64);
      SB();
      v4f d0 = {0.f, 0.f, 0.f, 0.f}, d1 = {0.f, 0.f, 0.f, 0.f};
#pragma unroll
      for (int ks = 0; ks < 14; ks += 2) { d0 = mfma(Fr[ks], bf[ks], d0); d1 = mfma(Fr[ks + 1], bf[ks + 1], d1); }
      d0 += d1;
      const unsigned g01 = pk2o(d0.x, d0.y), g23 = pk2o(d0.z, d0.w);
      const unsigned a01 = pk2o(bflo(g01) + cb.x, bfhi(g01) + cb.y), a23 = pk2o(bflo(g23) + cb.z, bfhi(g23) + cb.w);
      float u[4] = {bflo(a01), bfhi(a01), bflo(a23), bfhi(a23)};
#pragma unroll
      for (int k = 0; k < 4; ++k) { const float a = u[k] > 0.f ? u[k] : 0.f; u[k] = a * a; }
      *(LAS v2u*)(L + a_bw) = v2u{pk2(u[0], u[1]), pk2(u[2], u[3])};
      BAR();
      STAMP(18);
      const v4u h0 = *(const LAS v4u*)(L + a_br), h1 = *(const LAS v4u*)(L + a_br + 64);
      const bool up = lane & 8;
      v4u ev[4];
#pragma unroll
      for (int i = 0; i < 4; ++i) {
        v4f ea = mfma(Fr[14 + 4 * i], h0, v4f{0.f, 0.f, 0.f, 0.f}), eb = ea;
        if (i < 3) eb = mfma(Fr[14 + 4 * i + 2], h0, v4f{0.f, 0.f, 0.f, 0.f});
        ea = mfma(Fr[14 + 4 * i + 1], h1, ea);
        if (i < 3) eb = mfma(Fr[14 + 4 * i + 3], h1, eb);
        ev[i] = pack24(v4f{up ? eb.x : ea.x, up ? eb.y : ea.y, up ? eb.z : ea.z, up ? eb.w : ea.w}, tagl);
        ST128(ev[i], rws, (i == 3 && up) ? WS_END : p_st + i * 1024, 0, AUX_SC1);
      }
      asm volatile("" :: "v"(ev[0]), "v"(ev[1]), "v"(ev[2]), "v"(ev[3]));
      STAMP(16);
      if (KM > 1) pm.keep();
    }
  } else {
    const size_t kvoff = ((size_t)grp * SK + (size_t)s * (ILV ? 32 : 1024)) * DH;
    constexpr int TSTR = ILV ? 65536 : 16384, NREC = ILV ? 31 * 65536 + 16384 : 524288;
    const __amdgpu_buffer_rsrc_t rk = __builtin_amdgcn_make_buffer_rsrc((void*)(kc + kvoff), 0, NREC, RSRC3);
    const int voff = lane * 16 + wv * 4096;
    v4u X[NEXTRA * 4][4], R[4][4];
    unsigned tb = (unsigned)__builtin_amdgcn_s_memrealtime() + (unsigned)(T1) - (unsigned)(STAG == 1 ? ((wv * 4 + (r5 & 3)) * TAU) >> 8 : STAG == 2 ? ((r5 & 15) * TAU) >> 8 : 0);
#define LANDED() asm volatile("s_waitcnt vmcnt(%0)" :: "i"(4 * (LEAD - 1)) : "memory")
    STAMP(0);
    if (wv == 0) *(volatile LAS unsigned*)(L + L_ML) = tb;
    if (!EARLY) BAR();
#pragma unroll
    for (int n = 0; n < NEXTRA * 4 + LEAD; ++n) {
      if (EARLY && n == EARLY) BAR();
      if (n >= LEAD) LANDED();
      if (n >= LEAD) pace(tb, n);
      issue_turn(n < NEXTRA * 4 ? X[n] : R[n - NEXTRA * 4], rk, voff, n * TSTR);
    }
    const bool b0 = lane & 1, b1_ = lane & 2, b2_ = lane & 4, b3 = lane & 8;
    const int hf = lane >> 5, c = lane & 31;
    LAS float* P = (LAS float*)(L + L_P) + wv * 512;
    LAS float* Pl = P + ((lane >> 3) & 3) * 16 + hf * 8 + (lane & 7);
    asm volatile("" :: "v"(cpf));
    STAMP(1);
    lds_wait_ge(FL + FL_Q, 4u, 0);
    STAMP(2);
    if (GBASE) tb = __builtin_amdgcn_readfirstlane(FL[FL_TG]);
    const v4u q0 = *(const LAS v4u*)(L + L_Q + c * 16), q1 = *(const LAS v4u*)(L + L_Q + 512 + c * 16);
    float mx = -INFINITY;
#pragma unroll
    for (int g = 0; g < NEXTRA; ++g) {
      float r[4], rx[4];
#pragma unroll
      for (int d = 0; d < 4; ++d) {
        rx[d] = k_turn(X[4 * g + d], q0, q1, b0, b1_, b2_);
        SB();
        LANDED();
        pace(tb, (NEXTRA + g) * 4 + d + LEAD);
        issue_turn(R[(d + LEAD) & 3], rk, voff, ((NEXTRA + g) * 4 + d + LEAD) * TSTR);
        r[d] = k_turn(R[d], q0, q1, b0, b1_, b2_);
        SB();
      }
      const float sx = k_group(rx[0], rx[1], rx[2], rx[3], b3), sc = k_group(r[0], r[1], r[2], r[3], b3);
      mx = fmaxf(mx, fmaxf(sx, sc));
      Pl[g * 64] = sx; Pl[(NEXTRA + g) * 64] = sc;
      SB();
    }
    STAMP(3);
    const __amdgpu_buffer_rsrc_t rv = __builtin_amdgcn_make_buffer_rsrc((void*)(vc + kvoff), 0, NREC, RSRC3);
#pragma unroll 1
    for (int g = 2 * NEXTRA; g < 8; ++g) {
      float r[4];
#pragma unroll
      for (int d = 0; d < 4; ++d) {
        const int nx = g * 4 + d + LEAD;
        const bool tov = (d + LEAD >= 4) && g == 7;
        const __amdgpu_buffer_rsrc_t rn = __builtin_amdgcn_make_buffer_rsrc((void*)((tov ? vc : kc) + kvoff), 0, NREC, RSRC3);
        LANDED();
        pace2(tb, nx);
        issue_turn(R[(d + LEAD) & 3], rn, voff, (nx & 31) * TSTR);
        r[d] = k_turn(R[d], q0, q1, b0, b1_, b2_);
        SB();
      }
      const float sc = k_group(r[0], r[1], r[2], r[3], b3);
      mx = fmaxf(mx, sc);
      Pl[g * 64] = sc;
    }
    STAMP(4);
    mx = fmaxf(mx, dpp<XOR2>(mx)); mx = fmaxf(mx, dpp<ROR4>(mx)); mx = fmaxf(mx, dpp<ROR8>(mx));
    mx = sw16_max(mx); mx = sw32_max(mx);
    const float msub = mx == -INFINITY ? 0.f : mx;
    float ls = 0.f;
    {
      float sv[8];
#pragma unroll
      for (int g = 0; g < 8; ++g) sv[g] = Pl[g * 64];
#pragma unroll
      for (int g = 0; g < 8; ++g) { const float p = __builtin_amdgcn_exp2f((sv[g] - msub) * SC2); ls += p; Pl[g * 64] = p; }
    }
    ls += dpp<XOR2>(ls); ls += dpp<ROR8>(ls); ls += dpp<ROR4>(ls);
    ls = sw16_add(ls, ls); ls = sw32_add(ls, ls);
    *(LAS v2f*)(L + (lane < 2 ? L_ML + (wv * 2 + lane) * 8 : L_SINK + 1024 + lane * 8)) = v2f{mx, ls};
    lds_signal(L, FL_ML, lane, 1u);
    v2f a0[4], a1[4];
#pragma unroll
    for (int k = 0; k < 4; ++k) { a0[k] = v2f{0.f, 0.f}; a1[k] = v2f{0.f, 0.f}; }
    const LAS v4f* Pr = (const LAS v4f*)(P + hf * 8);
    LAS v4f* mo = (LAS v4f*)(L + L_MRG + wv * 2048 + lane * 32);
    auto v_group = [&](int g, auto last) {
      v4f pa[4], pb[4];
#pragma unroll
      for (int d = 0; d < 4; ++d) { pa[d] = Pr[(g * 4 + d) * 4]; pb[d] = Pr[(g * 4 + d) * 4 + 1]; }
#pragma unroll
      for (int d = 0; d < 4; ++d) {
        if (!last.value || d + LEAD < 4) {
          LANDED();
          pace2(tb, 32 + g * 4 + d + LEAD);
          issue_turn(R[(d + LEAD) & 3], rv, voff, (g * 4 + d + LEAD) * TSTR);
        }
        v_turn(R[d], pa[d], pb[d], a0, a1);
        SB();
        if (!last.value && d == 1) lds_signal(L, FL_PRE, lane, g == PREG ? 1u : 0u);
      }
    };
#pragma unroll 1
    for (int g = 0; g < 7; ++g) v_group(g, BoolC<false>{});
    v_group(7, BoolC<true>{});
    mo[0] = v4f{sw32_add(a0[0].x, a1[0].x), sw32_add(a0[0].y, a1[0].y), sw32_add(a0[1].x, a1[1].x), sw32_add(a0[1].y, a1[1].y)};
    mo[1] = v4f{sw32_add(a0[2].x, a1[2].x), sw32_add(a0[2].y, a1[2].y), sw32_add(a0[3].x, a1[3].x), sw32_add(a0[3].y, a1[3].y)};
    lds_signal(L, FL_DONE, lane, 1u);
    STAMP(5);
    if (isM) { asm volatile("s_waitcnt vmcnt(0)" ::: "memory"); Poll3<2> pm; mlp_ln(L, rws, rwv, pm, wv, lane, __builtin_amdgcn_readfirstlane(FL[FL_TAGL]), tb); if (KM > 1) pm.keep(); }
  }
}
}

extern "C" void kernel_launch(void* const* d_in, const int* in_sizes, int n_in, void* d_out, int out_size, void* d_ws,
                              size_t ws_size, hipStream_t stream) {
  using namespace fk;
  (void)hipFuncSetAttribute((const void*)fused_decode, hipFuncAttributeMaxDynamicSharedMemorySize, L_END + DBG_STAMPS * 2048);
  hipLaunchKernelGGL(fused_decode, dim3(256), dim3(512), L_END + DBG_STAMPS * 2048, stream, (const u16*)d_in[0], (const u16*)d_in[1],
                     (const u16*)d_in[2], (const u16*)d_in[3], (const u16*)d_in[4], (const u16*)d_in[6], (const u16*)d_in[7],
                     (const u16*)d_in[8], (const u16*)d_in[9], (u16*)d_out, (unsigned char*)d_ws);
}
```

```cpp
#define TAU0 1010
#define TAU1 912
#define TAU 884
#include <hip/hip_runtime.h>
#include <hip/hip_bf16.h>
#include <stdint.h>

#define DBG_STAMPS 0
#define ROTK 0
#define QT 500
#define QSLEEP 24
#define WOT 900
#define WOS 40
#define GOFF 20
#define WT 195
#define G1 92
#define G2 86
#define G3 166
#define WDELAY 6
#define RDELAY 8
#define FWAIT 60
#define PREG 6
#define CODE_BYTES 34944
#define W1E 1
#define ROT 1
#define EARLY 1
#define SLPT 0

namespace fk {
typedef unsigned short u16;
typedef unsigned long long u64;
typedef unsigned v4u __attribute__((ext_vector_type(4)));
typedef unsigned v2u __attribute__((ext_vector_type(2)));
typedef float v4f __attribute__((ext_vector_type(4)));
typedef float v2f __attribute__((ext_vector_type(2)));
typedef __bf16 bf16x2 __attribute__((ext_vector_type(2)));
typedef __bf16 bf16x8 __attribute__((ext_vector_type(8)));
typedef short s16x4 __attribute__((ext_vector_type(4)));
#define LAS __attribute__((address_space(3)))
#define GAS __attribute__((address_space(1)))

constexpr int D = 448, F = 1792, SK = 4096, DH = 256, NQ = 4096;
constexpr int AUX_NT = 2, AUX_SC1 = 16;
constexpr int NEXTRA = 3;
constexpr int LEAD = 2;
constexpr int PN0 = 16;
constexpr int RT = WT + G1, MT = RT + G2, FT = MT + G3;
constexpr int RSRC3 = 0x00020000;
constexpr float SC2 = 0.0625f * 1.4426950408889634f;
struct Spin {
  unsigned n = 0, t0 = 0;
  __device__ __forceinline__ bool expired() {
    if ((++n & 4095u) != 0u) return false;
    const unsigned t = (unsigned)__builtin_amdgcn_s_memrealtime();
    if (n == 4096u) t0 = t;
    return t - t0 > (1u << 27);
  }
};

constexpr int WS_EPOCH = 0;
constexpr int WS_T = 2048;
constexpr int WS_G = 4096;
constexpr int WS_ML = WS_G + 8 * 8 * 4 * 128 * 16;
constexpr int WS_H = WS_ML + 64 * 8 * 16;
constexpr int WS_P0 = WS_H + 16384;
constexpr int WS_PW = WS_P0 + 64 * 4 * 2048;
constexpr int WS_PF = WS_PW + 32 * 28 * 512;
constexpr int WS_END = WS_PF + 28 * 28 * 512;
constexpr int WS_SPAN = 2 << 20;
static_assert(WS_END <= WS_SPAN, "span");
constexpr int WS_DBG = 128 << 20;

constexpr int LN_STR = 912;
constexpr int L_P = 0;
constexpr int L_MRG = 8192;
constexpr int L_Q = 16384;
constexpr int L_FLAG = 17408;
constexpr int L_ML = L_FLAG + 64;
constexpr int L_LN = L_ML + 64;
constexpr int L_BF = L_LN + 8 * LN_STR;
constexpr int L_SINK = L_BF + 4096;
constexpr int L_RAW = L_SINK + 2048;
constexpr int L_END = 96 * 1024;
static_assert(L_RAW + 57344 <= L_END, "lds");
static_assert(L_LN % 16 == 0 && L_BF % 16 == 0 && L_RAW % 16 == 0, "align");
enum { FL_LN = 0, FL_QP = 1, FL_Q = 2, FL_PRE = 3, FL_DONE = 4, FL_RAW = 5, FL_RAW2 = 6, FL_RAW3 = 7, FL_ML = 9, FL_TAGL = 10, FL_TAGH = 11, FL_TG = 12 };

__device__ __forceinline__ float bflo(unsigned v) { return __builtin_bit_cast(float, v << 16); }
__device__ __forceinline__ float bfhi(unsigned v) { return __builtin_bit_cast(float, v & 0xffff0000u); }
__device__ __forceinline__ float rbf(float f) { return (float)(__bf16)f; }
__device__ __forceinline__ unsigned pk2(float lo, float hi) {
  bf16x2 v = {(__bf16)lo, (__bf16)hi};
  return __builtin_bit_cast(unsigned, v);
}
__device__ __forceinline__ unsigned pk2o(float lo, float hi) {
  unsigned p = pk2(lo, hi);
  asm("" : "+v"(p));
  return p;
}
__device__ __forceinline__ float dot2(unsigned a, unsigned b, float c) {
  return __builtin_amdgcn_fdot2_f32_bf16(__builtin_bit_cast(bf16x2, a), __builtin_bit_cast(bf16x2, b), c, false);
}
__device__ __forceinline__ v4f mfma(v4u a, v4u b, v4f c) {
  return __builtin_amdgcn_mfma_f32_16x16x32_bf16(__builtin_bit_cast(bf16x8, a), __builtin_bit_cast(bf16x8, b), c, 0, 0, 0);
}
template <int CTRL> __device__ __forceinline__ float dpp(float x) {
  return __builtin_bit_cast(float, __builtin_amdgcn_mov_dpp(__builtin_bit_cast(int, x), CTRL, 0xf, 0xf, true));
}
constexpr int XOR1 = 0xB1, XOR2 = 0x4E, HMIR = 0x141, ROR4 = 0x124, ROR8 = 0x128;
__device__ __forceinline__ float sw16_add(float a, float b) {
  auto s = __builtin_amdgcn_permlane16_swap(__float_as_uint(a), __float_as_uint(b), false, false);
  return __uint_as_float(s[0]) + __uint_as_float(s[1]);
}
__device__ __forceinline__ float sw32_add(float a, float b) {
  auto s = __builtin_amdgcn_permlane32_swap(__float_as_uint(a), __float_as_uint(b), false, false);
  return __uint_as_float(s[0]) + __uint_as_float(s[1]);
}
__device__ __forceinline__ float sw16_max(float a) {
  auto s = __builtin_amdgcn_permlane16_swap(__float_as_uint(a), __float_as_uint(a), false, false);
  return fmaxf(__uint_as_float(s[0]), __uint_as_float(s[1]));
}
__device__ __forceinline__ float sw32_max(float a) {
  auto s = __builtin_amdgcn_permlane32_swap(__float_as_uint(a), __float_as_uint(a), false, false);
  return fmaxf(__uint_as_float(s[0]), __uint_as_float(s[1]));
}
__device__ __forceinline__ v4u pack24(v4f v, unsigned tag) {
  const unsigned a = __float_as_uint(v.x), b = __float_as_uint(v.y), c = __float_as_uint(v.z), d = __float_as_uint(v.w);
  return v4u{__builtin_amdgcn_perm(b, a, 0x05030201u), __builtin_amdgcn_perm(c, b, 0x06050302u), __builtin_amdgcn_perm(d, c, 0x07060503u), tag};
}
__device__ __forceinline__ v4f unpack24(v4u g) {
  return v4f{__uint_as_float(g.x << 8), __uint_as_float(__builtin_amdgcn_perm(g.y, g.x, 0x0504030Cu)),
             __uint_as_float(__builtin_amdgcn_perm(g.z, g.y, 0x0403020Cu)), __uint_as_float(g.z & 0xFFFFFF00u)};
}
__device__ __forceinline__ v4u pack28(v4f v, unsigned tag) {
  return v4u{(__float_as_uint(v.x) & ~3u) | (tag & 3u), (__float_as_uint(v.y) & ~3u) | ((tag >> 2) & 3u),
             (__float_as_uint(v.z) & ~3u) | ((tag >> 4) & 3u), (__float_as_uint(v.w) & ~3u) | ((tag >> 6) & 3u)};
}
__device__ __forceinline__ v4f unpack28(v4u g) { return __builtin_bit_cast(v4f, g & ~3u); }
__device__ __forceinline__ bool bad28(v4u g, unsigned tag) {
  return (((g.x ^ tag) | (g.y ^ (tag >> 2)) | (g.z ^ (tag >> 4)) | (g.w ^ (tag >> 6))) & 3u) != 0u;
}
#define LGKM0() asm volatile("s_waitcnt lgkmcnt(0)" ::: "memory")
#define CLOB() asm volatile("" ::: "memory")
#define SB() __builtin_amdgcn_sched_barrier(0)
#define BAR() do { asm volatile("s_waitcnt lgkmcnt(0)" ::: "memory"); __builtin_amdgcn_s_barrier(); asm volatile("" ::: "memory"); __builtin_amdgcn_sched_barrier(0); } while (0)
#define LD128(rs, vo, so, aux) __builtin_amdgcn_raw_buffer_load_b128(rs, vo, so, aux)
#define ST128(v, rs, vo, so, aux) __builtin_amdgcn_raw_buffer_store_b128(v, rs, vo, so, aux)

#define STAMP(i) do {} while (0)

__device__ __forceinline__ void lds_wait_ge(volatile LAS unsigned* f, unsigned want, int slp) {
  Spin sp;
#pragma nounroll
  while (*f < want) {
    if (slp) __builtin_amdgcn_s_sleep(8);
    if (sp.expired()) break;
  }
  CLOB();
}
__device__ __forceinline__ void lds_signal(LAS unsigned char* L, int word, int lane, unsigned inc) {
  LGKM0();
  LAS unsigned* p = (LAS unsigned*)(L + (lane == 0 ? L_FLAG + word * 4 : L_SINK + lane * 4));
  __hip_atomic_fetch_add(p, inc, __ATOMIC_RELAXED, __HIP_MEMORY_SCOPE_WORKGROUP);
}
template <int N, int STR, int WIDE, int SLP>
__device__ __forceinline__ void gather(v4u (&v)[N], __amdgpu_buffer_rsrc_t rs, int off, unsigned tl, unsigned th, u64 last_mask) {
  Spin sp;
#pragma nounroll
  for (;;) {
    CLOB();
#pragma unroll
    for (int i = 0; i < N; ++i) v[i] = LD128(rs, off + i * STR, 0, AUX_SC1);
    u64 any = 0ull;
#pragma unroll
    for (int i = 0; i < N; ++i) {
      const u64 bad = __builtin_amdgcn_ballot_w64(WIDE == 2 ? bad28(v[i], tl) : WIDE ? ((v[i].z ^ tl) | (v[i].w ^ th)) != 0u : v[i].w != tl);
      any |= i == N - 1 ? bad & last_mask : bad;
    }
    if (any == 0ull || sp.expired()) break;
    if (SLP) __builtin_amdgcn_s_sleep(SLP);
  }
}
__device__ __forceinline__ void wait_until(unsigned until) {
  asm volatile("8:\n\ts_memrealtime vcc\n\ts_waitcnt lgkmcnt(0)\n\ts_sub_i32 vcc_lo, vcc_lo, %0\n\ts_cmp_lt_i32 vcc_lo, 0\n\ts_cbranch_scc1 8b\n" :: "s"(until) : "scc", "vcc", "memory");
}
__device__ __forceinline__ void doze_until(unsigned until) {
#pragma nounroll
  while ((int)((unsigned)__builtin_amdgcn_s_memrealtime() - until) < 0) __builtin_amdgcn_s_sleep(4);
}
constexpr int PN1 = 34;
constexpr int SCHED63 = (PN0 * TAU0 + (PN1 - PN0) * TAU1 + (63 - PN1) * TAU) >> 4;
__device__ __forceinline__ void pace(unsigned tb, int n) { wait_until(tb + (unsigned)((n < PN0 ? n * TAU0 : PN0 * TAU0 + (n - PN0) * TAU1) >> 4)); }
__device__ __forceinline__ void pace1(unsigned tb, int n) { wait_until(tb + (unsigned)((PN0 * TAU0 + (n - PN0) * TAU1) >> 4)); }
__device__ __forceinline__ void pace2(unsigned tb, int n) { wait_until(tb + (unsigned)((PN0 * TAU0 + (PN1 - PN0) * TAU1 + (n - PN1) * TAU) >> 4)); }
template <bool B> struct BoolC { static constexpr bool value = B; };
__device__ __forceinline__ void issue_turn(v4u* r, __amdgpu_buffer_rsrc_t rs, int voff, int soff) {
#pragma unroll
  for (int i = 0; i < 4; ++i) r[i] = LD128(rs, voff + i * 1024, soff, AUX_NT);
  SB();
}
__device__ __forceinline__ float k_turn(const v4u* k, v4u q0, v4u q1, bool b0, bool b1, bool b2) {
  float x[8];
#pragma unroll
  for (int i = 0; i < 4; ++i) {
    float a0 = dot2(k[i].x, q0.x, 0.f), a1 = dot2(k[i].x, q1.x, 0.f);
    a0 = dot2(k[i].y, q0.y, a0); a1 = dot2(k[i].y, q1.y, a1);
    a0 = dot2(k[i].z, q0.z, a0); a1 = dot2(k[i].z, q1.z, a1);
    a0 = dot2(k[i].w, q0.w, a0); a1 = dot2(k[i].w, q1.w, a1);
    x[2 * i] = a0; x[2 * i + 1] = a1;
  }
  float y[4], z[2];
#pragma unroll
  for (int v = 0; v < 4; ++v) y[v] = (b2 ? x[v + 4] : x[v]) + dpp<HMIR>(b2 ? x[v] : x[v + 4]);
#pragma unroll
  for (int v = 0; v < 2; ++v) z[v] = (b1 ? y[v + 2] : y[v]) + dpp<XOR2>(b1 ? y[v] : y[v + 2]);
  return (b0 ? z[1] : z[0]) + dpp<XOR1>(b0 ? z[0] : z[1]);
}
__device__ __forceinline__ float k_group(float r0, float r1, float r2, float r3, bool b3) {
  const float u0 = (b3 ? r1 : r0) + dpp<ROR8>(b3 ? r0 : r1);
  const float u1 = (b3 ? r3 : r2) + dpp<ROR8>(b3 ? r2 : r3);
  return rbf(sw16_add(u0, u1));
}
__device__ __forceinline__ void v_turn(const v4u* v, v4f pa, v4f pb, v2f (&a0)[4], v2f (&a1)[4]) {
#pragma unroll
  for (int i = 0; i < 4; ++i) {
    const float p0 = i == 0 ? pa.x : i == 1 ? pa.z : i == 2 ? pb.x : pb.z;
    const float p1 = i == 0 ? pa.y : i == 1 ? pa.w : i == 2 ? pb.y : pb.w;
#pragma unroll
    for (int j = 0; j < 4; ++j) {
      const v2f e = {bflo(v[i][j]), bfhi(v[i][j])};
      a0[j] = __builtin_elementwise_fma(e, v2f{p0, p0}, a0[j]);
      a1[j] = __builtin_elementwise_fma(e, v2f{p1, p1}, a1[j]);
    }
  }
}
template <bool FULL> __device__ __forceinline__ float row_sum(float v) {
  v += dpp<XOR1>(v); v += dpp<XOR2>(v); v += dpp<HMIR>(v); v += dpp<ROR8>(v);
  v = sw16_add(v, v);
  return FULL ? sw32_add(v, v) : v;
}
template <int NW> __device__ __forceinline__ void ln_row(unsigned (&x)[NW], bool act) {
  v2f v[NW];
#pragma unroll
  for (int k = 0; k < NW; ++k) v[k] = v2f{bflo(x[k]), bfhi(x[k])};
  float mean, var;
  if constexpr (NW == 4) {
    const float c = bflo(__builtin_amdgcn_readfirstlane(x[0]));
    v2f s1 = {0.f, 0.f}, s2 = {0.f, 0.f};
#pragma unroll
    for (int k = 0; k < NW; ++k) { const v2f d = v[k] - c; s1 += d; s2 = __builtin_elementwise_fma(d, d, s2); }
    float t = sw32_add(act ? s1.x + s1.y : 0.f, act ? s2.x + s2.y : 0.f);
    t = row_sum<false>(t) * (1.0f / D);
    const float m1 = __builtin_bit_cast(float, __builtin_amdgcn_readlane(__builtin_bit_cast(int, t), 0));
    const float m2 = __builtin_bit_cast(float, __builtin_amdgcn_readlane(__builtin_bit_cast(int, t), 32));
    mean = c + m1;
    var = fmaxf(fmaf(-m1, m1, m2), m2 - m2);
  } else {
    constexpr unsigned ONES = 0x3F803F80u;
    float sa = 0.f, sb = 0.f;
#pragma unroll
    for (int k = 0; k < NW; k += 2) { sa = dot2(x[k], ONES, sa); sb = dot2(x[k + 1], ONES, sb); }
    mean = row_sum<false>(sa + sb) * (1.0f / D);
    v2f q0 = {0.f, 0.f}, q1 = {0.f, 0.f};
#pragma unroll
    for (int k = 0; k < NW; k += 2) {
      const v2f d0 = v[k] - mean, d1 = v[k + 1] - mean;
      q0 = __builtin_elementwise_fma(d0, d0, q0); q1 = __builtin_elementwise_fma(d1, d1, q1);
    }
    q0 += q1;
    var = row_sum<false>(act ? q0.x + q0.y : 0.f) * (1.0f / D);
  }
  const float mb = rbf(mean);
#pragma unroll
  for (int k = 0; k < NW; ++k) { const v2f d = v[k] - mb; const unsigned p = pk2o(d.x, d.y); v[k] = v2f{bflo(p), bfhi(p)}; }
  const float den = rbf(__builtin_amdgcn_sqrtf(rbf(rbf(var) + 1e-5f)));
  const float ri = __builtin_amdgcn_rcpf(den);
#pragma unroll
  for (int k = 0; k < NW; ++k) { const v2f a = v[k] * ri; x[k] = pk2(a.x, a.y); }
}
__device__ __forceinline__ v4u frag_tr(const LAS unsigned char* p, int hi) {
#if defined(__HIP_DEVICE_COMPILE__)
  const v2u a = __builtin_bit_cast(v2u, __builtin_amdgcn_ds_read_tr16_b64_v4i16((LAS s16x4*)p));
  const v2u b = __builtin_bit_cast(v2u, __builtin_amdgcn_ds_read_tr16_b64_v4i16((LAS s16x4*)(p + hi)));
  return v4u{a.x, a.y, b.x, b.y};
#else
  return v4u{0u, 0u, 0u, 0u};
#endif
}
__device__ __forceinline__ v4f red16(v4f t) {
#pragma unroll
  for (int k = 0; k < 4; ++k) { float a = t[k]; a += dpp<ROR8>(a); a += dpp<ROR4>(a); a = sw16_add(a, a); a = sw32_add(a, a); t[k] = a; }
  return t;
}
__device__ __forceinline__ void mlp_ln(LAS unsigned char* L, __amdgpu_buffer_rsrc_t rt, int row, int lane, unsigned tl, unsigned t0) {
  const bool act = lane < 56;
  const int a = act ? L_LN + row * LN_STR + lane * 8 : L_SINK + lane * 8;
  v4u hg[2];
  int o0 = WS_H + (row * 112 + (act ? lane : 55)) * 16;
  asm volatile("" : "+v"(o0));
  wait_until(t0 + SCHED63 + MT);
  gather<2, 896, 0, SLPT>(hg, rt, o0, tl, 0u, ~0ull);
  { const int wv = row; (void)wv; STAMP(17); }
  unsigned x[4] = {hg[0].x, hg[0].y, hg[1].x, hg[1].y};
  ln_row<4>(x, act);
  *(LAS v2u*)(L + a) = v2u{x[0], x[1]}; *(LAS v2u*)(L + a + 448) = v2u{x[2], x[3]};
  BAR();
}

__global__ void __launch_bounds__(512, 2)
fused_decode(const u16* __restrict__ x, const u16* __restrict__ kc, const u16* __restrict__ vc, const u16* __restrict__ wq,
             const u16* __restrict__ wo, const u16* __restrict__ w1, const u16* __restrict__ b1, const u16* __restrict__ w2,
             const u16* __restrict__ b2, u16* __restrict__ out, unsigned char* __restrict__ ws) {
  extern __shared__ __attribute__((aligned(16))) unsigned char smem[];
  LAS unsigned char* L = (LAS unsigned char*)smem;
  const u64 pc0 = __builtin_amdgcn_s_getpc() & ~0xFFull;
  const int w = blockIdx.x, tid = threadIdx.x, lane = tid & 63, wv = __builtin_amdgcn_readfirstlane(tid >> 6);
  const int kvh = w & 7, r5 = w >> 3, b = r5 & 7, s = r5 >> 3, grp = b * 8 + kvh;
  const int m = (r5 - 28) * 8 + kvh;
  const bool isM = r5 >= 28 && m < 28;
  volatile LAS unsigned* FL = (volatile LAS unsigned*)(L + L_FLAG);
  const __amdgpu_buffer_rsrc_t rws = __builtin_amdgcn_make_buffer_rsrc((void*)ws, 0, WS_END, RSRC3);
  auto code_prefetch = [&]() {
    const __amdgpu_buffer_rsrc_t rc = __builtin_amdgcn_make_buffer_rsrc((void*)pc0, 0, CODE_BYTES, RSRC3);
    constexpr int CPL = (CODE_BYTES / 128 + 31) / 32;
    return __builtin_amdgcn_raw_buffer_load_b32(rc, lane < CPL ? (lane * 32 + r5) * 128 : CODE_BYTES, 0, 0);
  };
  unsigned cpf = 0;
  if (wv == 4) cpf = code_prefetch();
  SB();
  asm volatile("" :: "s"(x), "s"(kc), "s"(vc), "s"(wq), "s"(wo), "s"(w1), "s"(b1), "s"(w2), "s"(b2), "s"(out), "s"(ws));

  if (__builtin_expect(wv >= 4, 0)) {
    const int hw = wv - 4;
    STAMP(0);
    __builtin_amdgcn_s_setprio(3);
    const unsigned tstart = (unsigned)__builtin_amdgcn_s_memrealtime();
    const bool isW = r5 < 28;
    const int t = r5 >> 2, kq = r5 & 3;
    const int g = lane >> 4;
    const LAS unsigned char* ap = L + L_RAW + (g * 8 + ((lane & 15) >> 2)) * 128 + hw * 32 + (lane & 3) * 8;
    const LAS unsigned char* bp = L + L_LN + (lane & 7) * LN_STR + g * 16;
    const __amdgpu_buffer_rsrc_t rx = __builtin_amdgcn_make_buffer_rsrc((void*)x, 0, 8 * D * 2, RSRC3);
    const v2u ep = __builtin_amdgcn_raw_buffer_load_b64(rws, WS_EPOCH, 0, AUX_SC1);
    unsigned tagl, tagh;
#define MAKE_TAGS() do { const unsigned el = __builtin_amdgcn_readfirstlane(ep.x), eh = __builtin_amdgcn_readfirstlane(ep.y); \
      u64 z = (((u64)eh << 32) | el) + 0x9E3779B97F4A7C15ull; \
      z = (z ^ (z >> 30)) * 0xBF58476D1CE4E5B9ull; z = (z ^ (z >> 27)) * 0x94D049BB133111EBull; z ^= z >> 31; \
      tagl = (((unsigned)z ^ el) & 3u) == 0u ? (unsigned)z ^ 1u : (unsigned)z; \
      tagl = ((tagl ^ (tagl >> 2)) & 3u) == 0u ? tagl ^ 4u : tagl;     \
      tagh = ((unsigned)(z >> 32) & ~31u) | ((eh + 1u) & 31u); } while (0)
    if (tid < 256 + 16) FL[tid - 256] = 0u;
    v4u t1[14];
    auto w1_issue = [&]() {
      const __amdgpu_buffer_rsrc_t rw = __builtin_amdgcn_make_buffer_rsrc((void*)(w1 + (isM ? m : 0) * 64), 0, isM ? 447 * (F * 2) + 128 : 0, RSRC3);
      if (isM) {
#pragma unroll
        for (int i = 0; i < 14; ++i) t1[i] = LD128(rw, (lane >> 3) * (F * 2) + (lane & 7) * 16, (i * 4 + hw) * 8 * (F * 2), AUX_NT);
      }
    };
    {
      const int row = hw * 2 + (lane >> 5), c = lane & 31;
      const int ks0 = s < 3 ? s * 4 : 11, nks = s < 2 ? 4 : 3;
      const int xo = c < 28 ? row * (D * 2) + c * 32 : 8 * D * 2;
      const v4u xa = LD128(rx, xo, 0, AUX_NT), xb = LD128(rx, xo + 16, 0, AUX_NT);
      const __amdgpu_buffer_rsrc_t rq = __builtin_amdgcn_make_buffer_rsrc((void*)(wq + ks0 * 32 * NQ + (kvh * 8 + b) * 64), 0, (nks * 32 - 1) * (NQ * 2) + 128, RSRC3);
      v4u tt[4];
#pragma unroll
      for (int i = 0; i < 4; ++i) tt[i] = LD128(rq, (lane >> 3) * (NQ * 2) + (lane & 7) * 16, (i * 4 + hw) * 8 * (NQ * 2), AUX_NT);
      if (W1E == 1) w1_issue();
      SB();
      BAR();
      {
        unsigned xr[8] = {xa.x, xa.y, xa.z, xa.w, xb.x, xb.y, xb.z, xb.w};
        ln_row<8>(xr, c < 28);
        const int a = c < 28 ? L_LN + row * LN_STR + c * 32 : L_SINK + lane * 32;
        *(LAS v4u*)(L + a) = v4u{xr[0], xr[1], xr[2], xr[3]}; *(LAS v4u*)(L + a + 16) = v4u{xr[4], xr[5], xr[6], xr[7]};
      }
      MAKE_TAGS();
      ST128((v4u{*(volatile LAS unsigned*)(L + L_ML), 0u, tagl, tagh}), rws, (w == 0 && tid == 256) ? WS_T : WS_END, 0, AUX_SC1);
      STAMP(1);
#pragma unroll
      for (int i = 0; i < 4; ++i) *(LAS v4u*)(L + L_RAW + (i * 4 + hw) * 1024 + lane * 16) = tt[i];
      lds_signal(L, FL_LN, lane, 1u);
      lds_wait_ge(FL + FL_LN, 4u, 0);
      v4f d0 = {0.f, 0.f, 0.f, 0.f};
#pragma unroll
      for (int ks = 0; ks < 3; ++ks) d0 = mfma(frag_tr(ap + ks * 4096, 512), *(const LAS v4u*)(bp + (ks0 + ks) * 64), d0);
      if (s < 2) d0 = mfma(frag_tr(ap + 3 * 4096, 512), *(const LAS v4u*)(bp + (ks0 + 3) * 64), d0);
      ST128(pack28(d0, tagl), rws, (lane & 8) == 0 ? WS_G + (((kvh * 8 + (lane & 7)) * 4 + s) * 128 + b * 16 + hw * 4 + g) * 16 : WS_END, 0, AUX_SC1);
      lds_signal(L, FL_QP, lane, 1u);
      STAMP(2);
      if (W1E == 2) w1_issue();
    }
    auto q_gather = [&]() {
      const int off = WS_G + (kvh * 8 + b) * 8192 + ((hw - 2) * 64 + lane) * 16;
      doze_until(tstart + QT);
      v4u gq[4];
      const v4u gt = LD128(rws, WS_T, 0, AUX_SC1);
      gather<4, 2048, 2, QSLEEP>(gq, rws, off, tagl, 0u, ~0ull);
      FL[FL_TG] = (gt.z == tagl && gt.w == tagh) ? gt.x + (unsigned)(GOFF) : tstart + (unsigned)(GOFF);
      const v4f qv = (unpack28(gq[0]) + unpack28(gq[1])) + (unpack28(gq[2]) + unpack28(gq[3]));
      *(LAS v2u*)(L + L_Q + ((hw - 2) * 64 + lane) * 8) = v2u{pk2(qv.x, qv.y), pk2(qv.z, qv.w)};
      FL[FL_TAGL] = tagl; FL[FL_TAGH] = tagh;
      lds_signal(L, FL_Q, lane, 2u);
      STAMP(3);
    };
    if (isM) {
      lds_wait_ge(FL + FL_QP, 4u, 1);
#pragma unroll
      for (int i = 0; i < 14; ++i) *(LAS v4u*)(L + L_RAW + (i * 4 + hw) * 1024 + lane * 16) = t1[i];
    }
    if (hw >= 2) q_gather();
    lds_wait_ge(FL + FL_Q, 4u, 1);
    lds_wait_ge(FL + FL_QP, 4u, 1);
    v4u Fr[28];
    v4f cx, cb;
    {
      const v2u bv = __builtin_nontemporal_load((const GAS v2u*)(isW ? b2 + r5 * 16 + (lane & 3) * 4 : b1 + (isM ? m : 0) * 64 + hw * 16 + g * 4));
      const v2u xv = __builtin_amdgcn_raw_buffer_load_b64(rx, (kvh * D + r5 * 16 + (lane & 3) * 4) * 2, 0, AUX_NT);
      cb = v4f{bflo(bv.x), bfhi(bv.x), bflo(bv.y), bfhi(bv.y)};
      cx = v4f{bflo(xv.x), bfhi(xv.x), bflo(xv.y), bfhi(xv.y)};
    }
#pragma unroll
    for (int i = 0; i < 28; ++i) Fr[i] = v4u{0u, 0u, 0u, 0u};
    if (isW) {
      doze_until(tstart + WOT + r5 * WOS);
      const __amdgpu_buffer_rsrc_t rw = __builtin_amdgcn_make_buffer_rsrc((void*)(wo + (kvh * 512 + kq * 128) * D + t * 64), 0, 127 * (D * 2) + 128, RSRC3);
      v4u tt[4];
#pragma unroll
      for (int i = 0; i < 4; ++i) tt[i] = LD128(rw, (lane >> 3) * (D * 2) + (lane & 7) * 16, (i * 4 + hw) * 8 * (D * 2), AUX_NT);
#pragma unroll
      for (int i = 0; i < 4; ++i) *(LAS v4u*)(L + L_RAW + (i * 4 + hw) * 1024 + lane * 16) = tt[i];
      lds_signal(L, FL_RAW, lane, 1u);
      lds_wait_ge(FL + FL_RAW, 4u, 1);
#pragma unroll
      for (int ks = 0; ks < 4; ++ks) Fr[ks] = frag_tr(ap + ks * 4096, 512);
    } else if (isM) {
      lds_signal(L, FL_RAW, lane, 1u);
      lds_wait_ge(FL + FL_RAW, 4u, 1);
#pragma unroll
      for (int ks = 0; ks < 14; ++ks) Fr[ks] = frag_tr(ap + ks * 4096, 512);
      lds_signal(L, FL_RAW2, lane, 1u);
    }
    asm volatile("" :: "v"(cpf));
    STAMP(4);
    const __amdgpu_buffer_rsrc_t rt = __builtin_amdgcn_make_buffer_rsrc((void*)(ws + (size_t)(ROT ? (tagh & 31u) + 1u : 0u) * WS_SPAN), 0, WS_END, RSRC3);
    auto stage_w2 = [&]() {
      const __amdgpu_buffer_rsrc_t r2 = __builtin_amdgcn_make_buffer_rsrc((void*)(w2 + m * 64 * D), 0, 64 * D * 2, RSRC3);
      v4u uu[14];
#pragma unroll
      for (int i = 0; i < 14; ++i) uu[i] = LD128(r2, lane * 16, (i * 4 + hw) * 1024, AUX_NT);
      lds_wait_ge(FL + FL_RAW2, 4u, 0);
#pragma unroll
      for (int i = 0; i < 14; ++i) *(LAS v4u*)(L + L_RAW + (i * 4 + hw) * 1024 + lane * 16) = uu[i];
      lds_signal(L, FL_RAW3, lane, 1u);
      lds_wait_ge(FL + FL_RAW3, 4u, 0);
      const LAS unsigned char* a2 = L + L_RAW + (g * 8 + ((lane & 15) >> 2)) * 896 + hw * 224 + (lane & 3) * 8;
#pragma unroll
      for (int i = 0; i < 14; ++i) Fr[14 + i] = frag_tr(a2 + (i & 1) * 32 * 896 + (i >> 1) * 32, 4 * 896);
    };
    float fw[4] = {0.f, 0.f, 0.f, 0.f};
    if (hw < 2) {
      lds_wait_ge(FL + FL_ML, 4u, 1);
      const v2f ml = *(const LAS v2f*)(L + L_ML + ((lane & 3) * 2 + hw) * 8);
      float Ms = ml.x;
      Ms = fmaxf(Ms, dpp<XOR1>(Ms)); Ms = fmaxf(Ms, dpp<XOR2>(Ms));
      const float Msub = Ms == -INFINITY ? 0.f : Ms;
      float Lw = __builtin_amdgcn_exp2f((ml.x - Msub) * SC2) * ml.y;
      Lw += dpp<XOR1>(Lw); Lw += dpp<XOR2>(Lw);
      ST128((v4u{__float_as_uint(Ms), __float_as_uint(Lw), tagl, tagh}), rt, lane == 0 ? WS_ML + ((grp * 2 + hw) * 4 + s) * 16 : WS_END, 0, AUX_SC1);
      const int off = WS_ML + ((grp * 2 + hw) * 4 + (lane & 3)) * 16;
      v4u gm1[1];
      __builtin_amdgcn_s_sleep(96); __builtin_amdgcn_s_sleep(96);
      gather<1, 0, 1, 64>(gm1, rt, off, tagl, tagh, ~0ull);
      const v4u gm = gm1[0];
      float M = __uint_as_float(gm.x);
      M = fmaxf(M, dpp<XOR1>(M)); M = fmaxf(M, dpp<XOR2>(M));
      float Ls = __builtin_amdgcn_exp2f((__uint_as_float(gm.x) - M) * SC2) * __uint_as_float(gm.y);
      Ls += dpp<XOR1>(Ls); Ls += dpp<XOR2>(Ls);
      const float f = __builtin_amdgcn_exp2f((ml.x - M) * SC2) / Ls;
#pragma unroll
      for (int i = 0; i < 4; ++i) fw[i] = __builtin_bit_cast(float, __builtin_amdgcn_readlane(__builtin_bit_cast(int, f), i));
    }
    STAMP(5);
    const unsigned tg = __builtin_amdgcn_readfirstlane(FL[FL_TG]);
    asm volatile("" : "+v"(cx), "+v"(cb));
    int p_p0 = WS_P0 + ((kvh * 8 + b) * 4 + s) * 2048 + (hw & 1) * 1024 + lane * 16;
    int p_st = isW ? ((lane & 8) == 0 ? WS_PW + (((kvh * 4 + kq) * 28 + t * 4 + hw) * 8 + (lane & 7)) * 64 + g * 16 : WS_END)
                   : WS_PF + ((m * 28 + hw * 7) * 8 + (lane & 15)) * 64 + g * 16;
    int p_h = lane < 4 ? WS_H + (kvh * 112 + r5 * 4 + lane) * 16 : WS_END;
    int g_p0 = WS_P0 + (kvh * 8 + (lane >> 3)) * 8192 + (kq * 32 + hw * 8 + (lane & 7)) * 16;
    int g_pw = WS_PW + (((lane >> 2) * 28 + r5) * 8 + kvh) * 64 + (lane & 3) * 16;
    int g_o = lane < 4 ? (kvh * D + r5 * 16 + lane * 4) * 2 : 8 * D * 2;
    int g_e = (w == 0 && lane == 0) ? WS_EPOCH : WS_END;
    int a_bw = isW ? L_BF + hw * 512 + lane * 8 : L_BF + (lane & 15) * 144 + (hw * 16 + g * 4) * 2;
    int a_br = isW ? L_BF + (lane & 7) * 64 + g * 16 : L_BF + (lane & 15) * 144 + g * 16;
    asm volatile("" : "+v"(p_p0), "+v"(p_st), "+v"(p_h), "+v"(g_p0), "+v"(g_pw), "+v"(g_o), "+v"(g_e), "+v"(a_bw), "+v"(a_br));
    const __amdgpu_buffer_rsrc_t rout = __builtin_amdgcn_make_buffer_rsrc((void*)out, 0, 8 * D * 2, RSRC3);
    lds_wait_ge(FL + FL_PRE, 4u, 1);
    STAMP(6);
    if (hw == 3) { const unsigned z = code_prefetch(); asm volatile("" :: "v"(z)); }
    lds_wait_ge(FL + FL_DONE, 4u, 0);
    if (hw < 2) {
      STAMP(7);
      const LAS v4f* mp = (const LAS v4f*)(L + L_MRG + hw * 1024 + lane * 16);
      const v4f o = (mp[0] * fw[0] + mp[128] * fw[1]) + (mp[256] * fw[2] + mp[384] * fw[3]);
      ST128(pack24(o, tagl), rt, p_p0, 0, AUX_SC1);
      STAMP(8);
    }
    if (isW) {
      __builtin_amdgcn_s_sleep(WDELAY);
      wait_until(tg + SCHED63 + WT);
      v4u pv[4];
      gather<4, 2048, 0, SLPT>(pv, rt, g_p0, tagl, 0u, ~0ull);
      STAMP(9);
      const v4f o = (unpack24(pv[0]) + unpack24(pv[1])) + (unpack24(pv[2]) + unpack24(pv[3]));
      *(LAS v2u*)(L + a_bw) = v2u{pk2(o.x, o.y), pk2(o.z, o.w)};
      BAR();
      v4u bf[4];
#pragma unroll
      for (int ks = 0; ks < 4; ++ks) bf[ks] = *(const LAS v4u*)(L + a_br + ks * 512);
      SB();
      v4f d0 = {0.f, 0.f, 0.f, 0.f}, d1 = {0.f, 0.f, 0.f, 0.f};
      d0 = mfma(Fr[0], bf[0], d0); d1 = mfma(Fr[1], bf[1], d1); d0 = mfma(Fr[2], bf[2], d0); d1 = mfma(Fr[3], bf[3], d1);
      ST128(pack24(d0 + d1, tagl), rt, p_st, 0, AUX_SC1);
      STAMP(10);
      if (hw == 0) {
        v4u u[2];
        __builtin_amdgcn_s_sleep(RDELAY);
        wait_until(tg + SCHED63 + RT);
        gather<2, 16 * 28 * 512, 0, SLPT>(u, rt, g_pw, tagl, 0u, ~0ull);
        STAMP(11);
        const v4f hv = red16(unpack24(u[0]) + unpack24(u[1]));
        const unsigned h01 = pk2o(cx.x + rbf(hv.x), cx.y + rbf(hv.y)), h23 = pk2o(cx.z + rbf(hv.z), cx.w + rbf(hv.w));
        ST128((v4u{h01, h23, tagh, tagl}), rt, p_h, 0, AUX_SC1);
        STAMP(12);
        __builtin_amdgcn_s_sleep(FWAIT);
        wait_until(tg + SCHED63 + FT);
        gather<2, 16 * 28 * 512, 0, SLPT>(u, rt, g_pw + (WS_PF - WS_PW), tagl, 0u, (1ull << 48) - 1ull);
        STAMP(13);
        const v4f ov = red16(unpack24(u[0]) + unpack24(u[1]));
        const unsigned o01 = pk2(bflo(h01) + rbf(rbf(ov.x) + cb.x), bfhi(h01) + rbf(rbf(ov.y) + cb.y));
        const unsigned o23 = pk2(bflo(h23) + rbf(rbf(ov.z) + cb.z), bfhi(h23) + rbf(rbf(ov.w) + cb.w));
        __builtin_amdgcn_raw_buffer_store_b64(v2u{o01, o23}, rout, g_o, 0, 0);
        __builtin_amdgcn_raw_buffer_store_b64(v2u{tagl, tagh}, rws, g_e, 0, AUX_SC1);
        STAMP(14);
      }
    } else if (isM) {
      stage_w2();
      STAMP(19);
      mlp_ln(L, rt, wv, lane, tagl, tg);
      STAMP(15);
      v4u bf[14];
#pragma unroll
      for (int ks = 0; ks < 14; ++ks) bf[ks] = *(const LAS v4u*)(bp + ks * 64);
      SB();
      v4f d0 = {0.f, 0.f, 0.f, 0.f}, d1 = {0.f, 0.f, 0.f, 0.f};
#pragma unroll
      for (int ks = 0; ks < 14; ks += 2) { d0 = mfma(Fr[ks], bf[ks], d0); d1 = mfma(Fr[ks + 1], bf[ks + 1], d1); }
      d0 += d1;
      const unsigned g01 = pk2o(d0.x, d0.y), g23 = pk2o(d0.z, d0.w);
      const unsigned a01 = pk2o(bflo(g01) + cb.x, bfhi(g01) + cb.y), a23 = pk2o(bflo(g23) + cb.z, bfhi(g23) + cb.w);
      float u[4] = {bflo(a01), bfhi(a01), bflo(a23), bfhi(a23)};
#pragma unroll
      for (int k = 0; k < 4; ++k) { const float a = u[k] > 0.f ? u[k] : 0.f; u[k] = a * a; }
      *(LAS v2u*)(L + a_bw) = v2u{pk2(u[0], u[1]), pk2(u[2], u[3])};
      BAR();
      STAMP(18);
      const v4u h0 = *(const LAS v4u*)(L + a_br), h1 = *(const LAS v4u*)(L + a_br + 64);
      const bool up = lane & 8;
      v4u ev[4];
#pragma unroll
      for (int i = 0; i < 4; ++i) {
        v4f ea = mfma(Fr[14 + 4 * i], h0, v4f{0.f, 0.f, 0.f, 0.f}), eb = ea;
        if (i < 3) eb = mfma(Fr[14 + 4 * i + 2], h0, v4f{0.f, 0.f, 0.f, 0.f});
        ea = mfma(Fr[14 + 4 * i + 1], h1, ea);
        if (i < 3) eb = mfma(Fr[14 + 4 * i + 3], h1, eb);
        ev[i] = pack24(v4f{up ? eb.x : ea.x, up ? eb.y : ea.y, up ? eb.z : ea.z, up ? eb.w : ea.w}, tagl);
        ST128(ev[i], rt, (i == 3 && up) ? WS_END : p_st + i * 1024, 0, AUX_SC1);
      }
      asm volatile("" :: "v"(ev[0]), "v"(ev[1]), "v"(ev[2]), "v"(ev[3]));
      STAMP(16);
    }
  } else {
    const size_t kvoff = ((size_t)grp * SK + (size_t)s * 32) * DH;
    constexpr int TSTR = 65536, NREC = 31 * 65536 + 16384;
    const unsigned gh = ((unsigned)grp * 2654435761u) >> 16;
    const int rot = ROTK == 0 ? 0 : ROTK == 5 || ROTK == 7 || ROTK == 9 ? grp * 5 : ROTK == 6 ? 0 : (int)(gh >> 4);
    const int mul = ROTK == 6 || ROTK == 7 ? 2 * (grp & 15) + 1 : ROTK == 8 ? 2 * (int)(gh & 15u) + 1 : ROTK == 9 ? 2 * (b & 3) + 1 : ROTK == 10 ? 2 * (int)(gh & 3u) + 1 : 1;
#define ROW(n) ((ROTK == 9 || ROTK == 10 ? ((((((n) + rot) >> 2) * mul) & 7) << 2) | (((n) + rot) & 3) : ((n) * mul + rot) & 31) * TSTR)
    const __amdgpu_buffer_rsrc_t rk = __builtin_amdgcn_make_buffer_rsrc((void*)(kc + kvoff), 0, NREC, RSRC3);
    const int voff = lane * 16 + wv * 4096;
    v4u X[NEXTRA * 4][4], R[4][4];
    unsigned tb = (unsigned)__builtin_amdgcn_s_memrealtime();
#define LANDED() asm volatile("s_waitcnt vmcnt(%0)" :: "i"(4 * (LEAD - 1)) : "memory")
    STAMP(0);
    if (wv == 0) *(volatile LAS unsigned*)(L + L_ML) = tb;
    if (!EARLY) BAR();
#pragma unroll
    for (int n = 0; n < NEXTRA * 4 + LEAD; ++n) {
      if (EARLY && n == EARLY) BAR();
      if (n >= LEAD) LANDED();
      if (n >= LEAD) pace(tb, n);
      issue_turn(n < NEXTRA * 4 ? X[n] : R[n - NEXTRA * 4], rk, voff, ROW(n));
    }
    const bool b0 = lane & 1, b1_ = lane & 2, b2_ = lane & 4, b3 = lane & 8;
    const int hf = lane >> 5, c = lane & 31;
    LAS float* P = (LAS float*)(L + L_P) + wv * 512;
    LAS float* Pl = P + ((lane >> 3) & 3) * 16 + hf * 8 + (lane & 7);
    STAMP(1);
    lds_wait_ge(FL + FL_Q, 4u, 0);
    STAMP(2);
    tb = __builtin_amdgcn_readfirstlane(FL[FL_TG]);
    const v4u q0 = *(const LAS v4u*)(L + L_Q + c * 16), q1 = *(const LAS v4u*)(L + L_Q + 512 + c * 16);
    float mx = -INFINITY;
#pragma unroll
    for (int g = 0; g < NEXTRA; ++g) {
      float r[4], rx[4];
#pragma unroll
      for (int d = 0; d < 4; ++d) {
        rx[d] = k_turn(X[4 * g + d], q0, q1, b0, b1_, b2_);
        SB();
        LANDED();
        pace(tb, (NEXTRA + g) * 4 + d + LEAD);
        issue_turn(R[(d + LEAD) & 3], rk, voff, ROW((NEXTRA + g) * 4 + d + LEAD));
        r[d] = k_turn(R[d], q0, q1, b0, b1_, b2_);
        SB();
      }
      const float sx = k_group(rx[0], rx[1], rx[2], rx[3], b3), sc = k_group(r[0], r[1], r[2], r[3], b3);
      mx = fmaxf(mx, fmaxf(sx, sc));
      Pl[g * 64] = sx; Pl[(NEXTRA + g) * 64] = sc;
      SB();
    }
    STAMP(3);
    const __amdgpu_buffer_rsrc_t rv = __builtin_amdgcn_make_buffer_rsrc((void*)(vc + kvoff), 0, NREC, RSRC3);
#pragma unroll 1
    for (int g = 2 * NEXTRA; g < 8; ++g) {
      float r[4];
#pragma unroll
      for (int d = 0; d < 4; ++d) {
        const int nx = g * 4 + d + LEAD;
        const bool tov = (d + LEAD >= 4) && g == 7;
        const __amdgpu_buffer_rsrc_t rn = __builtin_amdgcn_make_buffer_rsrc((void*)((tov ? vc : kc) + kvoff), 0, NREC, RSRC3);
        LANDED();
        pace1(tb, nx);
        issue_turn(R[(d + LEAD) & 3], rn, voff, ROW(nx));
        r[d] = k_turn(R[d], q0, q1, b0, b1_, b2_);
        SB();
      }
      const float sc = k_group(r[0], r[1], r[2], r[3], b3);
      mx = fmaxf(mx, sc);
      Pl[g * 64] = sc;
    }
    STAMP(4);
    mx = fmaxf(mx, dpp<XOR2>(mx)); mx = fmaxf(mx, dpp<ROR4>(mx)); mx = fmaxf(mx, dpp<ROR8>(mx));
    mx = sw16_max(mx); mx = sw32_max(mx);
    const float msub = mx == -INFINITY ? 0.f : mx;
    float ls = 0.f;
    {
      float sv[8];
#pragma unroll
      for (int g = 0; g < 8; ++g) sv[g] = Pl[g * 64];
#pragma unroll
      for (int g = 0; g < 8; ++g) { const float p = __builtin_amdgcn_exp2f((sv[g] - msub) * SC2); ls += p; Pl[g * 64] = p; }
    }
    ls += dpp<XOR2>(ls); ls += dpp<ROR8>(ls); ls += dpp<ROR4>(ls);
    ls = sw16_add(ls, ls); ls = sw32_add(ls, ls);
    *(LAS v2f*)(L + (lane < 2 ? L_ML + (wv * 2 + lane) * 8 : L_SINK + 1024 + lane * 8)) = v2f{mx, ls};
    lds_signal(L, FL_ML, lane, 1u);
    v2f a0[4], a1[4];
#pragma unroll
    for (int k = 0; k < 4; ++k) { a0[k] = v2f{0.f, 0.f}; a1[k] = v2f{0.f, 0.f}; }
    const LAS v4f* Pr = (const LAS v4f*)(P + hf * 8);
    LAS v4f* mo = (LAS v4f*)(L + L_MRG + wv * 2048 + lane * 32);
    auto v_group = [&](int g, auto last) {
      v4f pa[4], pb[4];
#pragma unroll
      for (int d = 0; d < 4; ++d) { pa[d] = Pr[(g * 4 + d) * 4]; pb[d] = Pr[(g * 4 + d) * 4 + 1]; }
#pragma unroll
      for (int d = 0; d < 4; ++d) {
        if (!last.value || d + LEAD < 4) {
          LANDED();
          pace2(tb, 32 + g * 4 + d + LEAD);
          issue_turn(R[(d + LEAD) & 3], rv, voff, ROW(g * 4 + d + LEAD));
        }
        v_turn(R[d], pa[d], pb[d], a0, a1);
        SB();
        if (!last.value && d == 1) lds_signal(L, FL_PRE, lane, g == PREG ? 1u : 0u);
      }
    };
#pragma unroll 1
    for (int g = 0; g < 7; ++g) v_group(g, BoolC<false>{});
    v_group(7, BoolC<true>{});
    mo[0] = v4f{sw32_add(a0[0].x, a1[0].x), sw32_add(a0[0].y, a1[0].y), sw32_add(a0[1].x, a1[1].x), sw32_add(a0[1].y, a1[1].y)};
    mo[1] = v4f{sw32_add(a0[2].x, a1[2].x), sw32_add(a0[2].y, a1[2].y), sw32_add(a0[3].x, a1[3].x), sw32_add(a0[3].y, a1[3].y)};
    lds_signal(L, FL_DONE, lane, 1u);
    STAMP(5);
    if (isM) {
      const unsigned th = __builtin_amdgcn_readfirstlane(FL[FL_TAGH]);
      mlp_ln(L, __builtin_amdgcn_make_buffer_rsrc((void*)(ws + (size_t)(ROT ? (th & 31u) + 1u : 0u) * WS_SPAN), 0, WS_END, RSRC3), wv, lane,
             __builtin_amdgcn_readfirstlane(FL[FL_TAGL]), tb);
    }
  }
}
}

extern "C" void kernel_launch(void* const* d_in, const int* in_sizes, int n_in, void* d_out, int out_size, void* d_ws,
                              size_t ws_size, hipStream_t stream) {
  using namespace fk;
  (void)hipFuncSetAttribute((const void*)fused_decode, hipFuncAttributeMaxDynamicSharedMemorySize, L_END + DBG_STAMPS * 2048);
  hipLaunchKernelGGL(fused_decode, dim3(256), dim3(512), L_END + DBG_STAMPS * 2048, stream, (const u16*)d_in[0], (const u16*)d_in[1],
                     (const u16*)d_in[2], (const u16*)d_in[3], (const u16*)d_in[4], (const u16*)d_in[6], (const u16*)d_in[7],
                     (const u16*)d_in[8], (const u16*)d_in[9], (u16*)d_out, (unsigned char*)d_ws);
}
```

```cpp
#define WT 225
#define G1 100
#define G2 95
#define G3 180
#include <hip/hip_runtime.h>
#include <hip/hip_bf16.h>
#include <stdint.h>

#define DBG_STAMPS 0
#define TAU0 1030
#define ROTK 0
#define TAU1 920
#define TAU 886
#define QT 500
#define QSLEEP 24
#define WOT 900
#define WOS 40
#define GOFF 20
#define WDELAY 6
#define RDELAY 8
#define FWAIT 60
#define PREG 6
#define CODE_BYTES 34944
#define W1E 1
#define ROT 1
#define EARLY 1
#define SLPT 0

namespace fk {
typedef unsigned short u16;
typedef unsigned long long u64;
typedef unsigned v4u __attribute__((ext_vector_type(4)));
typedef unsigned v2u __attribute__((ext_vector_type(2)));
typedef float v4f __attribute__((ext_vector_type(4)));
typedef float v2f __attribute__((ext_vector_type(2)));
typedef __bf16 bf16x2 __attribute__((ext_vector_type(2)));
typedef __bf16 bf16x8 __attribute__((ext_vector_type(8)));
typedef short s16x4 __attribute__((ext_vector_type(4)));
#define LAS __attribute__((address_space(3)))
#define GAS __attribute__((address_space(1)))

constexpr int D = 448, F = 1792, SK = 4096, DH = 256, NQ = 4096;
constexpr int AUX_NT = 2, AUX_SC1 = 16;
constexpr int NEXTRA = 3;
constexpr int LEAD = 2;
constexpr int PN0 = 16;
constexpr int RT = WT + G1, MT = RT + G2, FT = MT + G3;
constexpr int RSRC3 = 0x00020000;
constexpr float SC2 = 0.0625f * 1.4426950408889634f;
struct Spin {
  unsigned n = 0, t0 = 0;
  __device__ __forceinline__ bool expired() {
    if ((++n & 4095u) != 0u) return false;
    const unsigned t = (unsigned)__builtin_amdgcn_s_memrealtime();
    if (n == 4096u) t0 = t;
    return t - t0 > (1u << 27);
  }
};

constexpr int WS_EPOCH = 0;
constexpr int WS_T = 2048;
constexpr int WS_G = 4096;
constexpr int WS_ML = WS_G + 8 * 8 * 4 * 128 * 16;
constexpr int WS_H = WS_ML + 64 * 8 * 16;
constexpr int WS_P0 = WS_H + 16384;
constexpr int WS_PW = WS_P0 + 64 * 4 * 2048;
constexpr int WS_PF = WS_PW + 32 * 28 * 512;
constexpr int WS_END = WS_PF + 28 * 28 * 512;
constexpr int WS_SPAN = 2 << 20;
static_assert(WS_END <= WS_SPAN, "span");
constexpr int WS_DBG = 128 << 20;

constexpr int LN_STR = 912;
constexpr int L_P = 0;
constexpr int L_MRG = 8192;
constexpr int L_Q = 16384;
constexpr int L_FLAG = 17408;
constexpr int L_ML = L_FLAG + 64;
constexpr int L_LN = L_ML + 64;
constexpr int L_BF = L_LN + 8 * LN_STR;
constexpr int L_SINK = L_BF + 4096;
constexpr int L_RAW = L_SINK + 2048;
constexpr int L_END = 96 * 1024;
static_assert(L_RAW + 57344 <= L_END, "lds");
static_assert(L_LN % 16 == 0 && L_BF % 16 == 0 && L_RAW % 16 == 0, "align");
enum { FL_LN = 0, FL_QP = 1, FL_Q = 2, FL_PRE = 3, FL_DONE = 4, FL_RAW = 5, FL_RAW2 = 6, FL_RAW3 = 7, FL_ML = 9, FL_TAGL = 10, FL_TAGH = 11, FL_TG = 12 };

__device__ __forceinline__ float bflo(unsigned v) { return __builtin_bit_cast(float, v << 16); }
__device__ __forceinline__ float bfhi(unsigned v) { return __builtin_bit_cast(float, v & 0xffff0000u); }
__device__ __forceinline__ float rbf(float f) { return (float)(__bf16)f; }
__device__ __forceinline__ unsigned pk2(float lo, float hi) {
  bf16x2 v = {(__bf16)lo, (__bf16)hi};
  return __builtin_bit_cast(unsigned, v);
}
__device__ __forceinline__ unsigned pk2o(float lo, float hi) {
  unsigned p = pk2(lo, hi);
  asm("" : "+v"(p));
  return p;
}
__device__ __forceinline__ float dot2(unsigned a, unsigned b, float c) {
  return __builtin_amdgcn_fdot2_f32_bf16(__builtin_bit_cast(bf16x2, a), __builtin_bit_cast(bf16x2, b), c, false);
}
__device__ __forceinline__ v4f mfma(v4u a, v4u b, v4f c) {
  return __builtin_amdgcn_mfma_f32_16x16x32_bf16(__builtin_bit_cast(bf16x8, a), __builtin_bit_cast(bf16x8, b), c, 0, 0, 0);
}
template <int CTRL> __device__ __forceinline__ float dpp(float x) {
  return __builtin_bit_cast(float, __builtin_amdgcn_mov_dpp(__builtin_bit_cast(int, x), CTRL, 0xf, 0xf, true));
}
constexpr int XOR1 = 0xB1, XOR2 = 0x4E, HMIR = 0x141, ROR4 = 0x124, ROR8 = 0x128;
__device__ __forceinline__ float sw16_add(float a, float b) {
  auto s = __builtin_amdgcn_permlane16_swap(__float_as_uint(a), __float_as_uint(b), false, false);
  return __uint_as_float(s[0]) + __uint_as_float(s[1]);
}
__device__ __forceinline__ float sw32_add(float a, float b) {
  auto s = __builtin_amdgcn_permlane32_swap(__float_as_uint(a), __float_as_uint(b), false, false);
  return __uint_as_float(s[0]) + __uint_as_float(s[1]);
}
__device__ __forceinline__ float sw16_max(float a) {
  auto s = __builtin_amdgcn_permlane16_swap(__float_as_uint(a), __float_as_uint(a), false, false);
  return fmaxf(__uint_as_float(s[0]), __uint_as_float(s[1]));
}
__device__ __forceinline__ float sw32_max(float a) {
  auto s = __builtin_amdgcn_permlane32_swap(__float_as_uint(a), __float_as_uint(a), false, false);
  return fmaxf(__uint_as_float(s[0]), __uint_as_float(s[1]));
}
__device__ __forceinline__ v4u pack24(v4f v, unsigned tag) {
  const unsigned a = __float_as_uint(v.x), b = __float_as_uint(v.y), c = __float_as_uint(v.z), d = __float_as_uint(v.w);
  return v4u{__builtin_amdgcn_perm(b, a, 0x05030201u), __builtin_amdgcn_perm(c, b, 0x06050302u), __builtin_amdgcn_perm(d, c, 0x07060503u), tag};
}
__device__ __forceinline__ v4f unpack24(v4u g) {
  return v4f{__uint_as_float(g.x << 8), __uint_as_float(__builtin_amdgcn_perm(g.y, g.x, 0x0504030Cu)),
             __uint_as_float(__builtin_amdgcn_perm(g.z, g.y, 0x0403020Cu)), __uint_as_float(g.z & 0xFFFFFF00u)};
}
__device__ __forceinline__ v4u pack28(v4f v, unsigned tag) {
  return v4u{(__float_as_uint(v.x) & ~3u) | (tag & 3u), (__float_as_uint(v.y) & ~3u) | ((tag >> 2) & 3u),
             (__float_as_uint(v.z) & ~3u) | ((tag >> 4) & 3u), (__float_as_uint(v.w) & ~3u) | ((tag >> 6) & 3u)};
}
__device__ __forceinline__ v4f unpack28(v4u g) { return __builtin_bit_cast(v4f, g & ~3u); }
__device__ __forceinline__ bool bad28(v4u g, unsigned tag) {
  return (((g.x ^ tag) | (g.y ^ (tag >> 2)) | (g.z ^ (tag >> 4)) | (g.w ^ (tag >> 6))) & 3u) != 0u;
}
#define LGKM0() asm volatile("s_waitcnt lgkmcnt(0)" ::: "memory")
#define CLOB() asm volatile("" ::: "memory")
#define SB() __builtin_amdgcn_sched_barrier(0)
#define BAR() do { asm volatile("s_waitcnt lgkmcnt(0)" ::: "memory"); __builtin_amdgcn_s_barrier(); asm volatile("" ::: "memory"); __builtin_amdgcn_sched_barrier(0); } while (0)
#define LD128(rs, vo, so, aux) __builtin_amdgcn_raw_buffer_load_b128(rs, vo, so, aux)
#define ST128(v, rs, vo, so, aux) __builtin_amdgcn_raw_buffer_store_b128(v, rs, vo, so, aux)

#define STAMP(i) do {} while (0)

__device__ __forceinline__ void lds_wait_ge(volatile LAS unsigned* f, unsigned want, int slp) {
  Spin sp;
#pragma nounroll
  while (*f < want) {
    if (slp) __builtin_amdgcn_s_sleep(8);
    if (sp.expired()) break;
  }
  CLOB();
}
__device__ __forceinline__ void lds_signal(LAS unsigned char* L, int word, int lane, unsigned inc) {
  LGKM0();
  LAS unsigned* p = (LAS unsigned*)(L + (lane == 0 ? L_FLAG + word * 4 : L_SINK + lane * 4));
  __hip_atomic_fetch_add(p, inc, __ATOMIC_RELAXED, __HIP_MEMORY_SCOPE_WORKGROUP);
}
template <int N, int STR, int WIDE, int SLP>
__device__ __forceinline__ void gather(v4u (&v)[N], __amdgpu_buffer_rsrc_t rs, int off, unsigned tl, unsigned th, u64 last_mask) {
  Spin sp;
#pragma nounroll
  for (;;) {
    CLOB();
#pragma unroll
    for (int i = 0; i < N; ++i) v[i] = LD128(rs, off + i * STR, 0, AUX_SC1);
    u64 any = 0ull;
#pragma unroll
    for (int i = 0; i < N; ++i) {
      const u64 bad = __builtin_amdgcn_ballot_w64(WIDE == 2 ? bad28(v[i], tl) : WIDE ? ((v[i].z ^ tl) | (v[i].w ^ th)) != 0u : v[i].w != tl);
      any |= i == N - 1 ? bad & last_mask : bad;
    }
    if (any == 0ull || sp.expired()) break;
    if (SLP) __builtin_amdgcn_s_sleep(SLP);
  }
}
__device__ __forceinline__ void wait_until(unsigned until) {
  asm volatile("8:\n\ts_memrealtime vcc\n\ts_waitcnt lgkmcnt(0)\n\ts_sub_i32 vcc_lo, vcc_lo, %0\n\ts_cmp_lt_i32 vcc_lo, 0\n\ts_cbranch_scc1 8b\n" :: "s"(until) : "scc", "vcc", "memory");
}
__device__ __forceinline__ void doze_until(unsigned until) {
#pragma nounroll
  while ((int)((unsigned)__builtin_amdgcn_s_memrealtime() - until) < 0) __builtin_amdgcn_s_sleep(4);
}
constexpr int PN1 = 34;
constexpr int SCHED63 = (PN0 * TAU0 + (PN1 - PN0) * TAU1 + (63 - PN1) * TAU) >> 4;
__device__ __forceinline__ void pace(unsigned tb, int n) { wait_until(tb + (unsigned)((n < PN0 ? n * TAU0 : PN0 * TAU0 + (n - PN0) * TAU1) >> 4)); }
__device__ __forceinline__ void pace1(unsigned tb, int n) { wait_until(tb + (unsigned)((PN0 * TAU0 + (n - PN0) * TAU1) >> 4)); }
__device__ __forceinline__ void pace2(unsigned tb, int n) { wait_until(tb + (unsigned)((PN0 * TAU0 + (PN1 - PN0) * TAU1 + (n - PN1) * TAU) >> 4)); }
template <bool B> struct BoolC { static constexpr bool value = B; };
__device__ __forceinline__ void issue_turn(v4u* r, __amdgpu_buffer_rsrc_t rs, int voff, int soff) {
#pragma unroll
  for (int i = 0; i < 4; ++i) r[i] = LD128(rs, voff + i * 1024, soff, AUX_NT);
  SB();
}
__device__ __forceinline__ float k_turn(const v4u* k, v4u q0, v4u q1, bool b0, bool b1, bool b2) {
  float x[8];
#pragma unroll
  for (int i = 0; i < 4; ++i) {
    float a0 = dot2(k[i].x, q0.x, 0.f), a1 = dot2(k[i].x, q1.x, 0.f);
    a0 = dot2(k[i].y, q0.y, a0); a1 = dot2(k[i].y, q1.y, a1);
    a0 = dot2(k[i].z, q0.z, a0); a1 = dot2(k[i].z, q1.z, a1);
    a0 = dot2(k[i].w, q0.w, a0); a1 = dot2(k[i].w, q1.w, a1);
    x[2 * i] = a0; x[2 * i + 1] = a1;
  }
  float y[4], z[2];
#pragma unroll
  for (int v = 0; v < 4; ++v) y[v] = (b2 ? x[v + 4] : x[v]) + dpp<HMIR>(b2 ? x[v] : x[v + 4]);
#pragma unroll
  for (int v = 0; v < 2; ++v) z[v] = (b1 ? y[v + 2] : y[v]) + dpp<XOR2>(b1 ? y[v] : y[v + 2]);
  return (b0 ? z[1] : z[0]) + dpp<XOR1>(b0 ? z[0] : z[1]);
}
__device__ __forceinline__ float k_group(float r0, float r1, float r2, float r3, bool b3) {
  const float u0 = (b3 ? r1 : r0) + dpp<ROR8>(b3 ? r0 : r1);
  const float u1 = (b3 ? r3 : r2) + dpp<ROR8>(b3 ? r2 : r3);
  return rbf(sw16_add(u0, u1));
}
__device__ __forceinline__ void v_turn(const v4u* v, v4f pa, v4f pb, v2f (&a0)[4], v2f (&a1)[4]) {
#pragma unroll
  for (int i = 0; i < 4; ++i) {
    const float p0 = i == 0 ? pa.x : i == 1 ? pa.z : i == 2 ? pb.x : pb.z;
    const float p1 = i == 0 ? pa.y : i == 1 ? pa.w : i == 2 ? pb.y : pb.w;
#pragma unroll
    for (int j = 0; j < 4; ++j) {
      const v2f e = {bflo(v[i][j]), bfhi(v[i][j])};
      a0[j] = __builtin_elementwise_fma(e, v2f{p0, p0}, a0[j]);
      a1[j] = __builtin_elementwise_fma(e, v2f{p1, p1}, a1[j]);
    }
  }
}
template <bool FULL> __device__ __forceinline__ float row_sum(float v) {
  v += dpp<XOR1>(v); v += dpp<XOR2>(v); v += dpp<HMIR>(v); v += dpp<ROR8>(v);
  v = sw16_add(v, v);
  return FULL ? sw32_add(v, v) : v;
}
template <int NW> __device__ __forceinline__ void ln_row(unsigned (&x)[NW], bool act) {
  v2f v[NW];
#pragma unroll
  for (int k = 0; k < NW; ++k) v[k] = v2f{bflo(x[k]), bfhi(x[k])};
  float mean, var;
  if constexpr (NW == 4) {
    const float c = bflo(__builtin_amdgcn_readfirstlane(x[0]));
    v2f s1 = {0.f, 0.f}, s2 = {0.f, 0.f};
#pragma unroll
    for (int k = 0; k < NW; ++k) { const v2f d = v[k] - c; s1 += d; s2 = __builtin_elementwise_fma(d, d, s2); }
    float t = sw32_add(act ? s1.x + s1.y : 0.f, act ? s2.x + s2.y : 0.f);
    t = row_sum<false>(t) * (1.0f / D);
    const float m1 = __builtin_bit_cast(float, __builtin_amdgcn_readlane(__builtin_bit_cast(int, t), 0));
    const float m2 = __builtin_bit_cast(float, __builtin_amdgcn_readlane(__builtin_bit_cast(int, t), 32));
    mean = c + m1;
    var = fmaxf(fmaf(-m1, m1, m2), m2 - m2);
  } else {
    constexpr unsigned ONES = 0x3F803F80u;
    float sa = 0.f, sb = 0.f;
#pragma unroll
    for (int k = 0; k < NW; k += 2) { sa = dot2(x[k], ONES, sa); sb = dot2(x[k + 1], ONES, sb); }
    mean = row_sum<false>(sa + sb) * (1.0f / D);
    v2f q0 = {0.f, 0.f}, q1 = {0.f, 0.f};
#pragma unroll
    for (int k = 0; k < NW; k += 2) {
      const v2f d0 = v[k] - mean, d1 = v[k + 1] - mean;
      q0 = __builtin_elementwise_fma(d0, d0, q0); q1 = __builtin_elementwise_fma(d1, d1, q1);
    }
    q0 += q1;
    var = row_sum<false>(act ? q0.x + q0.y : 0.f) * (1.0f / D);
  }
  const float mb = rbf(mean);
#pragma unroll
  for (int k = 0; k < NW; ++k) { const v2f d = v[k] - mb; const unsigned p = pk2o(d.x, d.y); v[k] = v2f{bflo(p), bfhi(p)}; }
  const float den = rbf(__builtin_amdgcn_sqrtf(rbf(rbf(var) + 1e-5f)));
  const float ri = __builtin_amdgcn_rcpf(den);
#pragma unroll
  for (int k = 0; k < NW; ++k) { const v2f a = v[k] * ri; x[k] = pk2(a.x, a.y); }
}
__device__ __forceinline__ v4u frag_tr(const LAS unsigned char* p, int hi) {
#if defined(__HIP_DEVICE_COMPILE__)
  const v2u a = __builtin_bit_cast(v2u, __builtin_amdgcn_ds_read_tr16_b64_v4i16((LAS s16x4*)p));
  const v2u b = __builtin_bit_cast(v2u, __builtin_amdgcn_ds_read_tr16_b64_v4i16((LAS s16x4*)(p + hi)));
  return v4u{a.x, a.y, b.x, b.y};
#else
  return v4u{0u, 0u, 0u, 0u};
#endif
}
__device__ __forceinline__ v4f red16(v4f t) {
#pragma unroll
  for (int k = 0; k < 4; ++k) { float a = t[k]; a += dpp<ROR8>(a); a += dpp<ROR4>(a); a = sw16_add(a, a); a = sw32_add(a, a); t[k] = a; }
  return t;
}
__device__ __forceinline__ void mlp_ln(LAS unsigned char* L, __amdgpu_buffer_rsrc_t rt, int row, int lane, unsigned tl, unsigned t0) {
  const bool act = lane < 56;
  const int a = act ? L_LN + row * LN_STR + lane * 8 : L_SINK + lane * 8;
  v4u hg[2];
  int o0 = WS_H + (row * 112 + (act ? lane : 55)) * 16;
  asm volatile("" : "+v"(o0));
  wait_until(t0 + SCHED63 + MT);
  gather<2, 896, 0, SLPT>(hg, rt, o0, tl, 0u, ~0ull);
  { const int wv = row; (void)wv; STAMP(17); }
  unsigned x[4] = {hg[0].x, hg[0].y, hg[1].x, hg[1].y};
  ln_row<4>(x, act);
  *(LAS v2u*)(L + a) = v2u{x[0], x[1]}; *(LAS v2u*)(L + a + 448) = v2u{x[2], x[3]};
  BAR();
}

__global__ void __launch_bounds__(512, 2)
fused_decode(const u16* __restrict__ x, const u16* __restrict__ kc, const u16* __restrict__ vc, const u16* __restrict__ wq,
             const u16* __restrict__ wo, const u16* __restrict__ w1, const u16* __restrict__ b1, const u16* __restrict__ w2,
             const u16* __restrict__ b2, u16* __restrict__ out, unsigned char* __restrict__ ws) {
  extern __shared__ __attribute__((aligned(16))) unsigned char smem[];
  LAS unsigned char* L = (LAS unsigned char*)smem;
  const u64 pc0 = __builtin_amdgcn_s_getpc() & ~0xFFull;
  const int w = blockIdx.x, tid = threadIdx.x, lane = tid & 63, wv = __builtin_amdgcn_readfirstlane(tid >> 6);
  const int kvh = w & 7, r5 = w >> 3, b = r5 & 7, s = r5 >> 3, grp = b * 8 + kvh;
  const int m = (r5 - 28) * 8 + kvh;
  const bool isM = r5 >= 28 && m < 28;
  volatile LAS unsigned* FL = (volatile LAS unsigned*)(L + L_FLAG);
  const __amdgpu_buffer_rsrc_t rws = __builtin_amdgcn_make_buffer_rsrc((void*)ws, 0, WS_END, RSRC3);
  auto code_prefetch = [&]() {
    const __amdgpu_buffer_rsrc_t rc = __builtin_amdgcn_make_buffer_rsrc((void*)pc0, 0, CODE_BYTES, RSRC3);
    constexpr int CPL = (CODE_BYTES / 128 + 31) / 32;
    return __builtin_amdgcn_raw_buffer_load_b32(rc, lane < CPL ? (lane * 32 + r5) * 128 : CODE_BYTES, 0, 0);
  };
  unsigned cpf = 0;
  if (wv == 4) cpf = code_prefetch();
  SB();
  asm volatile("" :: "s"(x), "s"(kc), "s"(vc), "s"(wq), "s"(wo), "s"(w1), "s"(b1), "s"(w2), "s"(b2), "s"(out), "s"(ws));

  if (__builtin_expect(wv >= 4, 0)) {
    const int hw = wv - 4;
    STAMP(0);
    __builtin_amdgcn_s_setprio(3);
    const unsigned tstart = (unsigned)__builtin_amdgcn_s_memrealtime();
    const bool isW = r5 < 28;
    const int t = r5 >> 2, kq = r5 & 3;
    const int g = lane >> 4;
    const LAS unsigned char* ap = L + L_RAW + (g * 8 + ((lane & 15) >> 2)) * 128 + hw * 32 + (lane & 3) * 8;
    const LAS unsigned char* bp = L + L_LN + (lane & 7) * LN_STR + g * 16;
    const __amdgpu_buffer_rsrc_t rx = __builtin_amdgcn_make_buffer_rsrc((void*)x, 0, 8 * D * 2, RSRC3);
    const v2u ep = __builtin_amdgcn_raw_buffer_load_b64(rws, WS_EPOCH, 0, AUX_SC1);
    unsigned tagl, tagh;
#define MAKE_TAGS() do { const unsigned el = __builtin_amdgcn_readfirstlane(ep.x), eh = __builtin_amdgcn_readfirstlane(ep.y); \
      u64 z = (((u64)eh << 32) | el) + 0x9E3779B97F4A7C15ull; \
      z = (z ^ (z >> 30)) * 0xBF58476D1CE4E5B9ull; z = (z ^ (z >> 27)) * 0x94D049BB133111EBull; z ^= z >> 31; \
      tagl = (((unsigned)z ^ el) & 3u) == 0u ? (unsigned)z ^ 1u : (unsigned)z; \
      tagl = ((tagl ^ (tagl >> 2)) & 3u) == 0u ? tagl ^ 4u : tagl;     \
      tagh = ((unsigned)(z >> 32) & ~31u) | ((eh + 1u) & 31u); } while (0)
    if (tid < 256 + 16) FL[tid - 256] = 0u;
    v4u t1[14];
    auto w1_issue = [&]() {
      const __amdgpu_buffer_rsrc_t rw = __builtin_amdgcn_make_buffer_rsrc((void*)(w1 + (isM ? m : 0) * 64), 0, isM ? 447 * (F * 2) + 128 : 0, RSRC3);
      if (isM) {
#pragma unroll
        for (int i = 0; i < 14; ++i) t1[i] = LD128(rw, (lane >> 3) * (F * 2) + (lane & 7) * 16, (i * 4 + hw) * 8 * (F * 2), AUX_NT);
      }
    };
    {
      const int row = hw * 2 + (lane >> 5), c = lane & 31;
      const int ks0 = s < 3 ? s * 4 : 11, nks = s < 2 ? 4 : 3;
      const int xo = c < 28 ? row * (D * 2) + c * 32 : 8 * D * 2;
      const v4u xa = LD128(rx, xo, 0, AUX_NT), xb = LD128(rx, xo + 16, 0, AUX_NT);
      const __amdgpu_buffer_rsrc_t rq = __builtin_amdgcn_make_buffer_rsrc((void*)(wq + ks0 * 32 * NQ + (kvh * 8 + b) * 64), 0, (nks * 32 - 1) * (NQ * 2) + 128, RSRC3);
      v4u tt[4];
#pragma unroll
      for (int i = 0; i < 4; ++i) tt[i] = LD128(rq, (lane >> 3) * (NQ * 2) + (lane & 7) * 16, (i * 4 + hw) * 8 * (NQ * 2), AUX_NT);
      if (W1E == 1) w1_issue();
      SB();
      BAR();
      {
        unsigned xr[8] = {xa.x, xa.y, xa.z, xa.w, xb.x, xb.y, xb.z, xb.w};
        ln_row<8>(xr, c < 28);
        const int a = c < 28 ? L_LN + row * LN_STR + c * 32 : L_SINK + lane * 32;
        *(LAS v4u*)(L + a) = v4u{xr[0], xr[1], xr[2], xr[3]}; *(LAS v4u*)(L + a + 16) = v4u{xr[4], xr[5], xr[6], xr[7]};
      }
      MAKE_TAGS();
      ST128((v4u{*(volatile LAS unsigned*)(L + L_ML), 0u, tagl, tagh}), rws, (w == 0 && tid == 256) ? WS_T : WS_END, 0, AUX_SC1);
      STAMP(1);
#pragma unroll
      for (int i = 0; i < 4; ++i) *(LAS v4u*)(L + L_RAW + (i * 4 + hw) * 1024 + lane * 16) = tt[i];
      lds_signal(L, FL_LN, lane, 1u);
      lds_wait_ge(FL + FL_LN, 4u, 0);
      v4f d0 = {0.f, 0.f, 0.f, 0.f};
#pragma unroll
      for (int ks = 0; ks < 3; ++ks) d0 = mfma(frag_tr(ap + ks * 4096, 512), *(const LAS v4u*)(bp + (ks0 + ks) * 64), d0);
      if (s < 2) d0 = mfma(frag_tr(ap + 3 * 4096, 512), *(const LAS v4u*)(bp + (ks0 + 3) * 64), d0);
      ST128(pack28(d0, tagl), rws, (lane & 8) == 0 ? WS_G + (((kvh * 8 + (lane & 7)) * 4 + s) * 128 + b * 16 + hw * 4 + g) * 16 : WS_END, 0, AUX_SC1);
      lds_signal(L, FL_QP, lane, 1u);
      STAMP(2);
      if (W1E == 2) w1_issue();
    }
    auto q_gather = [&]() {
      const int off = WS_G + (kvh * 8 + b) * 8192 + ((hw - 2) * 64 + lane) * 16;
      doze_until(tstart + QT);
      v4u gq[4];
      const v4u gt = LD128(rws, WS_T, 0, AUX_SC1);
      gather<4, 2048, 2, QSLEEP>(gq, rws, off, tagl, 0u, ~0ull);
      FL[FL_TG] = (gt.z == tagl && gt.w == tagh) ? gt.x + (unsigned)(GOFF) : tstart + (unsigned)(GOFF);
      const v4f qv = (unpack28(gq[0]) + unpack28(gq[1])) + (unpack28(gq[2]) + unpack28(gq[3]));
      *(LAS v2u*)(L + L_Q + ((hw - 2) * 64 + lane) * 8) = v2u{pk2(qv.x, qv.y), pk2(qv.z, qv.w)};
      FL[FL_TAGL] = tagl; FL[FL_TAGH] = tagh;
      lds_signal(L, FL_Q, lane, 2u);
      STAMP(3);
    };
    if (isM) {
      lds_wait_ge(FL + FL_QP, 4u, 1);
#pragma unroll
      for (int i = 0; i < 14; ++i) *(LAS v4u*)(L + L_RAW + (i * 4 + hw) * 1024 + lane * 16) = t1[i];
    }
    if (hw >= 2) q_gather();
    lds_wait_ge(FL + FL_Q, 4u, 1);
    lds_wait_ge(FL + FL_QP, 4u, 1);
    v4u Fr[28];
    v4f cx, cb;
    {
      const v2u bv = __builtin_nontemporal_load((const GAS v2u*)(isW ? b2 + r5 * 16 + (lane & 3) * 4 : b1 + (isM ? m : 0) * 64 + hw * 16 + g * 4));
      const v2u xv = __builtin_amdgcn_raw_buffer_load_b64(rx, (kvh * D + r5 * 16 + (lane & 3) * 4) * 2, 0, AUX_NT);
      cb = v4f{bflo(bv.x), bfhi(bv.x), bflo(bv.y), bfhi(bv.y)};
      cx = v4f{bflo(xv.x), bfhi(xv.x), bflo(xv.y), bfhi(xv.y)};
    }
#pragma unroll
    for (int i = 0; i < 28; ++i) Fr[i] = v4u{0u, 0u, 0u, 0u};
    if (isW) {
      doze_until(tstart + WOT + r5 * WOS);
      const __amdgpu_buffer_rsrc_t rw = __builtin_amdgcn_make_buffer_rsrc((void*)(wo + (kvh * 512 + kq * 128) * D + t * 64), 0, 127 * (D * 2) + 128, RSRC3);
      v4u tt[4];
#pragma unroll
      for (int i = 0; i < 4; ++i) tt[i] = LD128(rw, (lane >> 3) * (D * 2) + (lane & 7) * 16, (i * 4 + hw) * 8 * (D * 2), AUX_NT);
#pragma unroll
      for (int i = 0; i < 4; ++i) *(LAS v4u*)(L + L_RAW + (i * 4 + hw) * 1024 + lane * 16) = tt[i];
      lds_signal(L, FL_RAW, lane, 1u);
      lds_wait_ge(FL + FL_RAW, 4u, 1);
#pragma unroll
      for (int ks = 0; ks < 4; ++ks) Fr[ks] = frag_tr(ap + ks * 4096, 512);
    } else if (isM) {
      lds_signal(L, FL_RAW, lane, 1u);
      lds_wait_ge(FL + FL_RAW, 4u, 1);
#pragma unroll
      for (int ks = 0; ks < 14; ++ks) Fr[ks] = frag_tr(ap + ks * 4096, 512);
      lds_signal(L, FL_RAW2, lane, 1u);
    }
    asm volatile("" :: "v"(cpf));
    STAMP(4);
    const __amdgpu_buffer_rsrc_t rt = __builtin_amdgcn_make_buffer_rsrc((void*)(ws + (size_t)(ROT ? (tagh & 31u) + 1u : 0u) * WS_SPAN), 0, WS_END, RSRC3);
    auto stage_w2 = [&]() {
      const __amdgpu_buffer_rsrc_t r2 = __builtin_amdgcn_make_buffer_rsrc((void*)(w2 + m * 64 * D), 0, 64 * D * 2, RSRC3);
      v4u uu[14];
#pragma unroll
      for (int i = 0; i < 14; ++i) uu[i] = LD128(r2, lane * 16, (i * 4 + hw) * 1024, AUX_NT);
      lds_wait_ge(FL + FL_RAW2, 4u, 0);
#pragma unroll
      for (int i = 0; i < 14; ++i) *(LAS v4u*)(L + L_RAW + (i * 4 + hw) * 1024 + lane * 16) = uu[i];
      lds_signal(L, FL_RAW3, lane, 1u);
      lds_wait_ge(FL + FL_RAW3, 4u, 0);
      const LAS unsigned char* a2 = L + L_RAW + (g * 8 + ((lane & 15) >> 2)) * 896 + hw * 224 + (lane & 3) * 8;
#pragma unroll
      for (int i = 0; i < 14; ++i) Fr[14 + i] = frag_tr(a2 + (i & 1) * 32 * 896 + (i >> 1) * 32, 4 * 896);
    };
    float fw[4] = {0.f, 0.f, 0.f, 0.f};
    if (hw < 2) {
      lds_wait_ge(FL + FL_ML, 4u, 1);
      const v2f ml = *(const LAS v2f*)(L + L_ML + ((lane & 3) * 2 + hw) * 8);
      float Ms = ml.x;
      Ms = fmaxf(Ms, dpp<XOR1>(Ms)); Ms = fmaxf(Ms, dpp<XOR2>(Ms));
      const float Msub = Ms == -INFINITY ? 0.f : Ms;
      float Lw = __builtin_amdgcn_exp2f((ml.x - Msub) * SC2) * ml.y;
      Lw += dpp<XOR1>(Lw); Lw += dpp<XOR2>(Lw);
      ST128((v4u{__float_as_uint(Ms), __float_as_uint(Lw), tagl, tagh}), rt, lane == 0 ? WS_ML + ((grp * 2 + hw) * 4 + s) * 16 : WS_END, 0, AUX_SC1);
      const int off = WS_ML + ((grp * 2 + hw) * 4 + (lane & 3)) * 16;
      v4u gm1[1];
      __builtin_amdgcn_s_sleep(96); __builtin_amdgcn_s_sleep(96);
      gather<1, 0, 1, 64>(gm1, rt, off, tagl, tagh, ~0ull);
      const v4u gm = gm1[0];
      float M = __uint_as_float(gm.x);
      M = fmaxf(M, dpp<XOR1>(M)); M = fmaxf(M, dpp<XOR2>(M));
      float Ls = __builtin_amdgcn_exp2f((__uint_as_float(gm.x) - M) * SC2) * __uint_as_float(gm.y);
      Ls += dpp<XOR1>(Ls); Ls += dpp<XOR2>(Ls);
      const float f = __builtin_amdgcn_exp2f((ml.x - M) * SC2) / Ls;
#pragma unroll
      for (int i = 0; i < 4; ++i) fw[i] = __builtin_bit_cast(float, __builtin_amdgcn_readlane(__builtin_bit_cast(int, f), i));
    }
    STAMP(5);
    const unsigned tg = __builtin_amdgcn_readfirstlane(FL[FL_TG]);
    asm volatile("" : "+v"(cx), "+v"(cb));
    int p_p0 = WS_P0 + ((kvh * 8 + b) * 4 + s) * 2048 + (hw & 1) * 1024 + lane * 16;
    int p_st = isW ? ((lane & 8) == 0 ? WS_PW + (((kvh * 4 + kq) * 28 + t * 4 + hw) * 8 + (lane & 7)) * 64 + g * 16 : WS_END)
                   : WS_PF + ((m * 28 + hw * 7) * 8 + (lane & 15)) * 64 + g * 16;
    int p_h = lane < 4 ? WS_H + (kvh * 112 + r5 * 4 + lane) * 16 : WS_END;
    int g_p0 = WS_P0 + (kvh * 8 + (lane >> 3)) * 8192 + (kq * 32 + hw * 8 + (lane & 7)) * 16;
    int g_pw = WS_PW + (((lane >> 2) * 28 + r5) * 8 + kvh) * 64 + (lane & 3) * 16;
    int g_o = lane < 4 ? (kvh * D + r5 * 16 + lane * 4) * 2 : 8 * D * 2;
    int g_e = (w == 0 && lane == 0) ? WS_EPOCH : WS_END;
    int a_bw = isW ? L_BF + hw * 512 + lane * 8 : L_BF + (lane & 15) * 144 + (hw * 16 + g * 4) * 2;
    int a_br = isW ? L_BF + (lane & 7) * 64 + g * 16 : L_BF + (lane & 15) * 144 + g * 16;
    asm volatile("" : "+v"(p_p0), "+v"(p_st), "+v"(p_h), "+v"(g_p0), "+v"(g_pw), "+v"(g_o), "+v"(g_e), "+v"(a_bw), "+v"(a_br));
    const __amdgpu_buffer_rsrc_t rout = __builtin_amdgcn_make_buffer_rsrc((void*)out, 0, 8 * D * 2, RSRC3);
    lds_wait_ge(FL + FL_PRE, 4u, 1);
    STAMP(6);
    if (hw == 3) { const unsigned z = code_prefetch(); asm volatile("" :: "v"(z)); }
    lds_wait_ge(FL + FL_DONE, 4u, 0);
    if (hw < 2) {
      STAMP(7);
      const LAS v4f* mp = (const LAS v4f*)(L + L_MRG + hw * 1024 + lane * 16);
      const v4f o = (mp[0] * fw[0] + mp[128] * fw[1]) + (mp[256] * fw[2] + mp[384] * fw[3]);
      ST128(pack24(o, tagl), rt, p_p0, 0, AUX_SC1);
      STAMP(8);
    }
    if (isW) {
      __builtin_amdgcn_s_sleep(WDELAY);
      wait_until(tg + SCHED63 + WT);
      v4u pv[4];
      gather<4, 2048, 0, SLPT>(pv, rt, g_p0, tagl, 0u, ~0ull);
      STAMP(9);
      const v4f o = (unpack24(pv[0]) + unpack24(pv[1])) + (unpack24(pv[2]) + unpack24(pv[3]));
      *(LAS v2u*)(L + a_bw) = v2u{pk2(o.x, o.y), pk2(o.z, o.w)};
      BAR();
      v4u bf[4];
#pragma unroll
      for (int ks = 0; ks < 4; ++ks) bf[ks] = *(const LAS v4u*)(L + a_br + ks * 512);
      SB();
      v4f d0 = {0.f, 0.f, 0.f, 0.f}, d1 = {0.f, 0.f, 0.f, 0.f};
      d0 = mfma(Fr[0], bf[0], d0); d1 = mfma(Fr[1], bf[1], d1); d0 = mfma(Fr[2], bf[2], d0); d1 = mfma(Fr[3], bf[3], d1);
      ST128(pack24(d0 + d1, tagl), rt, p_st, 0, AUX_SC1);
      STAMP(10);
      if (hw == 0) {
        v4u u[2];
        __builtin_amdgcn_s_sleep(RDELAY);
        wait_until(tg + SCHED63 + RT);
        gather<2, 16 * 28 * 512, 0, SLPT>(u, rt, g_pw, tagl, 0u, ~0ull);
        STAMP(11);
        const v4f hv = red16(unpack24(u[0]) + unpack24(u[1]));
        const unsigned h01 = pk2o(cx.x + rbf(hv.x), cx.y + rbf(hv.y)), h23 = pk2o(cx.z + rbf(hv.z), cx.w + rbf(hv.w));
        ST128((v4u{h01, h23, tagh, tagl}), rt, p_h, 0, AUX_SC1);
        STAMP(12);
        __builtin_amdgcn_s_sleep(FWAIT);
        wait_until(tg + SCHED63 + FT);
        gather<2, 16 * 28 * 512, 0, SLPT>(u, rt, g_pw + (WS_PF - WS_PW), tagl, 0u, (1ull << 48) - 1ull);
        STAMP(13);
        const v4f ov = red16(unpack24(u[0]) + unpack24(u[1]));
        const unsigned o01 = pk2(bflo(h01) + rbf(rbf(ov.x) + cb.x), bfhi(h01) + rbf(rbf(ov.y) + cb.y));
        const unsigned o23 = pk2(bflo(h23) + rbf(rbf(ov.z) + cb.z), bfhi(h23) + rbf(rbf(ov.w) + cb.w));
        __builtin_amdgcn_raw_buffer_store_b64(v2u{o01, o23}, rout, g_o, 0, 0);
        __builtin_amdgcn_raw_buffer_store_b64(v2u{tagl, tagh}, rws, g_e, 0, AUX_SC1);
        STAMP(14);
      }
    } else if (isM) {
      stage_w2();
      STAMP(19);
      mlp_ln(L, rt, wv, lane, tagl, tg);
      STAMP(15);
      v4u bf[14];
#pragma unroll
      for (int ks = 0; ks < 14; ++ks) bf[ks] = *(const LAS v4u*)(bp + ks * 64);
      SB();
      v4f d0 = {0.f, 0.f, 0.f, 0.f}, d1 = {0.f, 0.f, 0.f, 0.f};
#pragma unroll
      for (int ks = 0; ks < 14; ks += 2) { d0 = mfma(Fr[ks], bf[ks], d0); d1 = mfma(Fr[ks + 1], bf[ks + 1], d1); }
      d0 += d1;
      const unsigned g01 = pk2o(d0.x, d0.y), g23 = pk2o(d0.z, d0.w);
      const unsigned a01 = pk2o(bflo(g01) + cb.x, bfhi(g01) + cb.y), a23 = pk2o(bflo(g23) + cb.z, bfhi(g23) + cb.w);
      float u[4] = {bflo(a01), bfhi(a01), bflo(a23), bfhi(a23)};
#pragma unroll
      for (int k = 0; k < 4; ++k) { const float a = u[k] > 0.f ? u[k] : 0.f; u[k] = a * a; }
      *(LAS v2u*)(L + a_bw) = v2u{pk2(u[0], u[1]), pk2(u[2], u[3])};
      BAR();
      STAMP(18);
      const v4u h0 = *(const LAS v4u*)(L + a_br), h1 = *(const LAS v4u*)(L + a_br + 64);
      const bool up = lane & 8;
      v4u ev[4];
#pragma unroll
      for (int i = 0; i < 4; ++i) {
        v4f ea = mfma(Fr[14 + 4 * i], h0, v4f{0.f, 0.f, 0.f, 0.f}), eb = ea;
        if (i < 3) eb = mfma(Fr[14 + 4 * i + 2], h0, v4f{0.f, 0.f, 0.f, 0.f});
        ea = mfma(Fr[14 + 4 * i + 1], h1, ea);
        if (i < 3) eb = mfma(Fr[14 + 4 * i + 3], h1, eb);
        ev[i] = pack24(v4f{up ? eb.x : ea.x, up ? eb.y : ea.y, up ? eb.z : ea.z, up ? eb.w : ea.w}, tagl);
        ST128(ev[i], rt, (i == 3 && up) ? WS_END : p_st + i * 1024, 0, AUX_SC1);
      }
      asm volatile("" :: "v"(ev[0]), "v"(ev[1]), "v"(ev[2]), "v"(ev[3]));
      STAMP(16);
    }
  } else {
    const size_t kvoff = ((size_t)grp * SK + (size_t)s * 32) * DH;
    constexpr int TSTR = 65536, NREC = 31 * 65536 + 16384;
    const unsigned gh = ((unsigned)grp * 2654435761u) >> 16;
    const int rot = ROTK == 0 ? 0 : ROTK == 5 || ROTK == 7 || ROTK == 9 ? grp * 5 : ROTK == 6 ? 0 : (int)(gh >> 4);
    const int mul = ROTK == 6 || ROTK == 7 ? 2 * (grp & 15) + 1 : ROTK == 8 ? 2 * (int)(gh & 15u) + 1 : ROTK == 9 ? 2 * (b & 3) + 1 : ROTK == 10 ? 2 * (int)(gh & 3u) + 1 : 1;
#define ROW(n) ((ROTK == 9 || ROTK == 10 ? ((((((n) + rot) >> 2) * mul) & 7) << 2) | (((n) + rot) & 3) : ((n) * mul + rot) & 31) * TSTR)
    const __amdgpu_buffer_rsrc_t rk = __builtin_amdgcn_make_buffer_rsrc((void*)(kc + kvoff), 0, NREC, RSRC3);
    const int voff = lane * 16 + wv * 4096;
    v4u X[NEXTRA * 4][4], R[4][4];
    unsigned tb = (unsigned)__builtin_amdgcn_s_memrealtime();
#define LANDED() asm volatile("s_waitcnt vmcnt(%0)" :: "i"(4 * (LEAD - 1)) : "memory")
    STAMP(0);
    if (wv == 0) *(volatile LAS unsigned*)(L + L_ML) = tb;
    if (!EARLY) BAR();
#pragma unroll
    for (int n = 0; n < NEXTRA * 4 + LEAD; ++n) {
      if (EARLY && n == EARLY) BAR();
      if (n >= LEAD) LANDED();
      if (n >= LEAD) pace(tb, n);
      issue_turn(n < NEXTRA * 4 ? X[n] : R[n - NEXTRA * 4], rk, voff, ROW(n));
    }
    const bool b0 = lane & 1, b1_ = lane & 2, b2_ = lane & 4, b3 = lane & 8;
    const int hf = lane >> 5, c = lane & 31;
    LAS float* P = (LAS float*)(L + L_P) + wv * 512;
    LAS float* Pl = P + ((lane >> 3) & 3) * 16 + hf * 8 + (lane & 7);
    STAMP(1);
    lds_wait_ge(FL + FL_Q, 4u, 0);
    STAMP(2);
    tb = __builtin_amdgcn_readfirstlane(FL[FL_TG]);
    const v4u q0 = *(const LAS v4u*)(L + L_Q + c * 16), q1 = *(const LAS v4u*)(L + L_Q + 512 + c * 16);
    float mx = -INFINITY;
#pragma unroll
    for (int g = 0; g < NEXTRA; ++g) {
      float r[4], rx[4];
#pragma unroll
      for (int d = 0; d < 4; ++d) {
        rx[d] = k_turn(X[4 * g + d], q0, q1, b0, b1_, b2_);
        SB();
        LANDED();
        pace(tb, (NEXTRA + g) * 4 + d + LEAD);
        issue_turn(R[(d + LEAD) & 3], rk, voff, ROW((NEXTRA + g) * 4 + d + LEAD));
        r[d] = k_turn(R[d], q0, q1, b0, b1_, b2_);
        SB();
      }
      const float sx = k_group(rx[0], rx[1], rx[2], rx[3], b3), sc = k_group(r[0], r[1], r[2], r[3], b3);
      mx = fmaxf(mx, fmaxf(sx, sc));
      Pl[g * 64] = sx; Pl[(NEXTRA + g) * 64] = sc;
      SB();
    }
    STAMP(3);
    const __amdgpu_buffer_rsrc_t rv = __builtin_amdgcn_make_buffer_rsrc((void*)(vc + kvoff), 0, NREC, RSRC3);
#pragma unroll 1
    for (int g = 2 * NEXTRA; g < 8; ++g) {
      float r[4];
#pragma unroll
      for (int d = 0; d < 4; ++d) {
        const int nx = g * 4 + d + LEAD;
        const bool tov = (d + LEAD >= 4) && g == 7;
        const __amdgpu_buffer_rsrc_t rn = __builtin_amdgcn_make_buffer_rsrc((void*)((tov ? vc : kc) + kvoff), 0, NREC, RSRC3);
        LANDED();
        pace1(tb, nx);
        issue_turn(R[(d + LEAD) & 3], rn, voff, ROW(nx));
        r[d] = k_turn(R[d], q0, q1, b0, b1_, b2_);
        SB();
      }
      const float sc = k_group(r[0], r[1], r[2], r[3], b3);
      mx = fmaxf(mx, sc);
      Pl[g * 64] = sc;
    }
    STAMP(4);
    mx = fmaxf(mx, dpp<XOR2>(mx)); mx = fmaxf(mx, dpp<ROR4>(mx)); mx = fmaxf(mx, dpp<ROR8>(mx));
    mx = sw16_max(mx); mx = sw32_max(mx);
    const float msub = mx == -INFINITY ? 0.f : mx;
    float ls = 0.f;
    {
      float sv[8];
#pragma unroll
      for (int g = 0; g < 8; ++g) sv[g] = Pl[g * 64];
#pragma unroll
      for (int g = 0; g < 8; ++g) { const float p = __builtin_amdgcn_exp2f((sv[g] - msub) * SC2); ls += p; Pl[g * 64] = p; }
    }
    ls += dpp<XOR2>(ls); ls += dpp<ROR8>(ls); ls += dpp<ROR4>(ls);
    ls = sw16_add(ls, ls); ls = sw32_add(ls, ls);
    *(LAS v2f*)(L + (lane < 2 ? L_ML + (wv * 2 + lane) * 8 : L_SINK + 1024 + lane * 8)) = v2f{mx, ls};
    lds_signal(L, FL_ML, lane, 1u);
    v2f a0[4], a1[4];
#pragma unroll
    for (int k = 0; k < 4; ++k) { a0[k] = v2f{0.f, 0.f}; a1[k] = v2f{0.f, 0.f}; }
    const LAS v4f* Pr = (const LAS v4f*)(P + hf * 8);
    LAS v4f* mo = (LAS v4f*)(L + L_MRG + wv * 2048 + lane * 32);
    auto v_group = [&](int g, auto last) {
      v4f pa[4], pb[4];
#pragma unroll
      for (int d = 0; d < 4; ++d) { pa[d] = Pr[(g * 4 + d) * 4]; pb[d] = Pr[(g * 4 + d) * 4 + 1]; }
#pragma unroll
      for (int d = 0; d < 4; ++d) {
        if (!last.value || d + LEAD < 4) {
          LANDED();
          pace2(tb, 32 + g * 4 + d + LEAD);
          issue_turn(R[(d + LEAD) & 3], rv, voff, ROW(g * 4 + d + LEAD));
        }
        v_turn(R[d], pa[d], pb[d], a0, a1);
        SB();
        if (!last.value && d == 1) lds_signal(L, FL_PRE, lane, g == PREG ? 1u : 0u);
      }
    };
#pragma unroll 1
    for (int g = 0; g < 7; ++g) v_group(g, BoolC<false>{});
    v_group(7, BoolC<true>{});
    mo[0] = v4f{sw32_add(a0[0].x, a1[0].x), sw32_add(a0[0].y, a1[0].y), sw32_add(a0[1].x, a1[1].x), sw32_add(a0[1].y, a1[1].y)};
    mo[1] = v4f{sw32_add(a0[2].x, a1[2].x), sw32_add(a0[2].y, a1[2].y), sw32_add(a0[3].x, a1[3].x), sw32_add(a0[3].y, a1[3].y)};
    lds_signal(L, FL_DONE, lane, 1u);
    STAMP(5);
    if (isM) {
      const unsigned th = __builtin_amdgcn_readfirstlane(FL[FL_TAGH]);
      mlp_ln(L, __builtin_amdgcn_make_buffer_rsrc((void*)(ws + (size_t)(ROT ? (th & 31u) + 1u : 0u) * WS_SPAN), 0, WS_END, RSRC3), wv, lane,
             __builtin_amdgcn_readfirstlane(FL[FL_TAGL]), tb);
    }
  }
}
}

extern "C" void kernel_launch(void* const* d_in, const int* in_sizes, int n_in, void* d_out, int out_size, void* d_ws,
                              size_t ws_size, hipStream_t stream) {
  using namespace fk;
  (void)hipFuncSetAttribute((const void*)fused_decode, hipFuncAttributeMaxDynamicSharedMemorySize, L_END + DBG_STAMPS * 2048);
  hipLaunchKernelGGL(fused_decode, dim3(256), dim3(512), L_END + DBG_STAMPS * 2048, stream, (const u16*)d_in[0], (const u16*)d_in[1],
                     (const u16*)d_in[2], (const u16*)d_in[3], (const u16*)d_in[4], (const u16*)d_in[6], (const u16*)d_in[7],
                     (const u16*)d_in[8], (const u16*)d_in[9], (u16*)d_out, (unsigned char*)d_ws);
}
```

```cpp
#define VPRE 2
#define VPT 400
#define TAU0 970
#define NSTAR 10
#define TGT 860
#include <hip/hip_runtime.h>
#include <hip/hip_bf16.h>
#include <stdint.h>

#define DBG_STAMPS 0
#define OGAIN 8
#define VOUT 0
#define SENT_R5 5
#define TAU1 920
#define TAU 886
#define QT 500
#define QSLEEP 24
#define WOT 900
#define WOS 40
#define GOFF 0
#define WT 195
#define G1 92
#define G2 86
#define G3 166
#define WDELAY 6
#define RDELAY 8
#define FWAIT 60
#define PREG 6
#define CODE_BYTES 36096
#define W1E 1
#define ROT 1
#define EARLY 1
#define SLPT 0

namespace fk {
typedef unsigned short u16;
typedef unsigned long long u64;
typedef unsigned v4u __attribute__((ext_vector_type(4)));
typedef unsigned v2u __attribute__((ext_vector_type(2)));
typedef float v4f __attribute__((ext_vector_type(4)));
typedef float v2f __attribute__((ext_vector_type(2)));
typedef __bf16 bf16x2 __attribute__((ext_vector_type(2)));
typedef __bf16 bf16x8 __attribute__((ext_vector_type(8)));
typedef short s16x4 __attribute__((ext_vector_type(4)));
#define LAS __attribute__((address_space(3)))
#define GAS __attribute__((address_space(1)))

constexpr int D = 448, F = 1792, SK = 4096, DH = 256, NQ = 4096;
constexpr int AUX_NT = 2, AUX_SC1 = 16;
constexpr int NEXTRA = 3;
constexpr int LEAD = 2;
constexpr int PN0 = 16;
constexpr int RT = WT + G1, MT = RT + G2, FT = MT + G3;
constexpr int RSRC3 = 0x00020000;
constexpr float SC2 = 0.0625f * 1.4426950408889634f;
struct Spin {
  unsigned n = 0, t0 = 0;
  __device__ __forceinline__ bool expired() {
    if ((++n & 4095u) != 0u) return false;
    const unsigned t = (unsigned)__builtin_amdgcn_s_memrealtime();
    if (n == 4096u) t0 = t;
    return t - t0 > (1u << 27);
  }
};

constexpr int WS_EPOCH = 0;
constexpr int WS_T = 2048;
constexpr int WS_G = 4096;
constexpr int WS_ML = WS_G + 8 * 8 * 4 * 128 * 16;
constexpr int WS_H = WS_ML + 64 * 8 * 16;
constexpr int WS_P0 = WS_H + 16384;
constexpr int WS_PW = WS_P0 + 64 * 4 * 2048;
constexpr int WS_PF = WS_PW + 32 * 28 * 512;
constexpr int WS_END = WS_PF + 28 * 28 * 512;
constexpr int WS_SPAN = 2 << 20;
static_assert(WS_END <= WS_SPAN, "span");
constexpr int WS_DBG = 128 << 20;

constexpr int LN_STR = 912;
constexpr int L_P = 0;
constexpr int L_MRG = 8192;
constexpr int L_Q = 16384;
constexpr int L_FLAG = 17408;
constexpr int L_ML = L_FLAG + 64;
constexpr int L_LN = L_ML + 64;
constexpr int L_BF = L_LN + 8 * LN_STR;
constexpr int L_SINK = L_BF + 4096;
constexpr int L_RAW = L_SINK + 2048;
constexpr int L_END = 96 * 1024;
static_assert(L_RAW + 57344 <= L_END, "lds");
static_assert(L_LN % 16 == 0 && L_BF % 16 == 0 && L_RAW % 16 == 0, "align");
enum { FL_LN = 0, FL_QP = 1, FL_Q = 2, FL_PRE = 3, FL_DONE = 4, FL_RAW = 5, FL_RAW2 = 6, FL_RAW3 = 7, FL_ML = 9, FL_TAGL = 10, FL_TAGH = 11, FL_TG = 12, FL_ORG = 13, FL_TGOK = 14 };

__device__ __forceinline__ float bflo(unsigned v) { return __builtin_bit_cast(float, v << 16); }
__device__ __forceinline__ float bfhi(unsigned v) { return __builtin_bit_cast(float, v & 0xffff0000u); }
__device__ __forceinline__ float rbf(float f) { return (float)(__bf16)f; }
__device__ __forceinline__ unsigned pk2(float lo, float hi) {
  bf16x2 v = {(__bf16)lo, (__bf16)hi};
  return __builtin_bit_cast(unsigned, v);
}
__device__ __forceinline__ unsigned pk2o(float lo, float hi) {
  unsigned p = pk2(lo, hi);
  asm("" : "+v"(p));
  return p;
}
__device__ __forceinline__ float dot2(unsigned a, unsigned b, float c) {
  return __builtin_amdgcn_fdot2_f32_bf16(__builtin_bit_cast(bf16x2, a), __builtin_bit_cast(bf16x2, b), c, false);
}
__device__ __forceinline__ v4f mfma(v4u a, v4u b, v4f c) {
  return __builtin_amdgcn_mfma_f32_16x16x32_bf16(__builtin_bit_cast(bf16x8, a), __builtin_bit_cast(bf16x8, b), c, 0, 0, 0);
}
template <int CTRL> __device__ __forceinline__ float dpp(float x) {
  return __builtin_bit_cast(float, __builtin_amdgcn_mov_dpp(__builtin_bit_cast(int, x), CTRL, 0xf, 0xf, true));
}
constexpr int XOR1 = 0xB1, XOR2 = 0x4E, HMIR = 0x141, ROR4 = 0x124, ROR8 = 0x128;
__device__ __forceinline__ float sw16_add(float a, float b) {
  auto s = __builtin_amdgcn_permlane16_swap(__float_as_uint(a), __float_as_uint(b), false, false);
  return __uint_as_float(s[0]) + __uint_as_float(s[1]);
}
__device__ __forceinline__ float sw32_add(float a, float b) {
  auto s = __builtin_amdgcn_permlane32_swap(__float_as_uint(a), __float_as_uint(b), false, false);
  return __uint_as_float(s[0]) + __uint_as_float(s[1]);
}
__device__ __forceinline__ float sw16_max(float a) {
  auto s = __builtin_amdgcn_permlane16_swap(__float_as_uint(a), __float_as_uint(a), false, false);
  return fmaxf(__uint_as_float(s[0]), __uint_as_float(s[1]));
}
__device__ __forceinline__ float sw32_max(float a) {
  auto s = __builtin_amdgcn_permlane32_swap(__float_as_uint(a), __float_as_uint(a), false, false);
  return fmaxf(__uint_as_float(s[0]), __uint_as_float(s[1]));
}
__device__ __forceinline__ v4u pack24(v4f v, unsigned tag) {
  const unsigned a = __float_as_uint(v.x), b = __float_as_uint(v.y), c = __float_as_uint(v.z), d = __float_as_uint(v.w);
  return v4u{__builtin_amdgcn_perm(b, a, 0x05030201u), __builtin_amdgcn_perm(c, b, 0x06050302u), __builtin_amdgcn_perm(d, c, 0x07060503u), tag};
}
__device__ __forceinline__ v4f unpack24(v4u g) {
  return v4f{__uint_as_float(g.x << 8), __uint_as_float(__builtin_amdgcn_perm(g.y, g.x, 0x0504030Cu)),
             __uint_as_float(__builtin_amdgcn_perm(g.z, g.y, 0x0403020Cu)), __uint_as_float(g.z & 0xFFFFFF00u)};
}
__device__ __forceinline__ v4u pack28(v4f v, unsigned tag) {
  return v4u{(__float_as_uint(v.x) & ~3u) | (tag & 3u), (__float_as_uint(v.y) & ~3u) | ((tag >> 2) & 3u),
             (__float_as_uint(v.z) & ~3u) | ((tag >> 4) & 3u), (__float_as_uint(v.w) & ~3u) | ((tag >> 6) & 3u)};
}
__device__ __forceinline__ v4f unpack28(v4u g) { return __builtin_bit_cast(v4f, g & ~3u); }
__device__ __forceinline__ bool bad28(v4u g, unsigned tag) {
  return (((g.x ^ tag) | (g.y ^ (tag >> 2)) | (g.z ^ (tag >> 4)) | (g.w ^ (tag >> 6))) & 3u) != 0u;
}
#define LGKM0() asm volatile("s_waitcnt lgkmcnt(0)" ::: "memory")
#define CLOB() asm volatile("" ::: "memory")
#define SB() __builtin_amdgcn_sched_barrier(0)
#define BAR() do { asm volatile("s_waitcnt lgkmcnt(0)" ::: "memory"); __builtin_amdgcn_s_barrier(); asm volatile("" ::: "memory"); __builtin_amdgcn_sched_barrier(0); } while (0)
#define LD128(rs, vo, so, aux) __builtin_amdgcn_raw_buffer_load_b128(rs, vo, so, aux)
#define ST128(v, rs, vo, so, aux) __builtin_amdgcn_raw_buffer_store_b128(v, rs, vo, so, aux)

#define STAMP(i) do {} while (0)

__device__ __forceinline__ void lds_wait_ge(volatile LAS unsigned* f, unsigned want, int slp) {
  Spin sp;
#pragma nounroll
  while (*f < want) {
    if (slp) __builtin_amdgcn_s_sleep(8);
    if (sp.expired()) break;
  }
  CLOB();
}
__device__ __forceinline__ void lds_signal(LAS unsigned char* L, int word, int lane, unsigned inc) {
  LGKM0();
  LAS unsigned* p = (LAS unsigned*)(L + (lane == 0 ? L_FLAG + word * 4 : L_SINK + lane * 4));
  __hip_atomic_fetch_add(p, inc, __ATOMIC_RELAXED, __HIP_MEMORY_SCOPE_WORKGROUP);
}
template <int N, int STR, int WIDE, int SLP>
__device__ __forceinline__ void gather(v4u (&v)[N], __amdgpu_buffer_rsrc_t rs, int off, unsigned tl, unsigned th, u64 last_mask) {
  Spin sp;
#pragma nounroll
  for (;;) {
    CLOB();
#pragma unroll
    for (int i = 0; i < N; ++i) v[i] = LD128(rs, off + i * STR, 0, AUX_SC1);
    u64 any = 0ull;
#pragma unroll
    for (int i = 0; i < N; ++i) {
      const u64 bad = __builtin_amdgcn_ballot_w64(WIDE == 2 ? bad28(v[i], tl) : WIDE ? ((v[i].z ^ tl) | (v[i].w ^ th)) != 0u : v[i].w != tl);
      any |= i == N - 1 ? bad & last_mask : bad;
    }
    if (any == 0ull || sp.expired()) break;
    if (SLP) __builtin_amdgcn_s_sleep(SLP);
  }
}
__device__ __forceinline__ void wait_until(unsigned until) {
  asm volatile("8:\n\ts_memrealtime vcc\n\ts_waitcnt lgkmcnt(0)\n\ts_sub_i32 vcc_lo, vcc_lo, %0\n\ts_cmp_lt_i32 vcc_lo, 0\n\ts_cbranch_scc1 8b\n" :: "s"(until) : "scc", "vcc", "memory");
}
__device__ __forceinline__ void doze_until(unsigned until) {
#pragma nounroll
  while ((int)((unsigned)__builtin_amdgcn_s_memrealtime() - until) < 0) __builtin_amdgcn_s_sleep(4);
}
constexpr int PN1 = 34;
constexpr int SCHED63 = (PN0 * TAU0 + (PN1 - PN0) * TAU1 + (63 - PN1) * TAU) >> 4;
__device__ __forceinline__ void pace(unsigned tb, int n) { wait_until(tb + (unsigned)((n < PN0 ? n * TAU0 : PN0 * TAU0 + (n - PN0) * TAU1) >> 4)); }
__device__ __forceinline__ void pace1(unsigned tb, int n) { wait_until(tb + (unsigned)((PN0 * TAU0 + (n - PN0) * TAU1) >> 4)); }
__device__ __forceinline__ void pace2(unsigned tb, int n) { wait_until(tb + (unsigned)((PN0 * TAU0 + (PN1 - PN0) * TAU1 + (n - PN1) * TAU) >> 4)); }
template <bool B> struct BoolC { static constexpr bool value = B; };
__device__ __forceinline__ void issue_turn(v4u* r, __amdgpu_buffer_rsrc_t rs, int voff, int soff) {
#pragma unroll
  for (int i = 0; i < 4; ++i) r[i] = LD128(rs, voff + i * 1024, soff, AUX_NT);
  SB();
}
__device__ __forceinline__ float k_turn(const v4u* k, v4u q0, v4u q1, bool b0, bool b1, bool b2) {
  float x[8];
#pragma unroll
  for (int i = 0; i < 4; ++i) {
    float a0 = dot2(k[i].x, q0.x, 0.f), a1 = dot2(k[i].x, q1.x, 0.f);
    a0 = dot2(k[i].y, q0.y, a0); a1 = dot2(k[i].y, q1.y, a1);
    a0 = dot2(k[i].z, q0.z, a0); a1 = dot2(k[i].z, q1.z, a1);
    a0 = dot2(k[i].w, q0.w, a0); a1 = dot2(k[i].w, q1.w, a1);
    x[2 * i] = a0; x[2 * i + 1] = a1;
  }
  float y[4], z[2];
#pragma unroll
  for (int v = 0; v < 4; ++v) y[v] = (b2 ? x[v + 4] : x[v]) + dpp<HMIR>(b2 ? x[v] : x[v + 4]);
#pragma unroll
  for (int v = 0; v < 2; ++v) z[v] = (b1 ? y[v + 2] : y[v]) + dpp<XOR2>(b1 ? y[v] : y[v + 2]);
  return (b0 ? z[1] : z[0]) + dpp<XOR1>(b0 ? z[0] : z[1]);
}
__device__ __forceinline__ float k_group(float r0, float r1, float r2, float r3, bool b3) {
  const float u0 = (b3 ? r1 : r0) + dpp<ROR8>(b3 ? r0 : r1);
  const float u1 = (b3 ? r3 : r2) + dpp<ROR8>(b3 ? r2 : r3);
  return rbf(sw16_add(u0, u1));
}
__device__ __forceinline__ void v_turn(const v4u* v, v4f pa, v4f pb, v2f (&a0)[4], v2f (&a1)[4]) {
#pragma unroll
  for (int i = 0; i < 4; ++i) {
    const float p0 = i == 0 ? pa.x : i == 1 ? pa.z : i == 2 ? pb.x : pb.z;
    const float p1 = i == 0 ? pa.y : i == 1 ? pa.w : i == 2 ? pb.y : pb.w;
#pragma unroll
    for (int j = 0; j < 4; ++j) {
      const v2f e = {bflo(v[i][j]), bfhi(v[i][j])};
      a0[j] = __builtin_elementwise_fma(e, v2f{p0, p0}, a0[j]);
      a1[j] = __builtin_elementwise_fma(e, v2f{p1, p1}, a1[j]);
    }
  }
}
template <bool FULL> __device__ __forceinline__ float row_sum(float v) {
  v += dpp<XOR1>(v); v += dpp<XOR2>(v); v += dpp<HMIR>(v); v += dpp<ROR8>(v);
  v = sw16_add(v, v);
  return FULL ? sw32_add(v, v) : v;
}
template <int NW> __device__ __forceinline__ void ln_row(unsigned (&x)[NW], bool act) {
  v2f v[NW];
#pragma unroll
  for (int k = 0; k < NW; ++k) v[k] = v2f{bflo(x[k]), bfhi(x[k])};
  float mean, var;
  if constexpr (NW == 4) {
    const float c = bflo(__builtin_amdgcn_readfirstlane(x[0]));
    v2f s1 = {0.f, 0.f}, s2 = {0.f, 0.f};
#pragma unroll
    for (int k = 0; k < NW; ++k) { const v2f d = v[k] - c; s1 += d; s2 = __builtin_elementwise_fma(d, d, s2); }
    float t = sw32_add(act ? s1.x + s1.y : 0.f, act ? s2.x + s2.y : 0.f);
    t = row_sum<false>(t) * (1.0f / D);
    const float m1 = __builtin_bit_cast(float, __builtin_amdgcn_readlane(__builtin_bit_cast(int, t), 0));
    const float m2 = __builtin_bit_cast(float, __builtin_amdgcn_readlane(__builtin_bit_cast(int, t), 32));
    mean = c + m1;
    var = fmaxf(fmaf(-m1, m1, m2), m2 - m2);
  } else {
    constexpr unsigned ONES = 0x3F803F80u;
    float sa = 0.f, sb = 0.f;
#pragma unroll
    for (int k = 0; k < NW; k += 2) { sa = dot2(x[k], ONES, sa); sb = dot2(x[k + 1], ONES, sb); }
    mean = row_sum<false>(sa + sb) * (1.0f / D);
    v2f q0 = {0.f, 0.f}, q1 = {0.f, 0.f};
#pragma unroll
    for (int k = 0; k < NW; k += 2) {
      const v2f d0 = v[k] - mean, d1 = v[k + 1] - mean;
      q0 = __builtin_elementwise_fma(d0, d0, q0); q1 = __builtin_elementwise_fma(d1, d1, q1);
    }
    q0 += q1;
    var = row_sum<false>(act ? q0.x + q0.y : 0.f) * (1.0f / D);
  }
  const float mb = rbf(mean);
#pragma unroll
  for (int k = 0; k < NW; ++k) { const v2f d = v[k] - mb; const unsigned p = pk2o(d.x, d.y); v[k] = v2f{bflo(p), bfhi(p)}; }
  const float den = rbf(__builtin_amdgcn_sqrtf(rbf(rbf(var) + 1e-5f)));
  const float ri = __builtin_amdgcn_rcpf(den);
#pragma unroll
  for (int k = 0; k < NW; ++k) { const v2f a = v[k] * ri; x[k] = pk2(a.x, a.y); }
}
__device__ __forceinline__ v4u frag_tr(const LAS unsigned char* p, int hi) {
#if defined(__HIP_DEVICE_COMPILE__)
  const v2u a = __builtin_bit_cast(v2u, __builtin_amdgcn_ds_read_tr16_b64_v4i16((LAS s16x4*)p));
  const v2u b = __builtin_bit_cast(v2u, __builtin_amdgcn_ds_read_tr16_b64_v4i16((LAS s16x4*)(p + hi)));
  return v4u{a.x, a.y, b.x, b.y};
#else
  return v4u{0u, 0u, 0u, 0u};
#endif
}
__device__ __forceinline__ v4f red16(v4f t) {
#pragma unroll
  for (int k = 0; k < 4; ++k) { float a = t[k]; a += dpp<ROR8>(a); a += dpp<ROR4>(a); a = sw16_add(a, a); a = sw32_add(a, a); t[k] = a; }
  return t;
}
__device__ __forceinline__ void mlp_ln(LAS unsigned char* L, __amdgpu_buffer_rsrc_t rt, int row, int lane, unsigned tl, unsigned t0) {
  const bool act = lane < 56;
  const int a = act ? L_LN + row * LN_STR + lane * 8 : L_SINK + lane * 8;
  v4u hg[2];
  int o0 = WS_H + (row * 112 + (act ? lane : 55)) * 16;
  asm volatile("" : "+v"(o0));
  wait_until(t0 + SCHED63 + MT);
  gather<2, 896, 0, SLPT>(hg, rt, o0, tl, 0u, ~0ull);
  { const int wv = row; (void)wv; STAMP(17); }
  unsigned x[4] = {hg[0].x, hg[0].y, hg[1].x, hg[1].y};
  ln_row<4>(x, act);
  *(LAS v2u*)(L + a) = v2u{x[0], x[1]}; *(LAS v2u*)(L + a + 448) = v2u{x[2], x[3]};
  BAR();
}

__global__ void __launch_bounds__(512, 2)
fused_decode(const u16* __restrict__ x, const u16* __restrict__ kc, const u16* __restrict__ vc, const u16* __restrict__ wq,
             const u16* __restrict__ wo, const u16* __restrict__ w1, const u16* __restrict__ b1, const u16* __restrict__ w2,
             const u16* __restrict__ b2, u16* __restrict__ out, unsigned char* __restrict__ ws) {
  extern __shared__ __attribute__((aligned(16))) unsigned char smem[];
  LAS unsigned char* L = (LAS unsigned char*)smem;
  const u64 pc0 = __builtin_amdgcn_s_getpc() & ~0xFFull;
  const int w = blockIdx.x, tid = threadIdx.x, lane = tid & 63, wv = __builtin_amdgcn_readfirstlane(tid >> 6);
  const int kvh = w & 7, r5 = w >> 3, b = r5 & 7, s = r5 >> 3, grp = b * 8 + kvh;
  const int m = (r5 - 28) * 8 + kvh;
  const bool isM = r5 >= 28 && m < 28;
  volatile LAS unsigned* FL = (volatile LAS unsigned*)(L + L_FLAG);
  const __amdgpu_buffer_rsrc_t rws = __builtin_amdgcn_make_buffer_rsrc((void*)ws, 0, WS_END, RSRC3);
  auto code_prefetch = [&]() {
    const __amdgpu_buffer_rsrc_t rc = __builtin_amdgcn_make_buffer_rsrc((void*)pc0, 0, CODE_BYTES, RSRC3);
    constexpr int CPL = (CODE_BYTES / 128 + 31) / 32;
    return __builtin_amdgcn_raw_buffer_load_b32(rc, lane < CPL ? (lane * 32 + r5) * 128 : CODE_BYTES, 0, 0);
  };
  unsigned cpf = 0;
  if (wv == 4) cpf = code_prefetch();
  SB();
  asm volatile("" :: "s"(x), "s"(kc), "s"(vc), "s"(wq), "s"(wo), "s"(w1), "s"(b1), "s"(w2), "s"(b2), "s"(out), "s"(ws));

  if (__builtin_expect(wv >= 4, 0)) {
    const int hw = wv - 4;
    STAMP(0);
    __builtin_amdgcn_s_setprio(3);
    const unsigned tstart = (unsigned)__builtin_amdgcn_s_memrealtime();
    const bool isW = r5 < 28;
    const int t = r5 >> 2, kq = r5 & 3;
    const int g = lane >> 4;
    const LAS unsigned char* ap = L + L_RAW + (g * 8 + ((lane & 15) >> 2)) * 128 + hw * 32 + (lane & 3) * 8;
    const LAS unsigned char* bp = L + L_LN + (lane & 7) * LN_STR + g * 16;
    const __amdgpu_buffer_rsrc_t rx = __builtin_amdgcn_make_buffer_rsrc((void*)x, 0, 8 * D * 2, RSRC3);
    const v2u ep = __builtin_amdgcn_raw_buffer_load_b64(rws, WS_EPOCH, 0, AUX_SC1);
    unsigned tagl, tagh;
#define MAKE_TAGS() do { const unsigned el = __builtin_amdgcn_readfirstlane(ep.x), eh = __builtin_amdgcn_readfirstlane(ep.y); \
      u64 z = (((u64)eh << 32) | el) + 0x9E3779B97F4A7C15ull; \
      z = (z ^ (z >> 30)) * 0xBF58476D1CE4E5B9ull; z = (z ^ (z >> 27)) * 0x94D049BB133111EBull; z ^= z >> 31; \
      tagl = (((unsigned)z ^ el) & 3u) == 0u ? (unsigned)z ^ 1u : (unsigned)z; \
      tagl = ((tagl ^ (tagl >> 2)) & 3u) == 0u ? tagl ^ 4u : tagl;     \
      tagh = ((unsigned)(z >> 32) & ~31u) | ((eh + 1u) & 31u); } while (0)
    if (tid < 256 + 16) FL[tid - 256] = 0u;
    v4u t1[14];
    auto w1_issue = [&]() {
      const __amdgpu_buffer_rsrc_t rw = __builtin_amdgcn_make_buffer_rsrc((void*)(w1 + (isM ? m : 0) * 64), 0, isM ? 447 * (F * 2) + 128 : 0, RSRC3);
      if (isM) {
#pragma unroll
        for (int i = 0; i < 14; ++i) t1[i] = LD128(rw, (lane >> 3) * (F * 2) + (lane & 7) * 16, (i * 4 + hw) * 8 * (F * 2), AUX_NT);
      }
    };
    {
      const int row = hw * 2 + (lane >> 5), c = lane & 31;
      const int ks0 = s < 3 ? s * 4 : 11, nks = s < 2 ? 4 : 3;
      const int xo = c < 28 ? row * (D * 2) + c * 32 : 8 * D * 2;
      const v4u xa = LD128(rx, xo, 0, AUX_NT), xb = LD128(rx, xo + 16, 0, AUX_NT);
      const __amdgpu_buffer_rsrc_t rq = __builtin_amdgcn_make_buffer_rsrc((void*)(wq + ks0 * 32 * NQ + (kvh * 8 + b) * 64), 0, (nks * 32 - 1) * (NQ * 2) + 128, RSRC3);
      v4u tt[4];
#pragma unroll
      for (int i = 0; i < 4; ++i) tt[i] = LD128(rq, (lane >> 3) * (NQ * 2) + (lane & 7) * 16, (i * 4 + hw) * 8 * (NQ * 2), AUX_NT);
      if (W1E == 1) w1_issue();
      SB();
      BAR();
      {
        unsigned xr[8] = {xa.x, xa.y, xa.z, xa.w, xb.x, xb.y, xb.z, xb.w};
        ln_row<8>(xr, c < 28);
        const int a = c < 28 ? L_LN + row * LN_STR + c * 32 : L_SINK + lane * 32;
        *(LAS v4u*)(L + a) = v4u{xr[0], xr[1], xr[2], xr[3]}; *(LAS v4u*)(L + a + 16) = v4u{xr[4], xr[5], xr[6], xr[7]};
      }
      MAKE_TAGS();
      STAMP(1);
#pragma unroll
      for (int i = 0; i < 4; ++i) *(LAS v4u*)(L + L_RAW + (i * 4 + hw) * 1024 + lane * 16) = tt[i];
      lds_signal(L, FL_LN, lane, 1u);
      lds_wait_ge(FL + FL_LN, 4u, 0);
      v4f d0 = {0.f, 0.f, 0.f, 0.f};
#pragma unroll
      for (int ks = 0; ks < 3; ++ks) d0 = mfma(frag_tr(ap + ks * 4096, 512), *(const LAS v4u*)(bp + (ks0 + ks) * 64), d0);
      if (s < 2) d0 = mfma(frag_tr(ap + 3 * 4096, 512), *(const LAS v4u*)(bp + (ks0 + 3) * 64), d0);
      ST128(pack28(d0, tagl), rws, (lane & 8) == 0 ? WS_G + (((kvh * 8 + (lane & 7)) * 4 + s) * 128 + b * 16 + hw * 4 + g) * 16 : WS_END, 0, AUX_SC1);
      lds_signal(L, FL_QP, lane, 1u);
      STAMP(2);
      if (W1E == 2) w1_issue();
    }
    auto q_gather = [&]() {
      const int off = WS_G + (kvh * 8 + b) * 8192 + ((hw - 2) * 64 + lane) * 16;
      doze_until(tstart + QT);
      v4u gq[4];
      gather<4, 2048, 2, QSLEEP>(gq, rws, off, tagl, 0u, ~0ull);
      const v4f qv = (unpack28(gq[0]) + unpack28(gq[1])) + (unpack28(gq[2]) + unpack28(gq[3]));
      *(LAS v2u*)(L + L_Q + ((hw - 2) * 64 + lane) * 8) = v2u{pk2(qv.x, qv.y), pk2(qv.z, qv.w)};
      FL[FL_TAGL] = tagl; FL[FL_TAGH] = tagh;
      lds_signal(L, FL_Q, lane, 2u);
      STAMP(3);
    };
    if (isM) {
      lds_wait_ge(FL + FL_QP, 4u, 1);
#pragma unroll
      for (int i = 0; i < 14; ++i) *(LAS v4u*)(L + L_RAW + (i * 4 + hw) * 1024 + lane * 16) = t1[i];
    }
    if (hw >= 2) q_gather();
    if (hw == 0 && r5 == SENT_R5) {
      lds_wait_ge(FL + FL_ORG, 1u, 1);
      const v2u og = *(volatile LAS v2u*)(L + L_ML);
      ST128((v4u{og.x, og.y, tagl, tagh}), rws, lane == 0 ? WS_T + kvh * 16 : WS_END, 0, AUX_SC1);
    }
    if (hw == 1) {
      doze_until(tstart + TGT);
      v4u gt[1];
      gather<1, 0, 1, 32>(gt, rws, WS_T + (lane & 7) * 16, tagl, tagh, ~0ull);
      const unsigned t0 = __builtin_amdgcn_readfirstlane(gt[0].x);
      int o = (int)(gt[0].x - t0) * 8 + (int)gt[0].y * OGAIN;
      o += __builtin_amdgcn_mov_dpp(o, XOR1, 0xf, 0xf, true); o += __builtin_amdgcn_mov_dpp(o, XOR2, 0xf, 0xf, true); o += __builtin_amdgcn_mov_dpp(o, HMIR, 0xf, 0xf, true);
      o >>= 6;
      o = o < -1000 ? -1000 : o > 2000 ? 2000 : o;
      FL[FL_TG] = t0 + (unsigned)o + (unsigned)(GOFF);
      lds_signal(L, FL_TGOK, lane, 1u);
      if (VPRE == 1) {
        const unsigned z0 = *(const GAS unsigned*)(vc + ((size_t)grp * SK + (lane & 1) * (SK - 1)) * DH);
        asm volatile("" :: "v"(z0));
      }
    }
    if (VPRE == 2 && hw == 0) {
      doze_until(tstart + VPT);
#define CAS __attribute__((address_space(4)))
      const unsigned z0 = *(const CAS unsigned*)(vc + (size_t)grp * SK * DH), z1 = *(const CAS unsigned*)(vc + ((size_t)grp * SK + SK - 1) * DH);
      const unsigned z2 = *(const CAS unsigned*)(isM ? w2 + m * 64 * D : wo + (kvh * 512 + kq * 128) * D), z3 = *(const CAS unsigned*)(isM ? w2 + (m * 64 + 63) * D : wo + (kvh * 512 + kq * 128 + 127) * D);
      const unsigned z4 = *(const CAS unsigned*)(ws + (size_t)((tagh & 31u) + 1u) * WS_SPAN), z5 = *(const CAS unsigned*)(ws + (size_t)((tagh & 31u) + 2u) * WS_SPAN - 64);
      const unsigned z6 = VOUT ? *(const CAS unsigned*)out : 0u;
      asm volatile("" :: "s"(z0), "s"(z1), "s"(z2), "s"(z3), "s"(z4), "s"(z5), "s"(z6));
    }
    lds_wait_ge(FL + FL_Q, 4u, 1);
    lds_wait_ge(FL + FL_QP, 4u, 1);
    v4u Fr[28];
    v4f cx, cb;
    {
      const v2u bv = __builtin_nontemporal_load((const GAS v2u*)(isW ? b2 + r5 * 16 + (lane & 3) * 4 : b1 + (isM ? m : 0) * 64 + hw * 16 + g * 4));
      const v2u xv = __builtin_amdgcn_raw_buffer_load_b64(rx, (kvh * D + r5 * 16 + (lane & 3) * 4) * 2, 0, AUX_NT);
      cb = v4f{bflo(bv.x), bfhi(bv.x), bflo(bv.y), bfhi(bv.y)};
      cx = v4f{bflo(xv.x), bfhi(xv.x), bflo(xv.y), bfhi(xv.y)};
    }
#pragma unroll
    for (int i = 0; i < 28; ++i) Fr[i] = v4u{0u, 0u, 0u, 0u};
    if (isW) {
      doze_until(tstart + WOT + r5 * WOS);
      const __amdgpu_buffer_rsrc_t rw = __builtin_amdgcn_make_buffer_rsrc((void*)(wo + (kvh * 512 + kq * 128) * D + t * 64), 0, 127 * (D * 2) + 128, RSRC3);
      v4u tt[4];
#pragma unroll
      for (int i = 0; i < 4; ++i) tt[i] = LD128(rw, (lane >> 3) * (D * 2) + (lane & 7) * 16, (i * 4 + hw) * 8 * (D * 2), AUX_NT);
#pragma unroll
      for (int i = 0; i < 4; ++i) *(LAS v4u*)(L + L_RAW + (i * 4 + hw) * 1024 + lane * 16) = tt[i];
      lds_signal(L, FL_RAW, lane, 1u);
      lds_wait_ge(FL + FL_RAW, 4u, 1);
#pragma unroll
      for (int ks = 0; ks < 4; ++ks) Fr[ks] = frag_tr(ap + ks * 4096, 512);
    } else if (isM) {
      lds_signal(L, FL_RAW, lane, 1u);
      lds_wait_ge(FL + FL_RAW, 4u, 1);
#pragma unroll
      for (int ks = 0; ks < 14; ++ks) Fr[ks] = frag_tr(ap + ks * 4096, 512);
      lds_signal(L, FL_RAW2, lane, 1u);
    }
    asm volatile("" :: "v"(cpf));
    STAMP(4);
    const __amdgpu_buffer_rsrc_t rt = __builtin_amdgcn_make_buffer_rsrc((void*)(ws + (size_t)(ROT ? (tagh & 31u) + 1u : 0u) * WS_SPAN), 0, WS_END, RSRC3);
    auto stage_w2 = [&]() {
      const __amdgpu_buffer_rsrc_t r2 = __builtin_amdgcn_make_buffer_rsrc((void*)(w2 + m * 64 * D), 0, 64 * D * 2, RSRC3);
      v4u uu[14];
#pragma unroll
      for (int i = 0; i < 14; ++i) uu[i] = LD128(r2, lane * 16, (i * 4 + hw) * 1024, AUX_NT);
      lds_wait_ge(FL + FL_RAW2, 4u, 0);
#pragma unroll
      for (int i = 0; i < 14; ++i) *(LAS v4u*)(L + L_RAW + (i * 4 + hw) * 1024 + lane * 16) = uu[i];
      lds_signal(L, FL_RAW3, lane, 1u);
      lds_wait_ge(FL + FL_RAW3, 4u, 0);
      const LAS unsigned char* a2 = L + L_RAW + (g * 8 + ((lane & 15) >> 2)) * 896 + hw * 224 + (lane & 3) * 8;
#pragma unroll
      for (int i = 0; i < 14; ++i) Fr[14 + i] = frag_tr(a2 + (i & 1) * 32 * 896 + (i >> 1) * 32, 4 * 896);
    };
    float fw[4] = {0.f, 0.f, 0.f, 0.f};
    if (hw < 2) {
      lds_wait_ge(FL + FL_ML, 4u, 1);
      const v2f ml = *(const LAS v2f*)(L + L_ML + ((lane & 3) * 2 + hw) * 8);
      float Ms = ml.x;
      Ms = fmaxf(Ms, dpp<XOR1>(Ms)); Ms = fmaxf(Ms, dpp<XOR2>(Ms));
      const float Msub = Ms == -INFINITY ? 0.f : Ms;
      float Lw = __builtin_amdgcn_exp2f((ml.x - Msub) * SC2) * ml.y;
      Lw += dpp<XOR1>(Lw); Lw += dpp<XOR2>(Lw);
      ST128((v4u{__float_as_uint(Ms), __float_as_uint(Lw), tagl, tagh}), rt, lane == 0 ? WS_ML + ((grp * 2 + hw) * 4 + s) * 16 : WS_END, 0, AUX_SC1);
      const int off = WS_ML + ((grp * 2 + hw) * 4 + (lane & 3)) * 16;
      v4u gm1[1];
      __builtin_amdgcn_s_sleep(96); __builtin_amdgcn_s_sleep(96);
      gather<1, 0, 1, 64>(gm1, rt, off, tagl, tagh, ~0ull);
      const v4u gm = gm1[0];
      float M = __uint_as_float(gm.x);
      M = fmaxf(M, dpp<XOR1>(M)); M = fmaxf(M, dpp<XOR2>(M));
      float Ls = __builtin_amdgcn_exp2f((__uint_as_float(gm.x) - M) * SC2) * __uint_as_float(gm.y);
      Ls += dpp<XOR1>(Ls); Ls += dpp<XOR2>(Ls);
      const float f = __builtin_amdgcn_exp2f((ml.x - M) * SC2) / Ls;
#pragma unroll
      for (int i = 0; i < 4; ++i) fw[i] = __builtin_bit_cast(float, __builtin_amdgcn_readlane(__builtin_bit_cast(int, f), i));
    }
    STAMP(5);
    lds_wait_ge(FL + FL_TGOK, 1u, 1);
    const unsigned tg = __builtin_amdgcn_readfirstlane(FL[FL_TG]);
    asm volatile("" : "+v"(cx), "+v"(cb));
    int p_p0 = WS_P0 + ((kvh * 8 + b) * 4 + s) * 2048 + (hw & 1) * 1024 + lane * 16;
    int p_st = isW ? ((lane & 8) == 0 ? WS_PW + (((kvh * 4 + kq) * 28 + t * 4 + hw) * 8 + (lane & 7)) * 64 + g * 16 : WS_END)
                   : WS_PF + ((m * 28 + hw * 7) * 8 + (lane & 15)) * 64 + g * 16;
    int p_h = lane < 4 ? WS_H + (kvh * 112 + r5 * 4 + lane) * 16 : WS_END;
    int g_p0 = WS_P0 + (kvh * 8 + (lane >> 3)) * 8192 + (kq * 32 + hw * 8 + (lane & 7)) * 16;
    int g_pw = WS_PW + (((lane >> 2) * 28 + r5) * 8 + kvh) * 64 + (lane & 3) * 16;
    int g_o = lane < 4 ? (kvh * D + r5 * 16 + lane * 4) * 2 : 8 * D * 2;
    int g_e = (w == 0 && lane == 0) ? WS_EPOCH : WS_END;
    int a_bw = isW ? L_BF + hw * 512 + lane * 8 : L_BF + (lane & 15) * 144 + (hw * 16 + g * 4) * 2;
    int a_br = isW ? L_BF + (lane & 7) * 64 + g * 16 : L_BF + (lane & 15) * 144 + g * 16;
    asm volatile("" : "+v"(p_p0), "+v"(p_st), "+v"(p_h), "+v"(g_p0), "+v"(g_pw), "+v"(g_o), "+v"(g_e), "+v"(a_bw), "+v"(a_br));
    const __amdgpu_buffer_rsrc_t rout = __builtin_amdgcn_make_buffer_rsrc((void*)out, 0, 8 * D * 2, RSRC3);
    lds_wait_ge(FL + FL_PRE, 4u, 1);
    STAMP(6);
    if (hw == 3) { const unsigned z = code_prefetch(); asm volatile("" :: "v"(z)); }
    lds_wait_ge(FL + FL_DONE, 4u, 0);
    if (hw < 2) {
      STAMP(7);
      const LAS v4f* mp = (const LAS v4f*)(L + L_MRG + hw * 1024 + lane * 16);
      const v4f o = (mp[0] * fw[0] + mp[128] * fw[1]) + (mp[256] * fw[2] + mp[384] * fw[3]);
      ST128(pack24(o, tagl), rt, p_p0, 0, AUX_SC1);
      STAMP(8);
    }
    if (isW) {
      __builtin_amdgcn_s_sleep(WDELAY);
      wait_until(tg + SCHED63 + WT);
      v4u pv[4];
      gather<4, 2048, 0, SLPT>(pv, rt, g_p0, tagl, 0u, ~0ull);
      STAMP(9);
      const v4f o = (unpack24(pv[0]) + unpack24(pv[1])) + (unpack24(pv[2]) + unpack24(pv[3]));
      *(LAS v2u*)(L + a_bw) = v2u{pk2(o.x, o.y), pk2(o.z, o.w)};
      BAR();
      v4u bf[4];
#pragma unroll
      for (int ks = 0; ks < 4; ++ks) bf[ks] = *(const LAS v4u*)(L + a_br + ks * 512);
      SB();
      v4f d0 = {0.f, 0.f, 0.f, 0.f}, d1 = {0.f, 0.f, 0.f, 0.f};
      d0 = mfma(Fr[0], bf[0], d0); d1 = mfma(Fr[1], bf[1], d1); d0 = mfma(Fr[2], bf[2], d0); d1 = mfma(Fr[3], bf[3], d1);
      ST128(pack24(d0 + d1, tagl), rt, p_st, 0, AUX_SC1);
      STAMP(10);
      if (hw == 0) {
        v4u u[2];
        __builtin_amdgcn_s_sleep(RDELAY);
        wait_until(tg + SCHED63 + RT);
        gather<2, 16 * 28 * 512, 0, SLPT>(u, rt, g_pw, tagl, 0u, ~0ull);
        STAMP(11);
        const v4f hv = red16(unpack24(u[0]) + unpack24(u[1]));
        const unsigned h01 = pk2o(cx.x + rbf(hv.x), cx.y + rbf(hv.y)), h23 = pk2o(cx.z + rbf(hv.z), cx.w + rbf(hv.w));
        ST128((v4u{h01, h23, tagh, tagl}), rt, p_h, 0, AUX_SC1);
        STAMP(12);
        __builtin_amdgcn_s_sleep(FWAIT);
        wait_until(tg + SCHED63 + FT);
        gather<2, 16 * 28 * 512, 0, SLPT>(u, rt, g_pw + (WS_PF - WS_PW), tagl, 0u, (1ull << 48) - 1ull);
        STAMP(13);
        const v4f ov = red16(unpack24(u[0]) + unpack24(u[1]));
        const unsigned o01 = pk2(bflo(h01) + rbf(rbf(ov.x) + cb.x), bfhi(h01) + rbf(rbf(ov.y) + cb.y));
        const unsigned o23 = pk2(bflo(h23) + rbf(rbf(ov.z) + cb.z), bfhi(h23) + rbf(rbf(ov.w) + cb.w));
        __builtin_amdgcn_raw_buffer_store_b64(v2u{o01, o23}, rout, g_o, 0, 0);
        __builtin_amdgcn_raw_buffer_store_b64(v2u{tagl, tagh}, rws, g_e, 0, AUX_SC1);
        STAMP(14);
      }
    } else if (isM) {
      stage_w2();
      STAMP(19);
      mlp_ln(L, rt, wv, lane, tagl, tg);
      STAMP(15);
      v4u bf[14];
#pragma unroll
      for (int ks = 0; ks < 14; ++ks) bf[ks] = *(const LAS v4u*)(bp + ks * 64);
      SB();
      v4f d0 = {0.f, 0.f, 0.f, 0.f}, d1 = {0.f, 0.f, 0.f, 0.f};
#pragma unroll
      for (int ks = 0; ks < 14; ks += 2) { d0 = mfma(Fr[ks], bf[ks], d0); d1 = mfma(Fr[ks + 1], bf[ks + 1], d1); }
      d0 += d1;
      const unsigned g01 = pk2o(d0.x, d0.y), g23 = pk2o(d0.z, d0.w);
      const unsigned a01 = pk2o(bflo(g01) + cb.x, bfhi(g01) + cb.y), a23 = pk2o(bflo(g23) + cb.z, bfhi(g23) + cb.w);
      float u[4] = {bflo(a01), bfhi(a01), bflo(a23), bfhi(a23)};
#pragma unroll
      for (int k = 0; k < 4; ++k) { const float a = u[k] > 0.f ? u[k] : 0.f; u[k] = a * a; }
      *(LAS v2u*)(L + a_bw) = v2u{pk2(u[0], u[1]), pk2(u[2], u[3])};
      BAR();
      STAMP(18);
      const v4u h0 = *(const LAS v4u*)(L + a_br), h1 = *(const LAS v4u*)(L + a_br + 64);
      const bool up = lane & 8;
      v4u ev[4];
#pragma unroll
      for (int i = 0; i < 4; ++i) {
        v4f ea = mfma(Fr[14 + 4 * i], h0, v4f{0.f, 0.f, 0.f, 0.f}), eb = ea;
        if (i < 3) eb = mfma(Fr[14 + 4 * i + 2], h0, v4f{0.f, 0.f, 0.f, 0.f});
        ea = mfma(Fr[14 + 4 * i + 1], h1, ea);
        if (i < 3) eb = mfma(Fr[14 + 4 * i + 3], h1, eb);
        ev[i] = pack24(v4f{up ? eb.x : ea.x, up ? eb.y : ea.y, up ? eb.z : ea.z, up ? eb.w : ea.w}, tagl);
        ST128(ev[i], rt, (i == 3 && up) ? WS_END : p_st + i * 1024, 0, AUX_SC1);
      }
      asm volatile("" :: "v"(ev[0]), "v"(ev[1]), "v"(ev[2]), "v"(ev[3]));
      STAMP(16);
    }
  } else {
    const size_t kvoff = ((size_t)grp * SK + (size_t)s * 32) * DH;
    constexpr int TSTR = 65536, NREC = 31 * 65536 + 16384;
#define ROW(n) (((n) & 31) * TSTR)
    const __amdgpu_buffer_rsrc_t rk = __builtin_amdgcn_make_buffer_rsrc((void*)(kc + kvoff), 0, NREC, RSRC3);
    const int voff = lane * 16 + wv * 4096;
    v4u X[NEXTRA * 4][4], R[4][4];
    unsigned tb = (unsigned)__builtin_amdgcn_s_memrealtime();
#define LANDED() asm volatile("s_waitcnt vmcnt(%0)" :: "i"(4 * (LEAD - 1)) : "memory")
    STAMP(0);
    if (!EARLY) BAR();
#pragma unroll
    for (int n = 0; n < NEXTRA * 4 + LEAD; ++n) {
      if (EARLY && n == EARLY) BAR();
      if (n >= LEAD) LANDED();
      if (n >= LEAD) pace(tb, n);
      if (n == NSTAR && wv == 0) {
        const unsigned late = (unsigned)__builtin_amdgcn_s_memrealtime() - tb - (unsigned)((NSTAR * TAU0) >> 4);
        *(volatile LAS v2u*)(L + L_ML) = v2u{tb, late};
        lds_signal(L, FL_ORG, lane, 1u);
      }
      issue_turn(n < NEXTRA * 4 ? X[n] : R[n - NEXTRA * 4], rk, voff, ROW(n));
    }
    const bool b0 = lane & 1, b1_ = lane & 2, b2_ = lane & 4, b3 = lane & 8;
    const int hf = lane >> 5, c = lane & 31;
    LAS float* P = (LAS float*)(L + L_P) + wv * 512;
    LAS float* Pl = P + ((lane >> 3) & 3) * 16 + hf * 8 + (lane & 7);
    STAMP(1);
    lds_wait_ge(FL + FL_Q, 4u, 0);
    STAMP(2);
#define NEW_TB() do { const unsigned ok_ = FL[FL_TGOK], t_ = FL[FL_TG]; tb = __builtin_amdgcn_readfirstlane(ok_ ? t_ : tb); } while (0)
    const v4u q0 = *(const LAS v4u*)(L + L_Q + c * 16), q1 = *(const LAS v4u*)(L + L_Q + 512 + c * 16);
    float mx = -INFINITY;
#pragma unroll
    for (int g = 0; g < NEXTRA; ++g) {
      float r[4], rx[4];
#pragma unroll
      for (int d = 0; d < 4; ++d) {
        rx[d] = k_turn(X[4 * g + d], q0, q1, b0, b1_, b2_);
        if ((d & 1) == 0) NEW_TB();
        SB();
        LANDED();
        pace(tb, (NEXTRA + g) * 4 + d + LEAD);
        issue_turn(R[(d + LEAD) & 3], rk, voff, ROW((NEXTRA + g) * 4 + d + LEAD));
        r[d] = k_turn(R[d], q0, q1, b0, b1_, b2_);
        SB();
      }
      const float sx = k_group(rx[0], rx[1], rx[2], rx[3], b3), sc = k_group(r[0], r[1], r[2], r[3], b3);
      mx = fmaxf(mx, fmaxf(sx, sc));
      Pl[g * 64] = sx; Pl[(NEXTRA + g) * 64] = sc;
      SB();
    }
    STAMP(3);
    lds_wait_ge(FL + FL_TGOK, 1u, 0);
    NEW_TB();
    const __amdgpu_buffer_rsrc_t rv = __builtin_amdgcn_make_buffer_rsrc((void*)(vc + kvoff), 0, NREC, RSRC3);
#pragma unroll 1
    for (int g = 2 * NEXTRA; g < 8; ++g) {
      float r[4];
#pragma unroll
      for (int d = 0; d < 4; ++d) {
        const int nx = g * 4 + d + LEAD;
        const bool tov = (d + LEAD >= 4) && g == 7;
        const __amdgpu_buffer_rsrc_t rn = __builtin_amdgcn_make_buffer_rsrc((void*)((tov ? vc : kc) + kvoff), 0, NREC, RSRC3);
        LANDED();
        pace1(tb, nx);
        issue_turn(R[(d + LEAD) & 3], rn, voff, ROW(nx));
        r[d] = k_turn(R[d], q0, q1, b0, b1_, b2_);
        SB();
      }
      const float sc = k_group(r[0], r[1], r[2], r[3], b3);
      mx = fmaxf(mx, sc);
      Pl[g * 64] = sc;
    }
    STAMP(4);
    mx = fmaxf(mx, dpp<XOR2>(mx)); mx = fmaxf(mx, dpp<ROR4>(mx)); mx = fmaxf(mx, dpp<ROR8>(mx));
    mx = sw16_max(mx); mx = sw32_max(mx);
    const float msub = mx == -INFINITY ? 0.f : mx;
    float ls = 0.f;
    {
      float sv[8];
#pragma unroll
      for (int g = 0; g < 8; ++g) sv[g] = Pl[g * 64];
#pragma unroll
      for (int g = 0; g < 8; ++g) { const float p = __builtin_amdgcn_exp2f((sv[g] - msub) * SC2); ls += p; Pl[g * 64] = p; }
    }
    ls += dpp<XOR2>(ls); ls += dpp<ROR8>(ls); ls += dpp<ROR4>(ls);
    ls = sw16_add(ls, ls); ls = sw32_add(ls, ls);
    *(LAS v2f*)(L + (lane < 2 ? L_ML + (wv * 2 + lane) * 8 : L_SINK + 1024 + lane * 8)) = v2f{mx, ls};
    lds_signal(L, FL_ML, lane, 1u);
    v2f a0[4], a1[4];
#pragma unroll
    for (int k = 0; k < 4; ++k) { a0[k] = v2f{0.f, 0.f}; a1[k] = v2f{0.f, 0.f}; }
    const LAS v4f* Pr = (const LAS v4f*)(P + hf * 8);
    LAS v4f* mo = (LAS v4f*)(L + L_MRG + wv * 2048 + lane * 32);
    auto v_group = [&](int g, auto last) {
      v4f pa[4], pb[4];
#pragma unroll
      for (int d = 0; d < 4; ++d) { pa[d] = Pr[(g * 4 + d) * 4]; pb[d] = Pr[(g * 4 + d) * 4 + 1]; }
#pragma unroll
      for (int d = 0; d < 4; ++d) {
        if (!last.value || d + LEAD < 4) {
          LANDED();
          pace2(tb, 32 + g * 4 + d + LEAD);
          issue_turn(R[(d + LEAD) & 3], rv, voff, ROW(g * 4 + d + LEAD));
        }
        v_turn(R[d], pa[d], pb[d], a0, a1);
        SB();
        if (!last.value && d == 1) lds_signal(L, FL_PRE, lane, g == PREG ? 1u : 0u);
      }
    };
#pragma unroll 1
    for (int g = 0; g < 7; ++g) v_group(g, BoolC<false>{});
    v_group(7, BoolC<true>{});
    mo[0] = v4f{sw32_add(a0[0].x, a1[0].x), sw32_add(a0[0].y, a1[0].y), sw32_add(a0[1].x, a1[1].x), sw32_add(a0[1].y, a1[1].y)};
    mo[1] = v4f{sw32_add(a0[2].x, a1[2].x), sw32_add(a0[2].y, a1[2].y), sw32_add(a0[3].x, a1[3].x), sw32_add(a0[3].y, a1[3].y)};
    lds_signal(L, FL_DONE, lane, 1u);
    STAMP(5);
    if (isM) {
      const unsigned th = __builtin_amdgcn_readfirstlane(FL[FL_TAGH]);
      mlp_ln(L, __builtin_amdgcn_make_buffer_rsrc((void*)(ws + (size_t)(ROT ? (th & 31u) + 1u : 0u) * WS_SPAN), 0, WS_END, RSRC3), wv, lane,
             __builtin_amdgcn_readfirstlane(FL[FL_TAGL]), tb);
    }
  }
}
}

extern "C" void kernel_launch(void* const* d_in, const int* in_sizes, int n_in, void* d_out, int out_size, void* d_ws,
                              size_t ws_size, hipStream_t stream) {
  using namespace fk;
  (void)hipFuncSetAttribute((const void*)fused_decode, hipFuncAttributeMaxDynamicSharedMemorySize, L_END + DBG_STAMPS * 2048);
  hipLaunchKernelGGL(fused_decode, dim3(256), dim3(512), L_END + DBG_STAMPS * 2048, stream, (const u16*)d_in[0], (const u16*)d_in[1],
                     (const u16*)d_in[2], (const u16*)d_in[3], (const u16*)d_in[4], (const u16*)d_in[6], (const u16*)d_in[7],
                     (const u16*)d_in[8], (const u16*)d_in[9], (u16*)d_out, (unsigned char*)d_ws);
}
```

```cpp
#include <hip/hip_runtime.h>
#include <hip/hip_bf16.h>
#include <stdint.h>

#define DBG_STAMPS 0
#define TAU0 970
#define NSTAR 10
#define TGT 860
#define OGAIN 7
#define VPRE 2
#define VPT 400
#define VOUT 0
#define OAUX 0
#define LDSKB 96
#define WOL 100
#define W1H 11
#define W1L 60
#define W2D 2
#define W1M 4
#define SENT_R5 5
#define TAU1 890
#define TAU 886
#define QT 500
#define QSLEEP 24
#define WOT 1100
#define WOS 40
#define GOFF 0
#define WT 188
#define G1 87
#define G2 79
#define G3 160
#define WDELAY 6
#define RDELAY 8
#define FWAIT 60
#define PREG 6
#define CODE_BYTES 36096
#define W1E 1
#define ROT 1
#define EARLY 1
#define SLPT 0

static_assert(W1H - W1M >= 4 && W1H <= 14, "the wo tile takes four loads per wave");
namespace fk {
typedef unsigned short u16;
typedef unsigned long long u64;
typedef unsigned v4u __attribute__((ext_vector_type(4)));
typedef unsigned v2u __attribute__((ext_vector_type(2)));
typedef float v4f __attribute__((ext_vector_type(4)));
typedef float v2f __attribute__((ext_vector_type(2)));
typedef __bf16 bf16x2 __attribute__((ext_vector_type(2)));
typedef __bf16 bf16x8 __attribute__((ext_vector_type(8)));
typedef short s16x4 __attribute__((ext_vector_type(4)));
#define LAS __attribute__((address_space(3)))
#define GAS __attribute__((address_space(1)))

constexpr int D = 448, F = 1792, SK = 4096, DH = 256, NQ = 4096;
constexpr int AUX_NT = 2, AUX_SC1 = 16;
constexpr int NEXTRA = 3;
constexpr int LEAD = 2;
constexpr int PN0 = 16;
constexpr int RT = WT + G1, MT = RT + G2, FT = MT + G3;
constexpr int RSRC3 = 0x00020000;
constexpr float SC2 = 0.0625f * 1.4426950408889634f;
struct Spin {
  unsigned n = 0, t0 = 0;
  __device__ __forceinline__ bool expired() {
    if ((++n & 4095u) != 0u) return false;
    const unsigned t = (unsigned)__builtin_amdgcn_s_memrealtime();
    if (n == 4096u) t0 = t;
    return t - t0 > (1u << 27);
  }
};

constexpr int WS_EPOCH = 0;
constexpr int WS_T = 2048;
constexpr int WS_G = 4096;
constexpr int WS_ML = WS_G + 8 * 8 * 4 * 128 * 16;
constexpr int WS_H = WS_ML + 64 * 8 * 16;
constexpr int WS_P0 = WS_H + 16384;
constexpr int WS_PW = WS_P0 + 64 * 4 * 2048;
constexpr int WS_PF = WS_PW + 32 * 28 * 512;
constexpr int WS_END = WS_PF + 28 * 28 * 512;
constexpr int WS_SPAN = 2 << 20;
static_assert(WS_END <= WS_SPAN, "span");
constexpr int WS_DBG = 128 << 20;

constexpr int LN_STR = 912;
constexpr int L_P = 0;
constexpr int L_MRG = 8192;
constexpr int L_Q = 16384;
constexpr int L_FLAG = 17408;
constexpr int L_ML = L_FLAG + 64;
constexpr int L_LN = L_ML + 64;
constexpr int L_BF = L_LN + 8 * LN_STR;
constexpr int L_SINK = L_BF + 4096;
constexpr int L_RAW = L_SINK + 2048;
constexpr int L_RAW2 = L_RAW + 57344;
constexpr int W2P = 928, WOP = 160;
constexpr int L_END = L_RAW2 + 64 * W2P;
static_assert(L_END + 2048 <= 160 * 1024 && L_END > 80 * 1024, "lds");
static_assert(L_LN % 16 == 0 && L_BF % 16 == 0 && L_RAW % 16 == 0, "align");
enum { FL_LN = 0, FL_QP = 1, FL_Q = 2, FL_PRE = 3, FL_DONE = 4, FL_RAW = 5, FL_RAW2 = 6, FL_RAW3 = 7, FL_ML = 9, FL_TAGL = 10, FL_TAGH = 11, FL_TG = 12, FL_ORG = 13, FL_TGOK = 14, FL_PUB = 15 };

__device__ __forceinline__ float bflo(unsigned v) { return __builtin_bit_cast(float, v << 16); }
__device__ __forceinline__ float bfhi(unsigned v) { return __builtin_bit_cast(float, v & 0xffff0000u); }
__device__ __forceinline__ float rbf(float f) { return (float)(__bf16)f; }
__device__ __forceinline__ unsigned pk2(float lo, float hi) {
  bf16x2 v = {(__bf16)lo, (__bf16)hi};
  return __builtin_bit_cast(unsigned, v);
}
__device__ __forceinline__ unsigned pk2o(float lo, float hi) {
  unsigned p = pk2(lo, hi);
  asm("" : "+v"(p));
  return p;
}
__device__ __forceinline__ float dot2(unsigned a, unsigned b, float c) {
  return __builtin_amdgcn_fdot2_f32_bf16(__builtin_bit_cast(bf16x2, a), __builtin_bit_cast(bf16x2, b), c, false);
}
__device__ __forceinline__ v4f mfma(v4u a, v4u b, v4f c) {
  return __builtin_amdgcn_mfma_f32_16x16x32_bf16(__builtin_bit_cast(bf16x8, a), __builtin_bit_cast(bf16x8, b), c, 0, 0, 0);
}
template <int CTRL> __device__ __forceinline__ float dpp(float x) {
  return __builtin_bit_cast(float, __builtin_amdgcn_mov_dpp(__builtin_bit_cast(int, x), CTRL, 0xf, 0xf, true));
}
constexpr int XOR1 = 0xB1, XOR2 = 0x4E, HMIR = 0x141, ROR4 = 0x124, ROR8 = 0x128;
__device__ __forceinline__ float sw16_add(float a, float b) {
  auto s = __builtin_amdgcn_permlane16_swap(__float_as_uint(a), __float_as_uint(b), false, false);
  return __uint_as_float(s[0]) + __uint_as_float(s[1]);
}
__device__ __forceinline__ float sw32_add(float a, float b) {
  auto s = __builtin_amdgcn_permlane32_swap(__float_as_uint(a), __float_as_uint(b), false, false);
  return __uint_as_float(s[0]) + __uint_as_float(s[1]);
}
__device__ __forceinline__ float sw16_max(float a) {
  auto s = __builtin_amdgcn_permlane16_swap(__float_as_uint(a), __float_as_uint(a), false, false);
  return fmaxf(__uint_as_float(s[0]), __uint_as_float(s[1]));
}
__device__ __forceinline__ float sw32_max(float a) {
  auto s = __builtin_amdgcn_permlane32_swap(__float_as_uint(a), __float_as_uint(a), false, false);
  return fmaxf(__uint_as_float(s[0]), __uint_as_float(s[1]));
}
__device__ __forceinline__ v4u pack24(v4f v, unsigned tag) {
  const unsigned a = __float_as_uint(v.x), b = __float_as_uint(v.y), c = __float_as_uint(v.z), d = __float_as_uint(v.w);
  return v4u{__builtin_amdgcn_perm(b, a, 0x05030201u), __builtin_amdgcn_perm(c, b, 0x06050302u), __builtin_amdgcn_perm(d, c, 0x07060503u), tag};
}
__device__ __forceinline__ v4f unpack24(v4u g) {
  return v4f{__uint_as_float(g.x << 8), __uint_as_float(__builtin_amdgcn_perm(g.y, g.x, 0x0504030Cu)),
             __uint_as_float(__builtin_amdgcn_perm(g.z, g.y, 0x0403020Cu)), __uint_as_float(g.z & 0xFFFFFF00u)};
}
__device__ __forceinline__ v4u pack28(v4f v, unsigned tag) {
  return v4u{(__float_as_uint(v.x) & ~3u) | (tag & 3u), (__float_as_uint(v.y) & ~3u) | ((tag >> 2) & 3u),
             (__float_as_uint(v.z) & ~3u) | ((tag >> 4) & 3u), (__float_as_uint(v.w) & ~3u) | ((tag >> 6) & 3u)};
}
__device__ __forceinline__ v4f unpack28(v4u g) { return __builtin_bit_cast(v4f, g & ~3u); }
__device__ __forceinline__ bool bad28(v4u g, unsigned tag) {
  return (((g.x ^ tag) | (g.y ^ (tag >> 2)) | (g.z ^ (tag >> 4)) | (g.w ^ (tag >> 6))) & 3u) != 0u;
}
#define LGKM0() asm volatile("s_waitcnt lgkmcnt(0)" ::: "memory")
#define CLOB() asm volatile("" ::: "memory")
#define SB() __builtin_amdgcn_sched_barrier(0)
#define BAR() do { asm volatile("s_waitcnt lgkmcnt(0)" ::: "memory"); __builtin_amdgcn_s_barrier(); asm volatile("" ::: "memory"); __builtin_amdgcn_sched_barrier(0); } while (0)
#define LD128(rs, vo, so, aux) __builtin_amdgcn_raw_buffer_load_b128(rs, vo, so, aux)
#define ST128(v, rs, vo, so, aux) __builtin_amdgcn_raw_buffer_store_b128(v, rs, vo, so, aux)

#define STAMP(i) do {} while (0)

__device__ __forceinline__ void lds_wait_ge(volatile LAS unsigned* f, unsigned want, int slp) {
  Spin sp;
#pragma nounroll
  while (*f < want) {
    if (slp) __builtin_amdgcn_s_sleep(8);
    if (sp.expired()) break;
  }
  CLOB();
}
__device__ __forceinline__ void lds_signal(LAS unsigned char* L, int word, int lane, unsigned inc) {
  LGKM0();
  LAS unsigned* p = (LAS unsigned*)(L + (lane == 0 ? L_FLAG + word * 4 : L_SINK + lane * 4));
  __hip_atomic_fetch_add(p, inc, __ATOMIC_RELAXED, __HIP_MEMORY_SCOPE_WORKGROUP);
}
template <int N, int STR, int WIDE, int SLP>
__device__ __forceinline__ void gather(v4u (&v)[N], __amdgpu_buffer_rsrc_t rs, int off, unsigned tl, unsigned th, u64 last_mask) {
  Spin sp;
#pragma nounroll
  for (;;) {
    CLOB();
#pragma unroll
    for (int i = 0; i < N; ++i) v[i] = LD128(rs, off + i * STR, 0, AUX_SC1);
    u64 any = 0ull;
#pragma unroll
    for (int i = 0; i < N; ++i) {
      const u64 bad = __builtin_amdgcn_ballot_w64(WIDE == 2 ? bad28(v[i], tl) : WIDE ? ((v[i].z ^ tl) | (v[i].w ^ th)) != 0u : v[i].w != tl);
      any |= i == N - 1 ? bad & last_mask : bad;
    }
    if (any == 0ull || sp.expired()) break;
    if (SLP) __builtin_amdgcn_s_sleep(SLP);
  }
}
__device__ __forceinline__ void wait_until(unsigned until) {
  asm volatile("8:\n\ts_memrealtime vcc\n\ts_waitcnt lgkmcnt(0)\n\ts_sub_i32 vcc_lo, vcc_lo, %0\n\ts_cmp_lt_i32 vcc_lo, 0\n\ts_cbranch_scc1 8b\n" :: "s"(until) : "scc", "vcc", "memory");
}
__device__ __forceinline__ void doze_until(unsigned until) {
#pragma nounroll
  while ((int)((unsigned)__builtin_amdgcn_s_memrealtime() - until) < 0) __builtin_amdgcn_s_sleep(4);
}
constexpr int PN1 = 34;
constexpr int SCHED63 = (PN0 * TAU0 + (PN1 - PN0) * TAU1 + (63 - PN1) * TAU) >> 4;
__device__ __forceinline__ void pace(unsigned tb, int n) { wait_until(tb + (unsigned)((n < PN0 ? n * TAU0 : PN0 * TAU0 + (n - PN0) * TAU1) >> 4)); }
__device__ __forceinline__ void pace1(unsigned tb, int n) { wait_until(tb + (unsigned)((PN0 * TAU0 + (n - PN0) * TAU1) >> 4)); }
__device__ __forceinline__ void pace2(unsigned tb, int n) { wait_until(tb + (unsigned)((PN0 * TAU0 + (PN1 - PN0) * TAU1 + (n - PN1) * TAU) >> 4)); }
template <bool B> struct BoolC { static constexpr bool value = B; };
__device__ __forceinline__ void issue_turn(v4u* r, __amdgpu_buffer_rsrc_t rs, int voff, int soff) {
#pragma unroll
  for (int i = 0; i < 4; ++i) r[i] = LD128(rs, voff + i * 1024, soff, AUX_NT);
  SB();
}
__device__ __forceinline__ float k_turn(const v4u* k, v4u q0, v4u q1, bool b0, bool b1, bool b2) {
  float x[8];
#pragma unroll
  for (int i = 0; i < 4; ++i) {
    float a0 = dot2(k[i].x, q0.x, 0.f), a1 = dot2(k[i].x, q1.x, 0.f);
    a0 = dot2(k[i].y, q0.y, a0); a1 = dot2(k[i].y, q1.y, a1);
    a0 = dot2(k[i].z, q0.z, a0); a1 = dot2(k[i].z, q1.z, a1);
    a0 = dot2(k[i].w, q0.w, a0); a1 = dot2(k[i].w, q1.w, a1);
    x[2 * i] = a0; x[2 * i + 1] = a1;
  }
  float y[4], z[2];
#pragma unroll
  for (int v = 0; v < 4; ++v) y[v] = (b2 ? x[v + 4] : x[v]) + dpp<HMIR>(b2 ? x[v] : x[v + 4]);
#pragma unroll
  for (int v = 0; v < 2; ++v) z[v] = (b1 ? y[v + 2] : y[v]) + dpp<XOR2>(b1 ? y[v] : y[v + 2]);
  return (b0 ? z[1] : z[0]) + dpp<XOR1>(b0 ? z[0] : z[1]);
}
__device__ __forceinline__ float k_group(float r0, float r1, float r2, float r3, bool b3) {
  const float u0 = (b3 ? r1 : r0) + dpp<ROR8>(b3 ? r0 : r1);
  const float u1 = (b3 ? r3 : r2) + dpp<ROR8>(b3 ? r2 : r3);
  return rbf(sw16_add(u0, u1));
}
__device__ __forceinline__ void v_turn(const v4u* v, v4f pa, v4f pb, v2f (&a0)[4], v2f (&a1)[4]) {
#pragma unroll
  for (int i = 0; i < 4; ++i) {
    const float p0 = i == 0 ? pa.x : i == 1 ? pa.z : i == 2 ? pb.x : pb.z;
    const float p1 = i == 0 ? pa.y : i == 1 ? pa.w : i == 2 ? pb.y : pb.w;
#pragma unroll
    for (int j = 0; j < 4; ++j) {
      const v2f e = {bflo(v[i][j]), bfhi(v[i][j])};
      a0[j] = __builtin_elementwise_fma(e, v2f{p0, p0}, a0[j]);
      a1[j] = __builtin_elementwise_fma(e, v2f{p1, p1}, a1[j]);
    }
  }
}
template <bool FULL> __device__ __forceinline__ float row_sum(float v) {
  v += dpp<XOR1>(v); v += dpp<XOR2>(v); v += dpp<HMIR>(v); v += dpp<ROR8>(v);
  v = sw16_add(v, v);
  return FULL ? sw32_add(v, v) : v;
}
template <int NW> __device__ __forceinline__ void ln_row(unsigned (&x)[NW], bool act) {
  v2f v[NW];
#pragma unroll
  for (int k = 0; k < NW; ++k) v[k] = v2f{bflo(x[k]), bfhi(x[k])};
  float mean, var;
  if constexpr (NW == 4) {
    const float c = bflo(__builtin_amdgcn_readfirstlane(x[0]));
    v2f s1 = {0.f, 0.f}, s2 = {0.f, 0.f};
#pragma unroll
    for (int k = 0; k < NW; ++k) { const v2f d = v[k] - c; s1 += d; s2 = __builtin_elementwise_fma(d, d, s2); }
    float t = sw32_add(act ? s1.x + s1.y : 0.f, act ? s2.x + s2.y : 0.f);
    t = row_sum<false>(t) * (1.0f / D);
    const float m1 = __builtin_bit_cast(float, __builtin_amdgcn_readlane(__builtin_bit_cast(int, t), 0));
    const float m2 = __builtin_bit_cast(float, __builtin_amdgcn_readlane(__builtin_bit_cast(int, t), 32));
    mean = c + m1;
    var = fmaxf(fmaf(-m1, m1, m2), m2 - m2);
  } else {
    constexpr unsigned ONES = 0x3F803F80u;
    float sa = 0.f, sb = 0.f;
#pragma unroll
    for (int k = 0; k < NW; k += 2) { sa = dot2(x[k], ONES, sa); sb = dot2(x[k + 1], ONES, sb); }
    mean = row_sum<false>(sa + sb) * (1.0f / D);
    v2f q0 = {0.f, 0.f}, q1 = {0.f, 0.f};
#pragma unroll
    for (int k = 0; k < NW; k += 2) {
      const v2f d0 = v[k] - mean, d1 = v[k + 1] - mean;
      q0 = __builtin_elementwise_fma(d0, d0, q0); q1 = __builtin_elementwise_fma(d1, d1, q1);
    }
    q0 += q1;
    var = row_sum<false>(act ? q0.x + q0.y : 0.f) * (1.0f / D);
  }
  const float mb = rbf(mean);
#pragma unroll
  for (int k = 0; k < NW; ++k) { const v2f d = v[k] - mb; const unsigned p = pk2o(d.x, d.y); v[k] = v2f{bflo(p), bfhi(p)}; }
  const float den = rbf(__builtin_amdgcn_sqrtf(rbf(rbf(var) + 1e-5f)));
  const float ri = __builtin_amdgcn_rcpf(den);
#pragma unroll
  for (int k = 0; k < NW; ++k) { const v2f a = v[k] * ri; x[k] = pk2(a.x, a.y); }
}
__device__ __forceinline__ v4u frag_tr(const LAS unsigned char* p, int hi) {
#if defined(__HIP_DEVICE_COMPILE__)
  const v2u a = __builtin_bit_cast(v2u, __builtin_amdgcn_ds_read_tr16_b64_v4i16((LAS s16x4*)p));
  const v2u b = __builtin_bit_cast(v2u, __builtin_amdgcn_ds_read_tr16_b64_v4i16((LAS s16x4*)(p + hi)));
  return v4u{a.x, a.y, b.x, b.y};
#else
  return v4u{0u, 0u, 0u, 0u};
#endif
}
__device__ __forceinline__ v4f red16(v4f t) {
#pragma unroll
  for (int k = 0; k < 4; ++k) { float a = t[k]; a += dpp<ROR8>(a); a += dpp<ROR4>(a); a = sw16_add(a, a); a = sw32_add(a, a); t[k] = a; }
  return t;
}
__device__ __forceinline__ void mlp_ln(LAS unsigned char* L, __amdgpu_buffer_rsrc_t rt, int row, int lane, unsigned tl, unsigned t0) {
  const bool act = lane < 56;
  const int a = act ? L_LN + row * LN_STR + lane * 8 : L_SINK + lane * 8;
  v4u hg[2];
  int o0 = WS_H + (row * 112 + (act ? lane : 55)) * 16;
  asm volatile("" : "+v"(o0));
  wait_until(t0 + SCHED63 + MT);
  gather<2, 896, 0, SLPT>(hg, rt, o0, tl, 0u, ~0ull);
  { const int wv = row; (void)wv; STAMP(17); }
  unsigned x[4] = {hg[0].x, hg[0].y, hg[1].x, hg[1].y};
  ln_row<4>(x, act);
  *(LAS v2u*)(L + a) = v2u{x[0], x[1]}; *(LAS v2u*)(L + a + 448) = v2u{x[2], x[3]};
  BAR();
}

__global__ void __launch_bounds__(512, 2)
fused_decode(const u16* __restrict__ x, const u16* __restrict__ kc, const u16* __restrict__ vc, const u16* __restrict__ wq,
             const u16* __restrict__ wo, const u16* __restrict__ w1, const u16* __restrict__ b1, const u16* __restrict__ w2,
             const u16* __restrict__ b2, u16* __restrict__ out, unsigned char* __restrict__ ws) {
  extern __shared__ __attribute__((aligned(16))) unsigned char smem[];
  LAS unsigned char* L = (LAS unsigned char*)smem;
  const u64 pc0 = __builtin_amdgcn_s_getpc() & ~0xFFull;
  const int w = blockIdx.x, tid = threadIdx.x, lane = tid & 63, wv = __builtin_amdgcn_readfirstlane(tid >> 6);
  const int kvh = w & 7, r5 = w >> 3, b = r5 & 7, s = r5 >> 3, grp = b * 8 + kvh;
  const int m = (r5 - 28) * 8 + kvh;
  const bool isM = r5 >= 28 && m < 28;
  volatile LAS unsigned* FL = (volatile LAS unsigned*)(L + L_FLAG);
  const __amdgpu_buffer_rsrc_t rws = __builtin_amdgcn_make_buffer_rsrc((void*)ws, 0, WS_END, RSRC3);
  auto code_prefetch = [&]() {
    const __amdgpu_buffer_rsrc_t rc = __builtin_amdgcn_make_buffer_rsrc((void*)pc0, 0, CODE_BYTES, RSRC3);
    constexpr int CPL = (CODE_BYTES / 128 + 31) / 32;
    return __builtin_amdgcn_raw_buffer_load_b32(rc, lane < CPL ? (lane * 32 + r5) * 128 : CODE_BYTES, 0, 0);
  };
  unsigned cpf = 0;
  if (wv == 4) cpf = code_prefetch();
  SB();
  asm volatile("" :: "s"(x), "s"(kc), "s"(vc), "s"(wq), "s"(wo), "s"(w1), "s"(b1), "s"(w2), "s"(b2), "s"(out), "s"(ws));

  if (__builtin_expect(wv >= 4, 0)) {
    const int hw = wv - 4;
    STAMP(0);
    __builtin_amdgcn_s_setprio(3);
    const unsigned tstart = (unsigned)__builtin_amdgcn_s_memrealtime();
    const bool isW = r5 < 28;
    const int t = r5 >> 2, kq = r5 & 3;
    const int g = lane >> 4;
    const LAS unsigned char* ap = L + L_RAW + (g * 8 + ((lane & 15) >> 2)) * 128 + hw * 32 + (lane & 3) * 8;
    const LAS unsigned char* bp = L + L_LN + (lane & 7) * LN_STR + g * 16;
    const __amdgpu_buffer_rsrc_t rx = __builtin_amdgcn_make_buffer_rsrc((void*)x, 0, 8 * D * 2, RSRC3);
    const v2u ep = __builtin_amdgcn_raw_buffer_load_b64(rws, WS_EPOCH, 0, AUX_SC1);
    unsigned tagl, tagh;
#define MAKE_TAGS() do { const unsigned el = __builtin_amdgcn_readfirstlane(ep.x), eh = __builtin_amdgcn_readfirstlane(ep.y); \
      u64 z = (((u64)eh << 32) | el) + 0x9E3779B97F4A7C15ull; \
      z = (z ^ (z >> 30)) * 0xBF58476D1CE4E5B9ull; z = (z ^ (z >> 27)) * 0x94D049BB133111EBull; z ^= z >> 31; \
      tagl = (((unsigned)z ^ el) & 3u) == 0u ? (unsigned)z ^ 1u : (unsigned)z; \
      tagl = ((tagl ^ (tagl >> 2)) & 3u) == 0u ? tagl ^ 4u : tagl;     \
      tagh = ((unsigned)(z >> 32) & ~31u) | ((eh + 1u) & 31u); } while (0)
    if (tid < 256 + 16) FL[tid - 256] = 0u;
    {
      const int row = hw * 2 + (lane >> 5), c = lane & 31;
      const int ks0 = s < 3 ? s * 4 : 11, nks = s < 2 ? 4 : 3;
      const int xo = c < 28 ? row * (D * 2) + c * 32 : 8 * D * 2;
      const v4u xa = LD128(rx, xo, 0, AUX_NT), xb = LD128(rx, xo + 16, 0, AUX_NT);
      const __amdgpu_buffer_rsrc_t rq = __builtin_amdgcn_make_buffer_rsrc((void*)(wq + ks0 * 32 * NQ + (kvh * 8 + b) * 64), 0, (nks * 32 - 1) * (NQ * 2) + 128, RSRC3);
      v4u tt[4];
#pragma unroll
      for (int i = 0; i < 4; ++i) tt[i] = LD128(rq, (lane >> 3) * (NQ * 2) + (lane & 7) * 16, (i * 4 + hw) * 8 * (NQ * 2), AUX_NT);
      SB();
      BAR();
      {
        unsigned xr[8] = {xa.x, xa.y, xa.z, xa.w, xb.x, xb.y, xb.z, xb.w};
        ln_row<8>(xr, c < 28);
        const int a = c < 28 ? L_LN + row * LN_STR + c * 32 : L_SINK + lane * 32;
        *(LAS v4u*)(L + a) = v4u{xr[0], xr[1], xr[2], xr[3]}; *(LAS v4u*)(L + a + 16) = v4u{xr[4], xr[5], xr[6], xr[7]};
      }
      MAKE_TAGS();
      STAMP(1);
#pragma unroll
      for (int i = 0; i < 4; ++i) *(LAS v4u*)(L + L_RAW + (i * 4 + hw) * 1024 + lane * 16) = tt[i];
      lds_signal(L, FL_LN, lane, 1u);
      lds_wait_ge(FL + FL_LN, 4u, 0);
      v4f d0 = {0.f, 0.f, 0.f, 0.f};
#pragma unroll
      for (int ks = 0; ks < 3; ++ks) d0 = mfma(frag_tr(ap + ks * 4096, 512), *(const LAS v4u*)(bp + (ks0 + ks) * 64), d0);
      if (s < 2) d0 = mfma(frag_tr(ap + 3 * 4096, 512), *(const LAS v4u*)(bp + (ks0 + 3) * 64), d0);
      ST128(pack28(d0, tagl), rws, (lane & 8) == 0 ? WS_G + (((kvh * 8 + (lane & 7)) * 4 + s) * 128 + b * 16 + hw * 4 + g) * 16 : WS_END, 0, AUX_SC1);
      lds_signal(L, FL_QP, lane, 1u);
      STAMP(2);
    }
    auto q_gather = [&]() {
      const int off = WS_G + (kvh * 8 + b) * 8192 + ((hw - 2) * 64 + lane) * 16;
      doze_until(tstart + QT);
      v4u gq[4];
      gather<4, 2048, 2, QSLEEP>(gq, rws, off, tagl, 0u, ~0ull);
      const v4f qv = (unpack28(gq[0]) + unpack28(gq[1])) + (unpack28(gq[2]) + unpack28(gq[3]));
      *(LAS v2u*)(L + L_Q + ((hw - 2) * 64 + lane) * 8) = v2u{pk2(qv.x, qv.y), pk2(qv.z, qv.w)};
      FL[FL_TAGL] = tagl; FL[FL_TAGH] = tagh;
      lds_signal(L, FL_Q, lane, 2u);
      STAMP(3);
    };
    if (hw >= 2) q_gather();
    if (hw == 0 && r5 == SENT_R5) {
      lds_wait_ge(FL + FL_ORG, 1u, 1);
      const v2u og = *(volatile LAS v2u*)(L + L_ML);
      ST128((v4u{og.x, og.y, tagl, tagh}), rws, lane == 0 ? WS_T + kvh * 16 : WS_END, 0, AUX_SC1);
    }
    if (hw == 1) {
      doze_until(tstart + TGT);
      v4u gt[1];
      gather<1, 0, 1, 32>(gt, rws, WS_T + (lane & 7) * 16, tagl, tagh, ~0ull);
      const unsigned t0 = __builtin_amdgcn_readfirstlane(gt[0].x);
      int o = (int)(gt[0].x - t0) * 8 + (int)gt[0].y * OGAIN;
      o += __builtin_amdgcn_mov_dpp(o, XOR1, 0xf, 0xf, true); o += __builtin_amdgcn_mov_dpp(o, XOR2, 0xf, 0xf, true); o += __builtin_amdgcn_mov_dpp(o, HMIR, 0xf, 0xf, true);
      o >>= 6;
      o = o < -1000 ? -1000 : o > 2000 ? 2000 : o;
      FL[FL_TG] = t0 + (unsigned)o + (unsigned)(GOFF);
      lds_signal(L, FL_TGOK, lane, 1u);
      if (VPRE == 1) {
        const unsigned z0 = *(const GAS unsigned*)(vc + ((size_t)grp * SK + (lane & 1) * (SK - 1)) * DH);
        asm volatile("" :: "v"(z0));
      }
    }
    if (VPRE == 2 && hw == 0) {
      doze_until(tstart + VPT);
#define CAS __attribute__((address_space(4)))
      const unsigned z0 = *(const CAS unsigned*)(vc + (size_t)grp * SK * DH), z1 = *(const CAS unsigned*)(vc + ((size_t)grp * SK + SK - 1) * DH);
      const unsigned z2 = *(const CAS unsigned*)(isM ? w2 + m * 64 * D : wo + (kvh * 512 + kq * 128) * D), z3 = *(const CAS unsigned*)(isM ? w2 + (m * 64 + 63) * D : wo + (kvh * 512 + kq * 128 + 127) * D);
      const unsigned z4 = *(const CAS unsigned*)(ws + (size_t)((tagh & 31u) + 1u) * WS_SPAN), z5 = *(const CAS unsigned*)(ws + (size_t)((tagh & 31u) + 2u) * WS_SPAN - 64);
      const unsigned z6 = *(const CAS unsigned*)(w1 + (r5 & 1) * (D - 1) * F);
      asm volatile("" :: "s"(z0), "s"(z1), "s"(z2), "s"(z3), "s"(z4), "s"(z5), "s"(z6));
    }
    lds_wait_ge(FL + FL_Q, 4u, 1);
    lds_wait_ge(FL + FL_QP, 4u, 1);
    v4f cx, cb;
    {
      const v2u bv = __builtin_nontemporal_load((const GAS v2u*)(isW ? b2 + r5 * 16 + (lane & 3) * 4 : b1 + (isM ? m : 0) * 64 + hw * 16 + g * 4));
      const v2u xv = __builtin_amdgcn_raw_buffer_load_b64(rx, (kvh * D + r5 * 16 + (lane & 3) * 4) * 2, 0, AUX_NT);
      cb = v4f{bflo(bv.x), bfhi(bv.x), bflo(bv.y), bfhi(bv.y)};
      cx = v4f{bflo(xv.x), bfhi(xv.x), bflo(xv.y), bfhi(xv.y)};
    }
    const __amdgpu_buffer_rsrc_t rw = __builtin_amdgcn_make_buffer_rsrc(
        isW ? (void*)(wo + (kvh * 512 + kq * 128) * D + t * 64) : (void*)(w1 + (isM ? m : 0) * 64), 0, isW ? 127 * (D * 2) + 128 : isM ? 447 * (F * 2) + 128 : 0, RSRC3);
    const int pitch = isW ? D * 2 : F * 2;
    int wvo = (lane >> 3) * pitch + (lane & 7) * 16;
    asm volatile("" : "+v"(wvo));
    if (W1M && isM) {
      doze_until(tstart + WOT);
      v4u t1[W1M + 1];
#pragma unroll
      for (int i = 0; i < W1M; ++i) t1[i] = LD128(rw, wvo, (i * 4 + hw) * 8 * (F * 2), AUX_NT);
#pragma unroll
      for (int i = 0; i < W1M; ++i) *(LAS v4u*)(L + L_RAW + (i * 4 + hw) * 1024 + lane * 16) = t1[i];
    }
    asm volatile("" :: "v"(cpf));
    STAMP(4);
    const __amdgpu_buffer_rsrc_t rt = __builtin_amdgcn_make_buffer_rsrc((void*)(ws + (size_t)(ROT ? (tagh & 31u) + 1u : 0u) * WS_SPAN), 0, WS_END, RSRC3);
    float fw[4] = {0.f, 0.f, 0.f, 0.f};
    if (hw < 2) {
      lds_wait_ge(FL + FL_ML, 4u, 1);
      const v2f ml = *(const LAS v2f*)(L + L_ML + ((lane & 3) * 2 + hw) * 8);
      float Ms = ml.x;
      Ms = fmaxf(Ms, dpp<XOR1>(Ms)); Ms = fmaxf(Ms, dpp<XOR2>(Ms));
      const float Msub = Ms == -INFINITY ? 0.f : Ms;
      float Lw = __builtin_amdgcn_exp2f((ml.x - Msub) * SC2) * ml.y;
      Lw += dpp<XOR1>(Lw); Lw += dpp<XOR2>(Lw);
      ST128((v4u{__float_as_uint(Ms), __float_as_uint(Lw), tagl, tagh}), rt, lane == 0 ? WS_ML + ((grp * 2 + hw) * 4 + s) * 16 : WS_END, 0, AUX_SC1);
      const int off = WS_ML + ((grp * 2 + hw) * 4 + (lane & 3)) * 16;
      v4u gm1[1];
      __builtin_amdgcn_s_sleep(96); __builtin_amdgcn_s_sleep(96);
      gather<1, 0, 1, 64>(gm1, rt, off, tagl, tagh, ~0ull);
      const v4u gm = gm1[0];
      float M = __uint_as_float(gm.x);
      M = fmaxf(M, dpp<XOR1>(M)); M = fmaxf(M, dpp<XOR2>(M));
      float Ls = __builtin_amdgcn_exp2f((__uint_as_float(gm.x) - M) * SC2) * __uint_as_float(gm.y);
      Ls += dpp<XOR1>(Ls); Ls += dpp<XOR2>(Ls);
      const float f = __builtin_amdgcn_exp2f((ml.x - M) * SC2) / Ls;
#pragma unroll
      for (int i = 0; i < 4; ++i) fw[i] = __builtin_bit_cast(float, __builtin_amdgcn_readlane(__builtin_bit_cast(int, f), i));
    }
    STAMP(5);
    lds_wait_ge(FL + FL_TGOK, 1u, 1);
    const unsigned tg = __builtin_amdgcn_readfirstlane(FL[FL_TG]);
    asm volatile("" : "+v"(cx), "+v"(cb));
    int p_p0 = WS_P0 + ((kvh * 8 + b) * 4 + s) * 2048 + (hw & 1) * 1024 + lane * 16;
    int p_st = isW ? ((lane & 8) == 0 ? WS_PW + (((kvh * 4 + kq) * 28 + t * 4 + hw) * 8 + (lane & 7)) * 64 + g * 16 : WS_END)
                   : WS_PF + ((m * 28 + hw * 7) * 8 + (lane & 15)) * 64 + g * 16;
    int p_h = lane < 4 ? WS_H + (kvh * 112 + r5 * 4 + lane) * 16 : WS_END;
    int g_p0 = WS_P0 + (kvh * 8 + (lane >> 3)) * 8192 + (kq * 32 + hw * 8 + (lane & 7)) * 16;
    int g_pw = WS_PW + (((lane >> 2) * 28 + r5) * 8 + kvh) * 64 + (lane & 3) * 16;
    int g_o = lane < 4 ? (kvh * D + r5 * 16 + lane * 4) * 2 : 8 * D * 2;
    int g_e = (w == 0 && lane == 0) ? WS_EPOCH : WS_END;
    int a_bw = isW ? L_BF + hw * 512 + (lane >> 3) * 64 + (lane & 3) * 16 + ((lane >> 2) & 1) * 8 : L_BF + (lane & 15) * 144 + (hw >> 1) * 64 + g * 16 + (hw & 1) * 8;
    int a_br = isW ? L_BF + (lane & 7) * 64 + g * 16 : L_BF + (lane & 15) * 144 + g * 16;
    int a_ow = L_RAW + (hw * 8 + (lane >> 3)) * WOP + (lane & 7) * 16, a_or = L_RAW + (g * 4 + ((lane & 15) >> 2)) * WOP + hw * 32 + (lane & 3) * 8;
    asm volatile("" : "+v"(a_ow), "+v"(a_or));
    asm volatile("" : "+v"(p_p0), "+v"(p_st), "+v"(p_h), "+v"(g_p0), "+v"(g_pw), "+v"(g_o), "+v"(g_e), "+v"(a_bw), "+v"(a_br));
    const __amdgpu_buffer_rsrc_t rout = __builtin_amdgcn_make_buffer_rsrc((void*)out, 0, 8 * D * 2, RSRC3);
    lds_wait_ge(FL + FL_PRE, 4u, 1);
    STAMP(6);
    if (hw == 3) { const unsigned z = code_prefetch(); asm volatile("" :: "v"(z)); }
    v4u to[14];
    wait_until(tg + SCHED63 + (isW ? WOL : W1L));
#pragma unroll
    for (int i = W1M; i < W1H; ++i) to[i] = LD128(rw, wvo, ((isW ? i - W1M : i) * 4 + hw) * 8 * pitch, AUX_NT);
    SB();
    lds_wait_ge(FL + FL_DONE, 4u, 0);
    if (hw < 2) {
      STAMP(7);
      const LAS v4f* mp = (const LAS v4f*)(L + L_MRG + hw * 1024 + lane * 16);
      const v4f o = (mp[0] * fw[0] + mp[128] * fw[1]) + (mp[256] * fw[2] + mp[384] * fw[3]);
      ST128(pack24(o, tagl), rt, p_p0, 0, AUX_SC1);
      if (isM) lds_signal(L, FL_PUB, lane, 1u);
      STAMP(8);
    }
    if (isW) {
      __builtin_amdgcn_s_sleep(WDELAY);
      wait_until(tg + SCHED63 + WT);
      v4u pv[4];
      {
        CLOB();
#pragma unroll
        for (int i = 0; i < 4; ++i) pv[i] = LD128(rt, g_p0 + i * 2048, 0, AUX_SC1);
        SB();
#pragma unroll
        for (int i = 0; i < 4; ++i) *(LAS v4u*)(L + a_ow + i * 32 * WOP) = to[W1M + i];
        SB();
        const u64 bad = __builtin_amdgcn_ballot_w64((((pv[0].w ^ tagl) | (pv[1].w ^ tagl)) | ((pv[2].w ^ tagl) | (pv[3].w ^ tagl))) != 0u);
        if (__builtin_expect(bad != 0ull, 0)) gather<4, 2048, 0, SLPT>(pv, rt, g_p0, tagl, 0u, ~0ull);
      }
      STAMP(9);
      const v4f o = (unpack24(pv[0]) + unpack24(pv[1])) + (unpack24(pv[2]) + unpack24(pv[3]));
      *(LAS v2u*)(L + a_bw) = v2u{pk2(o.x, o.y), pk2(o.z, o.w)};
      BAR();
      v4u bf[4], Fr[4];
#pragma unroll
      for (int ks = 0; ks < 4; ++ks) { Fr[ks] = frag_tr(L + a_or + ks * 32 * WOP, 16 * WOP); bf[ks] = *(const LAS v4u*)(L + a_br + ks * 512); }
      SB();
      v4f d0 = {0.f, 0.f, 0.f, 0.f}, d1 = {0.f, 0.f, 0.f, 0.f};
      d0 = mfma(Fr[0], bf[0], d0); d1 = mfma(Fr[1], bf[1], d1); d0 = mfma(Fr[2], bf[2], d0); d1 = mfma(Fr[3], bf[3], d1);
      ST128(pack24(d0 + d1, tagl), rt, p_st, 0, AUX_SC1);
      STAMP(10);
      if (hw == 0) {
        v4u u[2];
        __builtin_amdgcn_s_sleep(RDELAY);
        wait_until(tg + SCHED63 + RT);
        gather<2, 16 * 28 * 512, 0, SLPT>(u, rt, g_pw, tagl, 0u, ~0ull);
        STAMP(11);
        const v4f hv = red16(unpack24(u[0]) + unpack24(u[1]));
        const unsigned h01 = pk2o(cx.x + rbf(hv.x), cx.y + rbf(hv.y)), h23 = pk2o(cx.z + rbf(hv.z), cx.w + rbf(hv.w));
        ST128((v4u{h01, h23, tagh, tagl}), rt, p_h, 0, AUX_SC1);
        STAMP(12);
        __builtin_amdgcn_s_sleep(FWAIT);
        wait_until(tg + SCHED63 + FT);
        gather<2, 16 * 28 * 512, 0, SLPT>(u, rt, g_pw + (WS_PF - WS_PW), tagl, 0u, (1ull << 48) - 1ull);
        STAMP(13);
        const v4f ov = red16(unpack24(u[0]) + unpack24(u[1]));
        const unsigned o01 = pk2(bflo(h01) + rbf(rbf(ov.x) + cb.x), bfhi(h01) + rbf(rbf(ov.y) + cb.y));
        const unsigned o23 = pk2(bflo(h23) + rbf(rbf(ov.z) + cb.z), bfhi(h23) + rbf(rbf(ov.w) + cb.w));
        __builtin_amdgcn_raw_buffer_store_b64(v2u{o01, o23}, rout, g_o, 0, OAUX);
        __builtin_amdgcn_raw_buffer_store_b64(v2u{tagl, tagh}, rws, g_e, 0, AUX_SC1);
        STAMP(14);
      }
    } else if (isM) {
      v4u Fr[14];
      lds_wait_ge(FL + FL_PUB, 2u, 0);
#pragma unroll
      for (int i = W1H; i < 14; ++i) to[i] = LD128(rw, wvo, (i * 4 + hw) * 8 * (F * 2), AUX_NT);
      SB();
#pragma unroll
      for (int i = W1M; i < 14; ++i) *(LAS v4u*)(L + L_RAW + (i * 4 + hw) * 1024 + lane * 16) = to[i];
      lds_signal(L, FL_RAW, lane, 1u);
      lds_wait_ge(FL + FL_RAW, 4u, 0);
#pragma unroll
      for (int ks = 0; ks < 14; ++ks) Fr[ks] = frag_tr(ap + ks * 4096, 512);
#pragma unroll
      for (int ks = 0; ks < 14; ++ks) asm volatile("" : "+v"(Fr[ks]));
      STAMP(19);
      mlp_ln(L, rt, wv, lane, tagl, tg);
      STAMP(15);
      v4u bf[14];
#pragma unroll
      for (int ks = 0; ks < 14; ++ks) bf[ks] = *(const LAS v4u*)(bp + ks * 64);
      SB();
      v4f d0 = {0.f, 0.f, 0.f, 0.f}, d1 = {0.f, 0.f, 0.f, 0.f};
#pragma unroll
      for (int ks = 0; ks < 14; ks += 2) { d0 = mfma(Fr[ks], bf[ks], d0); d1 = mfma(Fr[ks + 1], bf[ks + 1], d1); }
      d0 += d1;
      const unsigned g01 = pk2o(d0.x, d0.y), g23 = pk2o(d0.z, d0.w);
      const unsigned a01 = pk2o(bflo(g01) + cb.x, bfhi(g01) + cb.y), a23 = pk2o(bflo(g23) + cb.z, bfhi(g23) + cb.w);
      float u[4] = {bflo(a01), bfhi(a01), bflo(a23), bfhi(a23)};
#pragma unroll
      for (int k = 0; k < 4; ++k) { const float a = u[k] > 0.f ? u[k] : 0.f; u[k] = a * a; }
      *(LAS v2u*)(L + a_bw) = v2u{pk2(u[0], u[1]), pk2(u[2], u[3])};
      BAR();
      STAMP(18);
    }
  } else {
    const size_t kvoff = ((size_t)grp * SK + (size_t)s * 32) * DH;
    constexpr int TSTR = 65536, NREC = 31 * 65536 + 16384;
#define ROW(n) (((n) & 31) * TSTR)
    const __amdgpu_buffer_rsrc_t rk = __builtin_amdgcn_make_buffer_rsrc((void*)(kc + kvoff), 0, NREC, RSRC3);
    const int voff = lane * 16 + wv * 4096;
    v4u X[NEXTRA * 4][4], R[4][4];
    unsigned tb = (unsigned)__builtin_amdgcn_s_memrealtime();
#define LANDED() asm volatile("s_waitcnt vmcnt(%0)" :: "i"(4 * (LEAD - 1)) : "memory")
    STAMP(0);
    if (!EARLY) BAR();
#pragma unroll
    for (int n = 0; n < NEXTRA * 4 + LEAD; ++n) {
      if (EARLY && n == EARLY) BAR();
      if (n >= LEAD) LANDED();
      if (n >= LEAD) pace(tb, n);
      if (n == NSTAR && wv == 0) {
        const unsigned late = (unsigned)__builtin_amdgcn_s_memrealtime() - tb - (unsigned)((NSTAR * TAU0) >> 4);
        *(volatile LAS v2u*)(L + L_ML) = v2u{tb, late};
        lds_signal(L, FL_ORG, lane, 1u);
      }
      issue_turn(n < NEXTRA * 4 ? X[n] : R[n - NEXTRA * 4], rk, voff, ROW(n));
    }
    const bool b0 = lane & 1, b1_ = lane & 2, b2_ = lane & 4, b3 = lane & 8;
    const int hf = lane >> 5, c = lane & 31;
    LAS float* P = (LAS float*)(L + L_P) + wv * 512;
    LAS float* Pl = P + ((lane >> 3) & 3) * 16 + hf * 8 + (lane & 7);
    STAMP(1);
    lds_wait_ge(FL + FL_Q, 4u, 0);
    STAMP(2);
#define NEW_TB() do { const unsigned ok_ = FL[FL_TGOK], t_ = FL[FL_TG]; tb = __builtin_amdgcn_readfirstlane(ok_ ? t_ : tb); } while (0)
    const v4u q0 = *(const LAS v4u*)(L + L_Q + c * 16), q1 = *(const LAS v4u*)(L + L_Q + 512 + c * 16);
    float mx = -INFINITY;
#pragma unroll
    for (int g = 0; g < NEXTRA; ++g) {
      float r[4], rx[4];
#pragma unroll
      for (int d = 0; d < 4; ++d) {
        rx[d] = k_turn(X[4 * g + d], q0, q1, b0, b1_, b2_);
        if ((d & 1) == 0) NEW_TB();
        SB();
        LANDED();
        pace(tb, (NEXTRA + g) * 4 + d + LEAD);
        issue_turn(R[(d + LEAD) & 3], rk, voff, ROW((NEXTRA + g) * 4 + d + LEAD));
        r[d] = k_turn(R[d], q0, q1, b0, b1_, b2_);
        SB();
      }
      const float sx = k_group(rx[0], rx[1], rx[2], rx[3], b3), sc = k_group(r[0], r[1], r[2], r[3], b3);
      mx = fmaxf(mx, fmaxf(sx, sc));
      Pl[g * 64] = sx; Pl[(NEXTRA + g) * 64] = sc;
      SB();
    }
    STAMP(3);
    lds_wait_ge(FL + FL_TGOK, 1u, 0);
    NEW_TB();
    const __amdgpu_buffer_rsrc_t rv = __builtin_amdgcn_make_buffer_rsrc((void*)(vc + kvoff), 0, NREC, RSRC3);
#pragma unroll 1
    for (int g = 2 * NEXTRA; g < 8; ++g) {
      float r[4];
#pragma unroll
      for (int d = 0; d < 4; ++d) {
        const int nx = g * 4 + d + LEAD;
        const bool tov = (d + LEAD >= 4) && g == 7;
        const __amdgpu_buffer_rsrc_t rn = __builtin_amdgcn_make_buffer_rsrc((void*)((tov ? vc : kc) + kvoff), 0, NREC, RSRC3);
        LANDED();
        pace1(tb, nx);
        issue_turn(R[(d + LEAD) & 3], rn, voff, ROW(nx));
        r[d] = k_turn(R[d], q0, q1, b0, b1_, b2_);
        SB();
      }
      const float sc = k_group(r[0], r[1], r[2], r[3], b3);
      mx = fmaxf(mx, sc);
      Pl[g * 64] = sc;
    }
    STAMP(4);
    mx = fmaxf(mx, dpp<XOR2>(mx)); mx = fmaxf(mx, dpp<ROR4>(mx)); mx = fmaxf(mx, dpp<ROR8>(mx));
    mx = sw16_max(mx); mx = sw32_max(mx);
    const float msub = mx == -INFINITY ? 0.f : mx;
    float ls = 0.f;
    {
      float sv[8];
#pragma unroll
      for (int g = 0; g < 8; ++g) sv[g] = Pl[g * 64];
#pragma unroll
      for (int g = 0; g < 8; ++g) { const float p = __builtin_amdgcn_exp2f((sv[g] - msub) * SC2); ls += p; Pl[g * 64] = p; }
    }
    ls += dpp<XOR2>(ls); ls += dpp<ROR8>(ls); ls += dpp<ROR4>(ls);
    ls = sw16_add(ls, ls); ls = sw32_add(ls, ls);
    *(LAS v2f*)(L + (lane < 2 ? L_ML + (wv * 2 + lane) * 8 : L_SINK + 1024 + lane * 8)) = v2f{mx, ls};
    lds_signal(L, FL_ML, lane, 1u);
    v2f a0[4], a1[4];
#pragma unroll
    for (int k = 0; k < 4; ++k) { a0[k] = v2f{0.f, 0.f}; a1[k] = v2f{0.f, 0.f}; }
    const LAS v4f* Pr = (const LAS v4f*)(P + hf * 8);
    LAS v4f* mo = (LAS v4f*)(L + L_MRG + wv * 2048 + lane * 32);
    auto v_group = [&](int g, auto last) {
      v4f pa[4], pb[4];
#pragma unroll
      for (int d = 0; d < 4; ++d) { pa[d] = Pr[(g * 4 + d) * 4]; pb[d] = Pr[(g * 4 + d) * 4 + 1]; }
#pragma unroll
      for (int d = 0; d < 4; ++d) {
        if (!last.value || d + LEAD < 4) {
          LANDED();
          pace2(tb, 32 + g * 4 + d + LEAD);
          issue_turn(R[(d + LEAD) & 3], rv, voff, ROW(g * 4 + d + LEAD));
        }
        v_turn(R[d], pa[d], pb[d], a0, a1);
        SB();
        if (!last.value && d == 1) lds_signal(L, FL_PRE, lane, g == PREG ? 1u : 0u);
      }
    };
#pragma unroll 1
    for (int g = 0; g < 7; ++g) v_group(g, BoolC<false>{});
    v_group(7, BoolC<true>{});
    mo[0] = v4f{sw32_add(a0[0].x, a1[0].x), sw32_add(a0[0].y, a1[0].y), sw32_add(a0[1].x, a1[1].x), sw32_add(a0[1].y, a1[1].y)};
    mo[1] = v4f{sw32_add(a0[2].x, a1[2].x), sw32_add(a0[2].y, a1[2].y), sw32_add(a0[3].x, a1[3].x), sw32_add(a0[3].y, a1[3].y)};
    lds_signal(L, FL_DONE, lane, 1u);
    STAMP(5);
    if (isM) {
      const __amdgpu_buffer_rsrc_t r2 = __builtin_amdgcn_make_buffer_rsrc((void*)(w2 + m * 64 * D), 0, 64 * D * 2, RSRC3);
      lds_wait_ge(FL + FL_PUB, 2u, 0);
      __builtin_amdgcn_s_sleep(2);
      v4u uu[16];
#pragma unroll
      for (int i = 0; i < 16; ++i) uu[i] = LD128(r2, lane < 56 ? lane * 16 : 64 * D * 2, (i * 4 + wv) * (D * 2), AUX_NT);
      const int aw = lane < 56 ? L_RAW2 + wv * W2P + lane * 16 : L_SINK + lane * 16, aws = lane < 56 ? 4 * W2P : 0;
#pragma unroll
      for (int i = 0; i < 16; ++i) *(LAS v4u*)(L + aw + i * aws) = uu[i];
      STAMP(19);
      const unsigned tagl = __builtin_amdgcn_readfirstlane(FL[FL_TAGL]), th = __builtin_amdgcn_readfirstlane(FL[FL_TAGH]);
      const __amdgpu_buffer_rsrc_t rt = __builtin_amdgcn_make_buffer_rsrc((void*)(ws + (size_t)(ROT ? (th & 31u) + 1u : 0u) * WS_SPAN), 0, WS_END, RSRC3);
      const int g = lane >> 4;
      int p_st = WS_PF + ((m * 28 + wv * 7) * 8 + (lane & 15)) * 64 + g * 16;
      int a_br = L_BF + (lane & 15) * 144 + g * 16;
      int a2 = L_RAW2 + (g * 4 + ((lane & 15) >> 2)) * W2P + wv * 224 + (lane & 3) * 8;
      asm volatile("" : "+v"(p_st), "+v"(a_br), "+v"(a2));
      mlp_ln(L, rt, wv, lane, tagl, tb);
      __builtin_amdgcn_s_sleep(W2D);
      v4u Fr[28];
#pragma unroll
      for (int i = 0; i < 14; ++i) Fr[14 + i] = frag_tr(L + a2 + (i & 1) * 32 * W2P + (i >> 1) * 32, 16 * W2P);
      BAR();
      const v4u h0 = *(const LAS v4u*)(L + a_br), h1 = *(const LAS v4u*)(L + a_br + 64);
      const bool up = lane & 8;
      v4u ev[4];
#pragma unroll
      for (int i = 0; i < 4; ++i) {
        v4f ea = mfma(Fr[14 + 4 * i], h0, v4f{0.f, 0.f, 0.f, 0.f}), eb = ea;
        if (i < 3) eb = mfma(Fr[14 + 4 * i + 2], h0, v4f{0.f, 0.f, 0.f, 0.f});
        ea = mfma(Fr[14 + 4 * i + 1], h1, ea);
        if (i < 3) eb = mfma(Fr[14 + 4 * i + 3], h1, eb);
        ev[i] = pack24(v4f{up ? eb.x : ea.x, up ? eb.y : ea.y, up ? eb.z : ea.z, up ? eb.w : ea.w}, tagl);
        ST128(ev[i], rt, (i == 3 && up) ? WS_END : p_st + i * 1024, 0, AUX_SC1);
      }
      asm volatile("" :: "v"(ev[0]), "v"(ev[1]), "v"(ev[2]), "v"(ev[3]));
      STAMP(16);
    }
  }
}
}

extern "C" void kernel_launch(void* const* d_in, const int* in_sizes, int n_in, void* d_out, int out_size, void* d_ws,
                              size_t ws_size, hipStream_t stream) {
  using namespace fk;
  (void)hipFuncSetAttribute((const void*)fused_decode, hipFuncAttributeMaxDynamicSharedMemorySize, L_END + DBG_STAMPS * 2048);
  hipLaunchKernelGGL(fused_decode, dim3(256), dim3(512), L_END + DBG_STAMPS * 2048, stream, (const u16*)d_in[0], (const u16*)d_in[1],
                     (const u16*)d_in[2], (const u16*)d_in[3], (const u16*)d_in[4], (const u16*)d_in[6], (const u16*)d_in[7],
                     (const u16*)d_in[8], (const u16*)d_in[9], (u16*)d_out, (unsigned char*)d_ws);
}
```

```cpp
#define HDELAY 50
#define HSP 20
#define QT 600
#include <hip/hip_runtime.h>
#include <hip/hip_bf16.h>
#include <stdint.h>

#define DBG_STAMPS 0
#define TAU0 950
#define NSTAR 10
#define TGT 860
#define OGAIN 7
#define VPRE 2
#define VPT 1500
#define VOUT 2
#define OAUX 0
#define LDSKB 96
#define WOL 100
#define W1H 11
#define W1L 60
#define W2D 2
#define W1M 4
#define VDENSE 65536
#define PRI 0
#define XAUX 0
#define CAUX 0
#define SENT_R5 5
#define TAU1 890
#define TAU 886
#define QSLEEP 24
#define WOT 1100
#define WOS 40
#define GOFF 0
#define WT 188
#define G1 87
#define G2 79
#define G3 160
#define WDELAY 6
#define RDELAY 8
#define FWAIT 60
#define PREG 6
#define CODE_BYTES 36480
#define W1E 1
#define ROT 1
#define EARLY 2
#define SLPT 0

static_assert(W1H - W1M >= 4 && W1H <= 14, "the wo tile takes four loads per wave");
namespace fk {
typedef unsigned short u16;
typedef unsigned long long u64;
typedef unsigned v4u __attribute__((ext_vector_type(4)));
typedef unsigned v2u __attribute__((ext_vector_type(2)));
typedef float v4f __attribute__((ext_vector_type(4)));
typedef float v2f __attribute__((ext_vector_type(2)));
typedef __bf16 bf16x2 __attribute__((ext_vector_type(2)));
typedef __bf16 bf16x8 __attribute__((ext_vector_type(8)));
typedef short s16x4 __attribute__((ext_vector_type(4)));
#define LAS __attribute__((address_space(3)))
#define GAS __attribute__((address_space(1)))

constexpr int D = 448, F = 1792, SK = 4096, DH = 256, NQ = 4096;
constexpr int AUX_NT = 2, AUX_SC1 = 16;
constexpr int NEXTRA = 3;
constexpr int LEAD = 2;
constexpr int PN0 = 16;
constexpr int RT = WT + G1, MT = RT + G2, FT = MT + G3;
constexpr int RSRC3 = 0x00020000;
constexpr float SC2 = 0.0625f * 1.4426950408889634f;
struct Spin {
  unsigned n = 0, t0 = 0;
  __device__ __forceinline__ bool expired() {
    if ((++n & 4095u) != 0u) return false;
    const unsigned t = (unsigned)__builtin_amdgcn_s_memrealtime();
    if (n == 4096u) t0 = t;
    return t - t0 > (1u << 27);
  }
};

constexpr int WS_EPOCH = 0;
constexpr int WS_T = 2048;
constexpr int WS_G = 4096;
constexpr int WS_ML = WS_G + 8 * 8 * 4 * 128 * 16;
constexpr int WS_H = WS_ML + 64 * 8 * 16;
constexpr int WS_P0 = WS_H + 16384;
constexpr int WS_PW = WS_P0 + 64 * 4 * 2048;
constexpr int WS_PF = WS_PW + 32 * 28 * 512;
constexpr int WS_END = WS_PF + 28 * 28 * 512;
constexpr int WS_SPAN = 2 << 20;
static_assert(WS_END <= WS_SPAN, "span");
constexpr int WS_DBG = 128 << 20;

constexpr int LN_STR = 912;
constexpr int L_P = 0;
constexpr int L_MRG = 8192;
constexpr int L_Q = 16384;
constexpr int L_FLAG = 17408;
constexpr int L_ML = L_FLAG + 64;
constexpr int L_LN = L_ML + 64;
constexpr int L_BF = L_LN + 8 * LN_STR;
constexpr int L_SINK = L_BF + 4096;
constexpr int L_RAW = L_SINK + 2048;
constexpr int L_RAW2 = L_RAW + 57344;
constexpr int W2P = 928, WOP = 160;
constexpr int L_END = L_RAW2 + 64 * W2P;
static_assert(L_END + 2048 <= 160 * 1024 && L_END > 80 * 1024, "lds");
static_assert(L_LN % 16 == 0 && L_BF % 16 == 0 && L_RAW % 16 == 0, "align");
enum { FL_LN = 0, FL_QP = 1, FL_Q = 2, FL_PRE = 3, FL_DONE = 4, FL_RAW = 5, FL_RAW2 = 6, FL_RAW3 = 7, FL_ML = 9, FL_TAGL = 10, FL_TAGH = 11, FL_TG = 12, FL_ORG = 13, FL_TGOK = 14, FL_PUB = 15 };

__device__ __forceinline__ float bflo(unsigned v) { return __builtin_bit_cast(float, v << 16); }
__device__ __forceinline__ float bfhi(unsigned v) { return __builtin_bit_cast(float, v & 0xffff0000u); }
__device__ __forceinline__ float rbf(float f) { return (float)(__bf16)f; }
__device__ __forceinline__ unsigned pk2(float lo, float hi) {
  bf16x2 v = {(__bf16)lo, (__bf16)hi};
  return __builtin_bit_cast(unsigned, v);
}
__device__ __forceinline__ unsigned pk2o(float lo, float hi) {
  unsigned p = pk2(lo, hi);
  asm("" : "+v"(p));
  return p;
}
__device__ __forceinline__ float dot2(unsigned a, unsigned b, float c) {
  return __builtin_amdgcn_fdot2_f32_bf16(__builtin_bit_cast(bf16x2, a), __builtin_bit_cast(bf16x2, b), c, false);
}
__device__ __forceinline__ v4f mfma(v4u a, v4u b, v4f c) {
  return __builtin_amdgcn_mfma_f32_16x16x32_bf16(__builtin_bit_cast(bf16x8, a), __builtin_bit_cast(bf16x8, b), c, 0, 0, 0);
}
template <int CTRL> __device__ __forceinline__ float dpp(float x) {
  return __builtin_bit_cast(float, __builtin_amdgcn_mov_dpp(__builtin_bit_cast(int, x), CTRL, 0xf, 0xf, true));
}
constexpr int XOR1 = 0xB1, XOR2 = 0x4E, HMIR = 0x141, ROR4 = 0x124, ROR8 = 0x128;
__device__ __forceinline__ float sw16_add(float a, float b) {
  auto s = __builtin_amdgcn_permlane16_swap(__float_as_uint(a), __float_as_uint(b), false, false);
  return __uint_as_float(s[0]) + __uint_as_float(s[1]);
}
__device__ __forceinline__ float sw32_add(float a, float b) {
  auto s = __builtin_amdgcn_permlane32_swap(__float_as_uint(a), __float_as_uint(b), false, false);
  return __uint_as_float(s[0]) + __uint_as_float(s[1]);
}
__device__ __forceinline__ float sw16_max(float a) {
  auto s = __builtin_amdgcn_permlane16_swap(__float_as_uint(a), __float_as_uint(a), false, false);
  return fmaxf(__uint_as_float(s[0]), __uint_as_float(s[1]));
}
__device__ __forceinline__ float sw32_max(float a) {
  auto s = __builtin_amdgcn_permlane32_swap(__float_as_uint(a), __float_as_uint(a), false, false);
  return fmaxf(__uint_as_float(s[0]), __uint_as_float(s[1]));
}
__device__ __forceinline__ v4u pack24(v4f v, unsigned tag) {
  const unsigned a = __float_as_uint(v.x), b = __float_as_uint(v.y), c = __float_as_uint(v.z), d = __float_as_uint(v.w);
  return v4u{__builtin_amdgcn_perm(b, a, 0x05030201u), __builtin_amdgcn_perm(c, b, 0x06050302u), __builtin_amdgcn_perm(d, c, 0x07060503u), tag};
}
__device__ __forceinline__ v4f unpack24(v4u g) {
  return v4f{__uint_as_float(g.x << 8), __uint_as_float(__builtin_amdgcn_perm(g.y, g.x, 0x0504030Cu)),
             __uint_as_float(__builtin_amdgcn_perm(g.z, g.y, 0x0403020Cu)), __uint_as_float(g.z & 0xFFFFFF00u)};
}
__device__ __forceinline__ v4u pack28(v4f v, unsigned tag) {
  return v4u{(__float_as_uint(v.x) & ~3u) | (tag & 3u), (__float_as_uint(v.y) & ~3u) | ((tag >> 2) & 3u),
             (__float_as_uint(v.z) & ~3u) | ((tag >> 4) & 3u), (__float_as_uint(v.w) & ~3u) | ((tag >> 6) & 3u)};
}
__device__ __forceinline__ v4f unpack28(v4u g) { return __builtin_bit_cast(v4f, g & ~3u); }
__device__ __forceinline__ bool bad28(v4u g, unsigned tag) {
  return (((g.x ^ tag) | (g.y ^ (tag >> 2)) | (g.z ^ (tag >> 4)) | (g.w ^ (tag >> 6))) & 3u) != 0u;
}
#define LGKM0() asm volatile("s_waitcnt lgkmcnt(0)" ::: "memory")
#define CLOB() asm volatile("" ::: "memory")
#define SB() __builtin_amdgcn_sched_barrier(0)
#define BAR() do { asm volatile("s_waitcnt lgkmcnt(0)" ::: "memory"); __builtin_amdgcn_s_barrier(); asm volatile("" ::: "memory"); __builtin_amdgcn_sched_barrier(0); } while (0)
#define LD128(rs, vo, so, aux) __builtin_amdgcn_raw_buffer_load_b128(rs, vo, so, aux)
#define ST128(v, rs, vo, so, aux) __builtin_amdgcn_raw_buffer_store_b128(v, rs, vo, so, aux)

#define STAMP(i) do {} while (0)

__device__ __forceinline__ void lds_wait_ge(volatile LAS unsigned* f, unsigned want, int slp) {
  Spin sp;
#pragma nounroll
  while (*f < want) {
    if (slp) __builtin_amdgcn_s_sleep(8);
    if (sp.expired()) break;
  }
  CLOB();
}
__device__ __forceinline__ void lds_signal(LAS unsigned char* L, int word, int lane, unsigned inc) {
  LGKM0();
  LAS unsigned* p = (LAS unsigned*)(L + (lane == 0 ? L_FLAG + word * 4 : L_SINK + lane * 4));
  __hip_atomic_fetch_add(p, inc, __ATOMIC_RELAXED, __HIP_MEMORY_SCOPE_WORKGROUP);
}
template <int N, int STR, int WIDE, int SLP>
__device__ __forceinline__ void gather(v4u (&v)[N], __amdgpu_buffer_rsrc_t rs, int off, unsigned tl, unsigned th, u64 last_mask) {
  Spin sp;
#pragma nounroll
  for (;;) {
    CLOB();
#pragma unroll
    for (int i = 0; i < N; ++i) v[i] = LD128(rs, off + i * STR, 0, AUX_SC1);
    u64 any = 0ull;
#pragma unroll
    for (int i = 0; i < N; ++i) {
      const u64 bad = __builtin_amdgcn_ballot_w64(WIDE == 2 ? bad28(v[i], tl) : WIDE ? ((v[i].z ^ tl) | (v[i].w ^ th)) != 0u : v[i].w != tl);
      any |= i == N - 1 ? bad & last_mask : bad;
    }
    if (any == 0ull || sp.expired()) break;
    if (SLP) __builtin_amdgcn_s_sleep(SLP);
  }
}
__device__ __forceinline__ void wait_until(unsigned until) {
  asm volatile("8:\n\ts_memrealtime vcc\n\ts_waitcnt lgkmcnt(0)\n\ts_sub_i32 vcc_lo, vcc_lo, %0\n\ts_cmp_lt_i32 vcc_lo, 0\n\ts_cbranch_scc1 8b\n" :: "s"(until) : "scc", "vcc", "memory");
}
__device__ __forceinline__ void doze_until(unsigned until) {
#pragma nounroll
  while ((int)((unsigned)__builtin_amdgcn_s_memrealtime() - until) < 0) __builtin_amdgcn_s_sleep(4);
}
constexpr int PN1 = 34;
constexpr int SCHED63 = (PN0 * TAU0 + (PN1 - PN0) * TAU1 + (63 - PN1) * TAU) >> 4;
__device__ __forceinline__ void pace(unsigned tb, int n) { wait_until(tb + (unsigned)((n < PN0 ? n * TAU0 : PN0 * TAU0 + (n - PN0) * TAU1) >> 4)); }
__device__ __forceinline__ void pace1(unsigned tb, int n) { wait_until(tb + (unsigned)((PN0 * TAU0 + (n - PN0) * TAU1) >> 4)); }
__device__ __forceinline__ void pace2(unsigned tb, int n) { wait_until(tb + (unsigned)((PN0 * TAU0 + (PN1 - PN0) * TAU1 + (n - PN1) * TAU) >> 4)); }
template <bool B> struct BoolC { static constexpr bool value = B; };
__device__ __forceinline__ void issue_turn(v4u* r, __amdgpu_buffer_rsrc_t rs, int voff, int soff) {
#pragma unroll
  for (int i = 0; i < 4; ++i) r[i] = LD128(rs, voff + i * 1024, soff, AUX_NT);
  SB();
}
__device__ __forceinline__ float k_turn(const v4u* k, v4u q0, v4u q1, bool b0, bool b1, bool b2) {
  float x[8];
#pragma unroll
  for (int i = 0; i < 4; ++i) {
    float a0 = dot2(k[i].x, q0.x, 0.f), a1 = dot2(k[i].x, q1.x, 0.f);
    a0 = dot2(k[i].y, q0.y, a0); a1 = dot2(k[i].y, q1.y, a1);
    a0 = dot2(k[i].z, q0.z, a0); a1 = dot2(k[i].z, q1.z, a1);
    a0 = dot2(k[i].w, q0.w, a0); a1 = dot2(k[i].w, q1.w, a1);
    x[2 * i] = a0; x[2 * i + 1] = a1;
  }
  float y[4], z[2];
#pragma unroll
  for (int v = 0; v < 4; ++v) y[v] = (b2 ? x[v + 4] : x[v]) + dpp<HMIR>(b2 ? x[v] : x[v + 4]);
#pragma unroll
  for (int v = 0; v < 2; ++v) z[v] = (b1 ? y[v + 2] : y[v]) + dpp<XOR2>(b1 ? y[v] : y[v + 2]);
  return (b0 ? z[1] : z[0]) + dpp<XOR1>(b0 ? z[0] : z[1]);
}
__device__ __forceinline__ float k_group(float r0, float r1, float r2, float r3, bool b3) {
  const float u0 = (b3 ? r1 : r0) + dpp<ROR8>(b3 ? r0 : r1);
  const float u1 = (b3 ? r3 : r2) + dpp<ROR8>(b3 ? r2 : r3);
  return rbf(sw16_add(u0, u1));
}
__device__ __forceinline__ void v_turn(const v4u* v, v4f pa, v4f pb, v2f (&a0)[4], v2f (&a1)[4]) {
#pragma unroll
  for (int i = 0; i < 4; ++i) {
    const float p0 = i == 0 ? pa.x : i == 1 ? pa.z : i == 2 ? pb.x : pb.z;
    const float p1 = i == 0 ? pa.y : i == 1 ? pa.w : i == 2 ? pb.y : pb.w;
#pragma unroll
    for (int j = 0; j < 4; ++j) {
      const v2f e = {bflo(v[i][j]), bfhi(v[i][j])};
      a0[j] = __builtin_elementwise_fma(e, v2f{p0, p0}, a0[j]);
      a1[j] = __builtin_elementwise_fma(e, v2f{p1, p1}, a1[j]);
    }
  }
}
template <bool FULL> __device__ __forceinline__ float row_sum(float v) {
  v += dpp<XOR1>(v); v += dpp<XOR2>(v); v += dpp<HMIR>(v); v += dpp<ROR8>(v);
  v = sw16_add(v, v);
  return FULL ? sw32_add(v, v) : v;
}
template <int NW> __device__ __forceinline__ void ln_row(unsigned (&x)[NW], bool act) {
  v2f v[NW];
#pragma unroll
  for (int k = 0; k < NW; ++k) v[k] = v2f{bflo(x[k]), bfhi(x[k])};
  float mean, var;
  if constexpr (NW == 4) {
    const float c = bflo(__builtin_amdgcn_readfirstlane(x[0]));
    v2f s1 = {0.f, 0.f}, s2 = {0.f, 0.f};
#pragma unroll
    for (int k = 0; k < NW; ++k) { const v2f d = v[k] - c; s1 += d; s2 = __builtin_elementwise_fma(d, d, s2); }
    float t = sw32_add(act ? s1.x + s1.y : 0.f, act ? s2.x + s2.y : 0.f);
    t = row_sum<false>(t) * (1.0f / D);
    const float m1 = __builtin_bit_cast(float, __builtin_amdgcn_readlane(__builtin_bit_cast(int, t), 0));
    const float m2 = __builtin_bit_cast(float, __builtin_amdgcn_readlane(__builtin_bit_cast(int, t), 32));
    mean = c + m1;
    var = fmaxf(fmaf(-m1, m1, m2), m2 - m2);
  } else {
    constexpr unsigned ONES = 0x3F803F80u;
    float sa = 0.f, sb = 0.f;
#pragma unroll
    for (int k = 0; k < NW; k += 2) { sa = dot2(x[k], ONES, sa); sb = dot2(x[k + 1], ONES, sb); }
    mean = row_sum<false>(sa + sb) * (1.0f / D);
    v2f q0 = {0.f, 0.f}, q1 = {0.f, 0.f};
#pragma unroll
    for (int k = 0; k < NW; k += 2) {
      const v2f d0 = v[k] - mean, d1 = v[k + 1] - mean;
      q0 = __builtin_elementwise_fma(d0, d0, q0); q1 = __builtin_elementwise_fma(d1, d1, q1);
    }
    q0 += q1;
    var = row_sum<false>(act ? q0.x + q0.y : 0.f) * (1.0f / D);
  }
  const float mb = rbf(mean);
#pragma unroll
  for (int k = 0; k < NW; ++k) { const v2f d = v[k] - mb; const unsigned p = pk2o(d.x, d.y); v[k] = v2f{bflo(p), bfhi(p)}; }
  const float den = rbf(__builtin_amdgcn_sqrtf(rbf(rbf(var) + 1e-5f)));
  const float ri = __builtin_amdgcn_rcpf(den);
#pragma unroll
  for (int k = 0; k < NW; ++k) { const v2f a = v[k] * ri; x[k] = pk2(a.x, a.y); }
}
__device__ __forceinline__ v4u frag_tr(const LAS unsigned char* p, int hi) {
#if defined(__HIP_DEVICE_COMPILE__)
  const v2u a = __builtin_bit_cast(v2u, __builtin_amdgcn_ds_read_tr16_b64_v4i16((LAS s16x4*)p));
  const v2u b = __builtin_bit_cast(v2u, __builtin_amdgcn_ds_read_tr16_b64_v4i16((LAS s16x4*)(p + hi)));
  return v4u{a.x, a.y, b.x, b.y};
#else
  return v4u{0u, 0u, 0u, 0u};
#endif
}
__device__ __forceinline__ v4f red16(v4f t) {
#pragma unroll
  for (int k = 0; k < 4; ++k) { float a = t[k]; a += dpp<ROR8>(a); a += dpp<ROR4>(a); a = sw16_add(a, a); a = sw32_add(a, a); t[k] = a; }
  return t;
}
__device__ __forceinline__ void mlp_ln(LAS unsigned char* L, __amdgpu_buffer_rsrc_t rt, int row, int lane, unsigned tl, unsigned t0) {
  const bool act = lane < 56;
  const int a = act ? L_LN + row * LN_STR + lane * 8 : L_SINK + lane * 8;
  v4u hg[2];
  int o0 = WS_H + (row * 112 + (act ? lane : 55)) * 16;
  asm volatile("" : "+v"(o0));
  wait_until(t0 + SCHED63 + MT);
  gather<2, 896, 0, SLPT>(hg, rt, o0, tl, 0u, ~0ull);
  { const int wv = row; (void)wv; STAMP(17); }
  unsigned x[4] = {hg[0].x, hg[0].y, hg[1].x, hg[1].y};
  ln_row<4>(x, act);
  *(LAS v2u*)(L + a) = v2u{x[0], x[1]}; *(LAS v2u*)(L + a + 448) = v2u{x[2], x[3]};
  BAR();
}

__global__ void __launch_bounds__(512, 2)
fused_decode(const u16* __restrict__ x, const u16* __restrict__ kc, const u16* __restrict__ vc, const u16* __restrict__ wq,
             const u16* __restrict__ wo, const u16* __restrict__ w1, const u16* __restrict__ b1, const u16* __restrict__ w2,
             const u16* __restrict__ b2, u16* __restrict__ out, unsigned char* __restrict__ ws) {
  extern __shared__ __attribute__((aligned(16))) unsigned char smem[];
  LAS unsigned char* L = (LAS unsigned char*)smem;
  const u64 pc0 = __builtin_amdgcn_s_getpc() & ~0xFFull;
  const int w = blockIdx.x, tid = threadIdx.x, lane = tid & 63, wv = __builtin_amdgcn_readfirstlane(tid >> 6);
  const int kvh = w & 7, r5 = w >> 3, b = r5 & 7, s = r5 >> 3, grp = b * 8 + kvh;
  const int m = (r5 - 28) * 8 + kvh;
  const bool isM = r5 >= 28 && m < 28;
  volatile LAS unsigned* FL = (volatile LAS unsigned*)(L + L_FLAG);
  const __amdgpu_buffer_rsrc_t rws = __builtin_amdgcn_make_buffer_rsrc((void*)ws, 0, WS_END, RSRC3);
  auto code_prefetch = [&]() {
    const __amdgpu_buffer_rsrc_t rc = __builtin_amdgcn_make_buffer_rsrc((void*)pc0, 0, CODE_BYTES, RSRC3);
    constexpr int CPL = (CODE_BYTES / 128 + 31) / 32;
    return __builtin_amdgcn_raw_buffer_load_b32(rc, lane < CPL ? (lane * 32 + r5) * 128 : CODE_BYTES, 0, CAUX);
  };
  unsigned cpf = 0;
  if (wv == 4) cpf = code_prefetch();
  SB();
  asm volatile("" :: "s"(x), "s"(kc), "s"(vc), "s"(wq), "s"(wo), "s"(w1), "s"(b1), "s"(w2), "s"(b2), "s"(out), "s"(ws));

  if (__builtin_expect(wv >= 4, 0)) {
    const int hw = wv - 4;
    STAMP(0);
    __builtin_amdgcn_s_setprio(3);
    const unsigned tstart = (unsigned)__builtin_amdgcn_s_memrealtime();
    const bool isW = r5 < 28;
    const int t = r5 >> 2, kq = r5 & 3;
    const int g = lane >> 4;
    const LAS unsigned char* ap = L + L_RAW + (g * 8 + ((lane & 15) >> 2)) * 128 + hw * 32 + (lane & 3) * 8;
    const LAS unsigned char* bp = L + L_LN + (lane & 7) * LN_STR + g * 16;
    const __amdgpu_buffer_rsrc_t rx = __builtin_amdgcn_make_buffer_rsrc((void*)x, 0, 8 * D * 2, RSRC3);
    if (HDELAY) { if (tid < 256 + 16) FL[tid - 256] = 0u; BAR(); doze_until(tstart + HDELAY + (HSP ? (r5 & 7) * HSP : 0)); }
    const v2u ep = __builtin_amdgcn_raw_buffer_load_b64(rws, WS_EPOCH, 0, AUX_SC1);
    unsigned tagl, tagh;
#define MAKE_TAGS() do { const unsigned el = __builtin_amdgcn_readfirstlane(ep.x), eh = __builtin_amdgcn_readfirstlane(ep.y); \
      u64 z = (((u64)eh << 32) | el) + 0x9E3779B97F4A7C15ull; \
      z = (z ^ (z >> 30)) * 0xBF58476D1CE4E5B9ull; z = (z ^ (z >> 27)) * 0x94D049BB133111EBull; z ^= z >> 31; \
      tagl = (((unsigned)z ^ el) & 3u) == 0u ? (unsigned)z ^ 1u : (unsigned)z; \
      tagl = ((tagl ^ (tagl >> 2)) & 3u) == 0u ? tagl ^ 4u : tagl;     \
      tagh = ((unsigned)(z >> 32) & ~31u) | ((eh + 1u) & 31u); } while (0)
    if (!HDELAY && tid < 256 + 16) FL[tid - 256] = 0u;
    {
      const int row = hw * 2 + (lane >> 5), c = lane & 31;
      const int ks0 = s < 3 ? s * 4 : 11, nks = s < 2 ? 4 : 3;
      const int xo = c < 28 ? row * (D * 2) + c * 32 : 8 * D * 2;
      const v4u xa = LD128(rx, xo, 0, XAUX), xb = LD128(rx, xo + 16, 0, XAUX);
      const __amdgpu_buffer_rsrc_t rq = __builtin_amdgcn_make_buffer_rsrc((void*)(wq + ks0 * 32 * NQ + (kvh * 8 + b) * 64), 0, (nks * 32 - 1) * (NQ * 2) + 128, RSRC3);
      v4u tt[4];
#pragma unroll
      for (int i = 0; i < 4; ++i) tt[i] = LD128(rq, (lane >> 3) * (NQ * 2) + (lane & 7) * 16, (i * 4 + hw) * 8 * (NQ * 2), AUX_NT);
      SB();
      if (!HDELAY) BAR();
      {
        unsigned xr[8] = {xa.x, xa.y, xa.z, xa.w, xb.x, xb.y, xb.z, xb.w};
        ln_row<8>(xr, c < 28);
        const int a = c < 28 ? L_LN + row * LN_STR + c * 32 : L_SINK + lane * 32;
        *(LAS v4u*)(L + a) = v4u{xr[0], xr[1], xr[2], xr[3]}; *(LAS v4u*)(L + a + 16) = v4u{xr[4], xr[5], xr[6], xr[7]};
      }
      MAKE_TAGS();
      STAMP(1);
#pragma unroll
      for (int i = 0; i < 4; ++i) *(LAS v4u*)(L + L_RAW + (i * 4 + hw) * 1024 + lane * 16) = tt[i];
      lds_signal(L, FL_LN, lane, 1u);
      lds_wait_ge(FL + FL_LN, 4u, 0);
      v4f d0 = {0.f, 0.f, 0.f, 0.f};
#pragma unroll
      for (int ks = 0; ks < 3; ++ks) d0 = mfma(frag_tr(ap + ks * 4096, 512), *(const LAS v4u*)(bp + (ks0 + ks) * 64), d0);
      if (s < 2) d0 = mfma(frag_tr(ap + 3 * 4096, 512), *(const LAS v4u*)(bp + (ks0 + 3) * 64), d0);
      ST128(pack28(d0, tagl), rws, (lane & 8) == 0 ? WS_G + (((kvh * 8 + (lane & 7)) * 4 + s) * 128 + b * 16 + hw * 4 + g) * 16 : WS_END, 0, AUX_SC1);
      lds_signal(L, FL_QP, lane, 1u);
      STAMP(2);
    }
    auto q_gather = [&]() {
      const int off = WS_G + (kvh * 8 + b) * 8192 + ((hw - 2) * 64 + lane) * 16;
      doze_until(tstart + QT);
      v4u gq[4];
      gather<4, 2048, 2, QSLEEP>(gq, rws, off, tagl, 0u, ~0ull);
      const v4f qv = (unpack28(gq[0]) + unpack28(gq[1])) + (unpack28(gq[2]) + unpack28(gq[3]));
      *(LAS v2u*)(L + L_Q + ((hw - 2) * 64 + lane) * 8) = v2u{pk2(qv.x, qv.y), pk2(qv.z, qv.w)};
      FL[FL_TAGL] = tagl; FL[FL_TAGH] = tagh;
      lds_signal(L, FL_Q, lane, 2u);
      STAMP(3);
    };
    if (hw >= 2) q_gather();
    if (hw == 0 && r5 == SENT_R5) {
      lds_wait_ge(FL + FL_ORG, 1u, 1);
      const v2u og = *(volatile LAS v2u*)(L + L_ML);
      ST128((v4u{og.x, og.y, tagl, tagh}), rws, lane == 0 ? WS_T + kvh * 16 : WS_END, 0, AUX_SC1);
    }
    if (hw == 1) {
      doze_until(tstart + TGT);
      v4u gt[1];
      gather<1, 0, 1, 32>(gt, rws, WS_T + (lane & 7) * 16, tagl, tagh, ~0ull);
      const unsigned t0 = __builtin_amdgcn_readfirstlane(gt[0].x);
      int o = (int)(gt[0].x - t0) * 8 + (int)gt[0].y * OGAIN;
      o += __builtin_amdgcn_mov_dpp(o, XOR1, 0xf, 0xf, true); o += __builtin_amdgcn_mov_dpp(o, XOR2, 0xf, 0xf, true); o += __builtin_amdgcn_mov_dpp(o, HMIR, 0xf, 0xf, true);
      o >>= 6;
      o = o < -1000 ? -1000 : o > 2000 ? 2000 : o;
      const unsigned tgv = t0 + (unsigned)o + (unsigned)(GOFF);
      FL[FL_TG] = tgv - tstart + 4000u < 8000u ? tgv : tstart;
      lds_signal(L, FL_TGOK, lane, 1u);
      if (VPRE == 1) {
        const unsigned z0 = *(const GAS unsigned*)(vc + ((size_t)grp * SK + (lane & 1) * (SK - 1)) * DH);
        asm volatile("" :: "v"(z0));
      }
    }
    if (VPRE == 2 && hw == 0) {
      doze_until(tstart + VPT);
#define CAS __attribute__((address_space(4)))
      const unsigned z0 = *(const CAS unsigned*)(vc + (size_t)grp * SK * DH), z1 = *(const CAS unsigned*)(vc + ((size_t)grp * SK + SK - 1) * DH);
      const unsigned z2 = *(const CAS unsigned*)(isM ? w2 + m * 64 * D : wo + (kvh * 512 + kq * 128) * D), z3 = *(const CAS unsigned*)(isM ? w2 + (m * 64 + 63) * D : wo + (kvh * 512 + kq * 128 + 127) * D);
      const unsigned z4 = *(const CAS unsigned*)(ws + (size_t)((tagh & 31u) + 1u) * WS_SPAN), z5 = *(const CAS unsigned*)(ws + (size_t)((tagh & 31u) + 2u) * WS_SPAN - 64);
      const unsigned z6 = *(const CAS unsigned*)(w1 + (r5 & 1) * (D - 1) * F);
      asm volatile("" :: "s"(z0), "s"(z1), "s"(z2), "s"(z3), "s"(z4), "s"(z5), "s"(z6));
      if (VOUT == 1 || (VOUT == 2 && r5 == 1)) { const unsigned z7 = *(const CAS unsigned*)(out + (VOUT == 2 ? kvh * D : 0)), z8 = *(const CAS unsigned*)(out + 8 * D - 2); asm volatile("" :: "s"(z7), "s"(z8)); }
      if (VDENSE) {
#pragma unroll 1
        for (int o = r5 * VDENSE; o < D * F * 2; o += 32 * VDENSE) {
          const unsigned y0 = *(const CAS unsigned*)((const unsigned char*)w1 + o), y1 = *(const CAS unsigned*)((const unsigned char*)w2 + o);
          const unsigned y2 = *(const CAS unsigned*)((const unsigned char*)wo + kvh * (512 * D * 2) + (o < 512 * D * 2 ? o : 0));
          asm volatile("" :: "s"(y0), "s"(y1), "s"(y2));
        }
      }
    }
    lds_wait_ge(FL + FL_Q, 4u, 1);
    lds_wait_ge(FL + FL_QP, 4u, 1);
    v4f cx, cb;
    {
      const v2u bv = *((const GAS v2u*)(isW ? b2 + r5 * 16 + (lane & 3) * 4 : b1 + (isM ? m : 0) * 64 + hw * 16 + g * 4));
      const v2u xv = __builtin_amdgcn_raw_buffer_load_b64(rx, (kvh * D + r5 * 16 + (lane & 3) * 4) * 2, 0, 0);
      cb = v4f{bflo(bv.x), bfhi(bv.x), bflo(bv.y), bfhi(bv.y)};
      cx = v4f{bflo(xv.x), bfhi(xv.x), bflo(xv.y), bfhi(xv.y)};
    }
    const __amdgpu_buffer_rsrc_t rw = __builtin_amdgcn_make_buffer_rsrc(
        isW ? (void*)(wo + (kvh * 512 + kq * 128) * D + t * 64) : (void*)(w1 + (isM ? m : 0) * 64), 0, isW ? 127 * (D * 2) + 128 : isM ? 447 * (F * 2) + 128 : 0, RSRC3);
    const int pitch = isW ? D * 2 : F * 2;
    int wvo = (lane >> 3) * pitch + (lane & 7) * 16;
    asm volatile("" : "+v"(wvo));
    if (W1M && isM) {
      doze_until(tstart + WOT);
      v4u t1[W1M + 1];
#pragma unroll
      for (int i = 0; i < W1M; ++i) t1[i] = LD128(rw, wvo, (i * 4 + hw) * 8 * (F * 2), AUX_NT);
#pragma unroll
      for (int i = 0; i < W1M; ++i) *(LAS v4u*)(L + L_RAW + (i * 4 + hw) * 1024 + lane * 16) = t1[i];
    }
    asm volatile("" :: "v"(cpf));
    STAMP(4);
    const __amdgpu_buffer_rsrc_t rt = __builtin_amdgcn_make_buffer_rsrc((void*)(ws + (size_t)(ROT ? (tagh & 31u) + 1u : 0u) * WS_SPAN), 0, WS_END, RSRC3);
    float fw[4] = {0.f, 0.f, 0.f, 0.f};
    if (hw < 2) {
      lds_wait_ge(FL + FL_ML, 4u, 1);
      const v2f ml = *(const LAS v2f*)(L + L_ML + ((lane & 3) * 2 + hw) * 8);
      float Ms = ml.x;
      Ms = fmaxf(Ms, dpp<XOR1>(Ms)); Ms = fmaxf(Ms, dpp<XOR2>(Ms));
      const float Msub = Ms == -INFINITY ? 0.f : Ms;
      float Lw = __builtin_amdgcn_exp2f((ml.x - Msub) * SC2) * ml.y;
      Lw += dpp<XOR1>(Lw); Lw += dpp<XOR2>(Lw);
      ST128((v4u{__float_as_uint(Ms), __float_as_uint(Lw), tagl, tagh}), rt, lane == 0 ? WS_ML + ((grp * 2 + hw) * 4 + s) * 16 : WS_END, 0, AUX_SC1);
      const int off = WS_ML + ((grp * 2 + hw) * 4 + (lane & 3)) * 16;
      v4u gm1[1];
      __builtin_amdgcn_s_sleep(96); __builtin_amdgcn_s_sleep(96);
      gather<1, 0, 1, 64>(gm1, rt, off, tagl, tagh, ~0ull);
      const v4u gm = gm1[0];
      float M = __uint_as_float(gm.x);
      M = fmaxf(M, dpp<XOR1>(M)); M = fmaxf(M, dpp<XOR2>(M));
      float Ls = __builtin_amdgcn_exp2f((__uint_as_float(gm.x) - M) * SC2) * __uint_as_float(gm.y);
      Ls += dpp<XOR1>(Ls); Ls += dpp<XOR2>(Ls);
      const float f = __builtin_amdgcn_exp2f((ml.x - M) * SC2) / Ls;
#pragma unroll
      for (int i = 0; i < 4; ++i) fw[i] = __builtin_bit_cast(float, __builtin_amdgcn_readlane(__builtin_bit_cast(int, f), i));
    }
    STAMP(5);
    lds_wait_ge(FL + FL_TGOK, 1u, 1);
    const unsigned tg = __builtin_amdgcn_readfirstlane(FL[FL_TG]);
    asm volatile("" : "+v"(cx), "+v"(cb));
    int p_p0 = WS_P0 + ((kvh * 8 + b) * 4 + s) * 2048 + (hw & 1) * 1024 + lane * 16;
    int p_st = isW ? ((lane & 8) == 0 ? WS_PW + (((kvh * 4 + kq) * 28 + t * 4 + hw) * 8 + (lane & 7)) * 64 + g * 16 : WS_END)
                   : WS_PF + ((m * 28 + hw * 7) * 8 + (lane & 15)) * 64 + g * 16;
    int p_h = lane < 4 ? WS_H + (kvh * 112 + r5 * 4 + lane) * 16 : WS_END;
    int g_p0 = WS_P0 + (kvh * 8 + (lane >> 3)) * 8192 + (kq * 32 + hw * 8 + (lane & 7)) * 16;
    int g_pw = WS_PW + (((lane >> 2) * 28 + r5) * 8 + kvh) * 64 + (lane & 3) * 16;
    int g_o = lane < 4 ? (kvh * D + r5 * 16 + lane * 4) * 2 : 8 * D * 2;
    int g_e = (w == 0 && lane == 0) ? WS_EPOCH : WS_END;
    int a_bw = isW ? L_BF + hw * 512 + (lane >> 3) * 64 + (lane & 3) * 16 + ((lane >> 2) & 1) * 8 : L_BF + (lane & 15) * 144 + (hw >> 1) * 64 + g * 16 + (hw & 1) * 8;
    int a_br = isW ? L_BF + (lane & 7) * 64 + g * 16 : L_BF + (lane & 15) * 144 + g * 16;
    int a_ow = L_RAW + (hw * 8 + (lane >> 3)) * WOP + (lane & 7) * 16, a_or = L_RAW + (g * 4 + ((lane & 15) >> 2)) * WOP + hw * 32 + (lane & 3) * 8;
    asm volatile("" : "+v"(a_ow), "+v"(a_or));
    asm volatile("" : "+v"(p_p0), "+v"(p_st), "+v"(p_h), "+v"(g_p0), "+v"(g_pw), "+v"(g_o), "+v"(g_e), "+v"(a_bw), "+v"(a_br));
    const __amdgpu_buffer_rsrc_t rout = __builtin_amdgcn_make_buffer_rsrc((void*)out, 0, 8 * D * 2, RSRC3);
    lds_wait_ge(FL + FL_PRE, 4u, 1);
    STAMP(6);
    if (hw == 3) { const unsigned z = code_prefetch(); asm volatile("" :: "v"(z)); }
    v4u to[14];
    if (PRI) { __builtin_amdgcn_s_setprio(0); doze_until(tg + SCHED63 + (isW ? WOL : W1L) - 6); } else wait_until(tg + SCHED63 + (isW ? WOL : W1L));
#pragma unroll
    for (int i = W1M; i < W1H; ++i) to[i] = LD128(rw, wvo, ((isW ? i - W1M : i) * 4 + hw) * 8 * pitch, AUX_NT);
    SB();
    if (PRI >= 2 && hw >= 2) doze_until(tg + SCHED63 + WT - 40);
    lds_wait_ge(FL + FL_DONE, 4u, 0);
    if (hw < 2) {
      STAMP(7);
      const LAS v4f* mp = (const LAS v4f*)(L + L_MRG + hw * 1024 + lane * 16);
      const v4f o = (mp[0] * fw[0] + mp[128] * fw[1]) + (mp[256] * fw[2] + mp[384] * fw[3]);
      ST128(pack24(o, tagl), rt, p_p0, 0, AUX_SC1);
      if (isM) lds_signal(L, FL_PUB, lane, 1u);
      STAMP(8);
    }
    if (isW) {
      __builtin_amdgcn_s_sleep(WDELAY);
      wait_until(tg + SCHED63 + WT);
      v4u pv[4];
      {
        CLOB();
#pragma unroll
        for (int i = 0; i < 4; ++i) pv[i] = LD128(rt, g_p0 + i * 2048, 0, AUX_SC1);
        SB();
#pragma unroll
        for (int i = 0; i < 4; ++i) *(LAS v4u*)(L + a_ow + i * 32 * WOP) = to[W1M + i];
        SB();
        const u64 bad = __builtin_amdgcn_ballot_w64((((pv[0].w ^ tagl) | (pv[1].w ^ tagl)) | ((pv[2].w ^ tagl) | (pv[3].w ^ tagl))) != 0u);
        if (__builtin_expect(bad != 0ull, 0)) gather<4, 2048, 0, SLPT>(pv, rt, g_p0, tagl, 0u, ~0ull);
      }
      STAMP(9);
      const v4f o = (unpack24(pv[0]) + unpack24(pv[1])) + (unpack24(pv[2]) + unpack24(pv[3]));
      *(LAS v2u*)(L + a_bw) = v2u{pk2(o.x, o.y), pk2(o.z, o.w)};
      BAR();
      v4u bf[4], Fr[4];
#pragma unroll
      for (int ks = 0; ks < 4; ++ks) { Fr[ks] = frag_tr(L + a_or + ks * 32 * WOP, 16 * WOP); bf[ks] = *(const LAS v4u*)(L + a_br + ks * 512); }
      SB();
      v4f d0 = {0.f, 0.f, 0.f, 0.f}, d1 = {0.f, 0.f, 0.f, 0.f};
      d0 = mfma(Fr[0], bf[0], d0); d1 = mfma(Fr[1], bf[1], d1); d0 = mfma(Fr[2], bf[2], d0); d1 = mfma(Fr[3], bf[3], d1);
      ST128(pack24(d0 + d1, tagl), rt, p_st, 0, AUX_SC1);
      STAMP(10);
      if (hw == 0) {
        v4u u[2];
        __builtin_amdgcn_s_sleep(RDELAY);
        wait_until(tg + SCHED63 + RT);
        gather<2, 16 * 28 * 512, 0, SLPT>(u, rt, g_pw, tagl, 0u, ~0ull);
        STAMP(11);
        const v4f hv = red16(unpack24(u[0]) + unpack24(u[1]));
        const unsigned h01 = pk2o(cx.x + rbf(hv.x), cx.y + rbf(hv.y)), h23 = pk2o(cx.z + rbf(hv.z), cx.w + rbf(hv.w));
        ST128((v4u{h01, h23, tagh, tagl}), rt, p_h, 0, AUX_SC1);
        STAMP(12);
        __builtin_amdgcn_s_sleep(FWAIT);
        wait_until(tg + SCHED63 + FT);
        gather<2, 16 * 28 * 512, 0, SLPT>(u, rt, g_pw + (WS_PF - WS_PW), tagl, 0u, (1ull << 48) - 1ull);
        STAMP(13);
        const v4f ov = red16(unpack24(u[0]) + unpack24(u[1]));
        const unsigned o01 = pk2(bflo(h01) + rbf(rbf(ov.x) + cb.x), bfhi(h01) + rbf(rbf(ov.y) + cb.y));
        const unsigned o23 = pk2(bflo(h23) + rbf(rbf(ov.z) + cb.z), bfhi(h23) + rbf(rbf(ov.w) + cb.w));
        __builtin_amdgcn_raw_buffer_store_b64(v2u{o01, o23}, rout, g_o, 0, OAUX);
        __builtin_amdgcn_raw_buffer_store_b64(v2u{tagl, tagh}, rws, g_e, 0, AUX_SC1);
        STAMP(14);
      }
    } else if (isM) {
      v4u Fr[14];
      lds_wait_ge(FL + FL_PUB, 2u, 0);
#pragma unroll
      for (int i = W1H; i < 14; ++i) to[i] = LD128(rw, wvo, (i * 4 + hw) * 8 * (F * 2), AUX_NT);
      SB();
#pragma unroll
      for (int i = W1M; i < 14; ++i) *(LAS v4u*)(L + L_RAW + (i * 4 + hw) * 1024 + lane * 16) = to[i];
      lds_signal(L, FL_RAW, lane, 1u);
      lds_wait_ge(FL + FL_RAW, 4u, 0);
#pragma unroll
      for (int ks = 0; ks < 14; ++ks) Fr[ks] = frag_tr(ap + ks * 4096, 512);
#pragma unroll
      for (int ks = 0; ks < 14; ++ks) asm volatile("" : "+v"(Fr[ks]));
      STAMP(19);
      mlp_ln(L, rt, wv, lane, tagl, tg);
      STAMP(15);
      v4u bf[14];
#pragma unroll
      for (int ks = 0; ks < 14; ++ks) bf[ks] = *(const LAS v4u*)(bp + ks * 64);
      SB();
      v4f d0 = {0.f, 0.f, 0.f, 0.f}, d1 = {0.f, 0.f, 0.f, 0.f};
#pragma unroll
      for (int ks = 0; ks < 14; ks += 2) { d0 = mfma(Fr[ks], bf[ks], d0); d1 = mfma(Fr[ks + 1], bf[ks + 1], d1); }
      d0 += d1;
      const unsigned g01 = pk2o(d0.x, d0.y), g23 = pk2o(d0.z, d0.w);
      const unsigned a01 = pk2o(bflo(g01) + cb.x, bfhi(g01) + cb.y), a23 = pk2o(bflo(g23) + cb.z, bfhi(g23) + cb.w);
      float u[4] = {bflo(a01), bfhi(a01), bflo(a23), bfhi(a23)};
#pragma unroll
      for (int k = 0; k < 4; ++k) { const float a = u[k] > 0.f ? u[k] : 0.f; u[k] = a * a; }
      *(LAS v2u*)(L + a_bw) = v2u{pk2(u[0], u[1]), pk2(u[2], u[3])};
      BAR();
      STAMP(18);
    }
  } else {
    const size_t kvoff = ((size_t)grp * SK + (size_t)s * 32) * DH;
    constexpr int TSTR = 65536, NREC = 31 * 65536 + 16384;
#define ROW(n) (((n) & 31) * TSTR)
    const __amdgpu_buffer_rsrc_t rk = __builtin_amdgcn_make_buffer_rsrc((void*)(kc + kvoff), 0, NREC, RSRC3);
    const int voff = lane * 16 + wv * 4096;
    v4u X[NEXTRA * 4][4], R[4][4];
    unsigned tb = (unsigned)__builtin_amdgcn_s_memrealtime();
#define LANDED() asm volatile("s_waitcnt vmcnt(%0)" :: "i"(4 * (LEAD - 1)) : "memory")
    STAMP(0);
    if (!EARLY) BAR();
#pragma unroll
    for (int n = 0; n < NEXTRA * 4 + LEAD; ++n) {
      if (EARLY && n == EARLY) BAR();
      if (n >= LEAD) LANDED();
      if (n == 2) STAMP(20);
      if (n == 3) STAMP(21);
      if (n == 6) STAMP(22);
      if (n >= LEAD) pace(tb, n);
      if (n == NSTAR && wv == 0) {
        const unsigned late = (unsigned)__builtin_amdgcn_s_memrealtime() - tb - (unsigned)((NSTAR * TAU0) >> 4);
        *(volatile LAS v2u*)(L + L_ML) = v2u{tb, late};
        lds_signal(L, FL_ORG, lane, 1u);
      }
      issue_turn(n < NEXTRA * 4 ? X[n] : R[n - NEXTRA * 4], rk, voff, ROW(n));
    }
    const bool b0 = lane & 1, b1_ = lane & 2, b2_ = lane & 4, b3 = lane & 8;
    const int hf = lane >> 5, c = lane & 31;
    LAS float* P = (LAS float*)(L + L_P) + wv * 512;
    LAS float* Pl = P + ((lane >> 3) & 3) * 16 + hf * 8 + (lane & 7);
    STAMP(1);
    lds_wait_ge(FL + FL_Q, 4u, 0);
    STAMP(2);
#define NEW_TB() do { const unsigned ok_ = FL[FL_TGOK], t_ = FL[FL_TG]; tb = __builtin_amdgcn_readfirstlane(ok_ ? t_ : tb); } while (0)
    const v4u q0 = *(const LAS v4u*)(L + L_Q + c * 16), q1 = *(const LAS v4u*)(L + L_Q + 512 + c * 16);
    float mx = -INFINITY;
#pragma unroll
    for (int g = 0; g < NEXTRA; ++g) {
      float r[4], rx[4];
#pragma unroll
      for (int d = 0; d < 4; ++d) {
        rx[d] = k_turn(X[4 * g + d], q0, q1, b0, b1_, b2_);
        if ((d & 1) == 0) NEW_TB();
        SB();
        LANDED();
        pace(tb, (NEXTRA + g) * 4 + d + LEAD);
        issue_turn(R[(d + LEAD) & 3], rk, voff, ROW((NEXTRA + g) * 4 + d + LEAD));
        r[d] = k_turn(R[d], q0, q1, b0, b1_, b2_);
        SB();
      }
      const float sx = k_group(rx[0], rx[1], rx[2], rx[3], b3), sc = k_group(r[0], r[1], r[2], r[3], b3);
      mx = fmaxf(mx, fmaxf(sx, sc));
      Pl[g * 64] = sx; Pl[(NEXTRA + g) * 64] = sc;
      SB();
    }
    STAMP(3);
    lds_wait_ge(FL + FL_TGOK, 1u, 0);
    NEW_TB();
    const __amdgpu_buffer_rsrc_t rv = __builtin_amdgcn_make_buffer_rsrc((void*)(vc + kvoff), 0, NREC, RSRC3);
#pragma unroll 1
    for (int g = 2 * NEXTRA; g < 8; ++g) {
      float r[4];
#pragma unroll
      for (int d = 0; d < 4; ++d) {
        const int nx = g * 4 + d + LEAD;
        const bool tov = (d + LEAD >= 4) && g == 7;
        const __amdgpu_buffer_rsrc_t rn = __builtin_amdgcn_make_buffer_rsrc((void*)((tov ? vc : kc) + kvoff), 0, NREC, RSRC3);
        LANDED();
        pace1(tb, nx);
        issue_turn(R[(d + LEAD) & 3], rn, voff, ROW(nx));
        r[d] = k_turn(R[d], q0, q1, b0, b1_, b2_);
        SB();
      }
      const float sc = k_group(r[0], r[1], r[2], r[3], b3);
      mx = fmaxf(mx, sc);
      Pl[g * 64] = sc;
    }
    STAMP(4);
    mx = fmaxf(mx, dpp<XOR2>(mx)); mx = fmaxf(mx, dpp<ROR4>(mx)); mx = fmaxf(mx, dpp<ROR8>(mx));
    mx = sw16_max(mx); mx = sw32_max(mx);
    const float msub = mx == -INFINITY ? 0.f : mx;
    float ls = 0.f;
    {
      float sv[8];
#pragma unroll
      for (int g = 0; g < 8; ++g) sv[g] = Pl[g * 64];
#pragma unroll
      for (int g = 0; g < 8; ++g) { const float p = __builtin_amdgcn_exp2f((sv[g] - msub) * SC2); ls += p; Pl[g * 64] = p; }
    }
    ls += dpp<XOR2>(ls); ls += dpp<ROR8>(ls); ls += dpp<ROR4>(ls);
    ls = sw16_add(ls, ls); ls = sw32_add(ls, ls);
    *(LAS v2f*)(L + (lane < 2 ? L_ML + (wv * 2 + lane) * 8 : L_SINK + 1024 + lane * 8)) = v2f{mx, ls};
    lds_signal(L, FL_ML, lane, 1u);
    v2f a0[4], a1[4];
#pragma unroll
    for (int k = 0; k < 4; ++k) { a0[k] = v2f{0.f, 0.f}; a1[k] = v2f{0.f, 0.f}; }
    const LAS v4f* Pr = (const LAS v4f*)(P + hf * 8);
    LAS v4f* mo = (LAS v4f*)(L + L_MRG + wv * 2048 + lane * 32);
    auto v_group = [&](int g, auto last) {
      v4f pa[4], pb[4];
#pragma unroll
      for (int d = 0; d < 4; ++d) { pa[d] = Pr[(g * 4 + d) * 4]; pb[d] = Pr[(g * 4 + d) * 4 + 1]; }
#pragma unroll
      for (int d = 0; d < 4; ++d) {
        if (!last.value || d + LEAD < 4) {
          LANDED();
          pace2(tb, 32 + g * 4 + d + LEAD);
          issue_turn(R[(d + LEAD) & 3], rv, voff, ROW(g * 4 + d + LEAD));
        }
        v_turn(R[d], pa[d], pb[d], a0, a1);
        SB();
        if (!last.value && d == 1) lds_signal(L, FL_PRE, lane, g == PREG ? 1u : 0u);
      }
    };
#pragma unroll 1
    for (int g = 0; g < 7; ++g) v_group(g, BoolC<false>{});
    if (PRI) __builtin_amdgcn_s_setprio(2);
    v_group(7, BoolC<true>{});
    mo[0] = v4f{sw32_add(a0[0].x, a1[0].x), sw32_add(a0[0].y, a1[0].y), sw32_add(a0[1].x, a1[1].x), sw32_add(a0[1].y, a1[1].y)};
    mo[1] = v4f{sw32_add(a0[2].x, a1[2].x), sw32_add(a0[2].y, a1[2].y), sw32_add(a0[3].x, a1[3].x), sw32_add(a0[3].y, a1[3].y)};
    lds_signal(L, FL_DONE, lane, 1u);
    STAMP(5);
    if (isM) {
      const __amdgpu_buffer_rsrc_t r2 = __builtin_amdgcn_make_buffer_rsrc((void*)(w2 + m * 64 * D), 0, 64 * D * 2, RSRC3);
      lds_wait_ge(FL + FL_PUB, 2u, 0);
      __builtin_amdgcn_s_sleep(2);
      v4u uu[16];
#pragma unroll
      for (int i = 0; i < 16; ++i) uu[i] = LD128(r2, lane < 56 ? lane * 16 : 64 * D * 2, (i * 4 + wv) * (D * 2), AUX_NT);
      const int aw = lane < 56 ? L_RAW2 + wv * W2P + lane * 16 : L_SINK + lane * 16, aws = lane < 56 ? 4 * W2P : 0;
#pragma unroll
      for (int i = 0; i < 16; ++i) *(LAS v4u*)(L + aw + i * aws) = uu[i];
      STAMP(19);
      const unsigned tagl = __builtin_amdgcn_readfirstlane(FL[FL_TAGL]), th = __builtin_amdgcn_readfirstlane(FL[FL_TAGH]);
      const __amdgpu_buffer_rsrc_t rt = __builtin_amdgcn_make_buffer_rsrc((void*)(ws + (size_t)(ROT ? (th & 31u) + 1u : 0u) * WS_SPAN), 0, WS_END, RSRC3);
      const int g = lane >> 4;
      int p_st = WS_PF + ((m * 28 + wv * 7) * 8 + (lane & 15)) * 64 + g * 16;
      int a_br = L_BF + (lane & 15) * 144 + g * 16;
      int a2 = L_RAW2 + (g * 4 + ((lane & 15) >> 2)) * W2P + wv * 224 + (lane & 3) * 8;
      asm volatile("" : "+v"(p_st), "+v"(a_br), "+v"(a2));
      mlp_ln(L, rt, wv, lane, tagl, tb);
      __builtin_amdgcn_s_sleep(W2D);
      v4u Fr[28];
#pragma unroll
      for (int i = 0; i < 14; ++i) Fr[14 + i] = frag_tr(L + a2 + (i & 1) * 32 * W2P + (i >> 1) * 32, 16 * W2P);
      BAR();
      const v4u h0 = *(const LAS v4u*)(L + a_br), h1 = *(const LAS v4u*)(L + a_br + 64);
      const bool up = lane & 8;
      v4u ev[4];
#pragma unroll
      for (int i = 0; i < 4; ++i) {
        v4f ea = mfma(Fr[14 + 4 * i], h0, v4f{0.f, 0.f, 0.f, 0.f}), eb = ea;
        if (i < 3) eb = mfma(Fr[14 + 4 * i + 2], h0, v4f{0.f, 0.f, 0.f, 0.f});
        ea = mfma(Fr[14 + 4 * i + 1], h1, ea);
        if (i < 3) eb = mfma(Fr[14 + 4 * i + 3], h1, eb);
        ev[i] = pack24(v4f{up ? eb.x : ea.x, up ? eb.y : ea.y, up ? eb.z : ea.z, up ? eb.w : ea.w}, tagl);
        ST128(ev[i], rt, (i == 3 && up) ? WS_END : p_st + i * 1024, 0, AUX_SC1);
      }
      asm volatile("" :: "v"(ev[0]), "v"(ev[1]), "v"(ev[2]), "v"(ev[3]));
      STAMP(16);
    }
  }
}
}

extern "C" void kernel_launch(void* const* d_in, const int* in_sizes, int n_in, void* d_out, int out_size, void* d_ws,
                              size_t ws_size, hipStream_t stream) {
  using namespace fk;
  (void)hipFuncSetAttribute((const void*)fused_decode, hipFuncAttributeMaxDynamicSharedMemorySize, L_END + DBG_STAMPS * 2048);
  hipLaunchKernelGGL(fused_decode, dim3(256), dim3(512), L_END + DBG_STAMPS * 2048, stream, (const u16*)d_in[0], (const u16*)d_in[1],
                     (const u16*)d_in[2], (const u16*)d_in[3], (const u16*)d_in[4], (const u16*)d_in[6], (const u16*)d_in[7],
                     (const u16*)d_in[8], (const u16*)d_in[9], (u16*)d_out, (unsigned char*)d_ws);
}
```

```cpp
#include <hip/hip_runtime.h>
#include <hip/hip_bf16.h>
#include <stdint.h>

#define DBG_STAMPS 0
#define TAU0 950
#define NSTAR 10
#define TGT 860
#define OGAIN 7
#define VPRE 2
#define VPT 1500
#define VOUT 2
#define OAUX 0
#define LDSKB 96
#define WOL 100
#define W1H 11
#define W1L 60
#define W2D 2
#define W1M 4
#define VDENSE 65536
#define PRI 0
#define XAUX 0
#define CAUX 0
#define HDELAY 50
#define HSP 20
#define QAUX 2
#define LAUX 0
#define SENT_R5 5
#define TAU1 890
#define TAU 886
#define QT 600
#define QSLEEP 24
#define WOT 1100
#define WOS 40
#define GOFF 0
#define WT 188
#define G1 87
#define G2 79
#define G3 160
#define WDELAY 6
#define RDELAY 8
#define FWAIT 60
#define PREG 6
#define CODE_BYTES 36480
#define W1E 1
#define ROT 1
#define EARLY 2
#define SLPT 0

static_assert(W1H - W1M >= 4 && W1H <= 14, "the wo tile takes four loads per wave");
namespace fk {
typedef unsigned short u16;
typedef unsigned long long u64;
typedef unsigned v4u __attribute__((ext_vector_type(4)));
typedef unsigned v2u __attribute__((ext_vector_type(2)));
typedef float v4f __attribute__((ext_vector_type(4)));
typedef float v2f __attribute__((ext_vector_type(2)));
typedef __bf16 bf16x2 __attribute__((ext_vector_type(2)));
typedef __bf16 bf16x8 __attribute__((ext_vector_type(8)));
typedef short s16x4 __attribute__((ext_vector_type(4)));
#define LAS __attribute__((address_space(3)))
#define GAS __attribute__((address_space(1)))

constexpr int D = 448, F = 1792, SK = 4096, DH = 256, NQ = 4096;
constexpr int AUX_NT = 2, AUX_SC1 = 16;
constexpr int NEXTRA = 3;
constexpr int LEAD = 2;
constexpr int PN0 = 16;
constexpr int RT = WT + G1, MT = RT + G2, FT = MT + G3;
constexpr int RSRC3 = 0x00020000;
constexpr float SC2 = 0.0625f * 1.4426950408889634f;
struct Spin {
  unsigned n = 0, t0 = 0;
  __device__ __forceinline__ bool expired() {
    if ((++n & 4095u) != 0u) return false;
    const unsigned t = (unsigned)__builtin_amdgcn_s_memrealtime();
    if (n == 4096u) t0 = t;
    return t - t0 > (1u << 27);
  }
};

constexpr int WS_EPOCH = 0;
constexpr int WS_T = 2048;
constexpr int WS_G = 4096;
constexpr int WS_ML = WS_G + 8 * 8 * 4 * 128 * 16;
constexpr int WS_H = WS_ML + 64 * 8 * 16;
constexpr int WS_P0 = WS_H + 16384;
constexpr int WS_PW = WS_P0 + 64 * 4 * 2048;
constexpr int WS_PF = WS_PW + 32 * 28 * 512;
constexpr int WS_END = WS_PF + 28 * 28 * 512;
constexpr int WS_SPAN = 2 << 20;
static_assert(WS_END <= WS_SPAN, "span");
constexpr int WS_DBG = 128 << 20;

constexpr int LN_STR = 912;
constexpr int L_P = 0;
constexpr int L_MRG = 8192;
constexpr int L_Q = 16384;
constexpr int L_FLAG = 17408;
constexpr int L_ML = L_FLAG + 64;
constexpr int L_LN = L_ML + 64;
constexpr int L_BF = L_LN + 8 * LN_STR;
constexpr int L_SINK = L_BF + 4096;
constexpr int L_RAW = L_SINK + 2048;
constexpr int L_RAW2 = L_RAW + 57344;
constexpr int W2P = 928, WOP = 160;
constexpr int L_END = L_RAW2 + 64 * W2P;
static_assert(L_END + 2048 <= 160 * 1024 && L_END > 80 * 1024, "lds");
static_assert(L_LN % 16 == 0 && L_BF % 16 == 0 && L_RAW % 16 == 0, "align");
enum { FL_LN = 0, FL_QP = 1, FL_Q = 2, FL_PRE = 3, FL_DONE = 4, FL_RAW = 5, FL_RAW2 = 6, FL_RAW3 = 7, FL_ML = 9, FL_TAGL = 10, FL_TAGH = 11, FL_TG = 12, FL_ORG = 13, FL_TGOK = 14, FL_PUB = 15 };

__device__ __forceinline__ float bflo(unsigned v) { return __builtin_bit_cast(float, v << 16); }
__device__ __forceinline__ float bfhi(unsigned v) { return __builtin_bit_cast(float, v & 0xffff0000u); }
__device__ __forceinline__ float rbf(float f) { return (float)(__bf16)f; }
__device__ __forceinline__ unsigned pk2(float lo, float hi) {
  bf16x2 v = {(__bf16)lo, (__bf16)hi};
  return __builtin_bit_cast(unsigned, v);
}
__device__ __forceinline__ unsigned pk2o(float lo, float hi) {
  unsigned p = pk2(lo, hi);
  asm("" : "+v"(p));
  return p;
}
__device__ __forceinline__ float dot2(unsigned a, unsigned b, float c) {
  return __builtin_amdgcn_fdot2_f32_bf16(__builtin_bit_cast(bf16x2, a), __builtin_bit_cast(bf16x2, b), c, false);
}
__device__ __forceinline__ v4f mfma(v4u a, v4u b, v4f c) {
  return __builtin_amdgcn_mfma_f32_16x16x32_bf16(__builtin_bit_cast(bf16x8, a), __builtin_bit_cast(bf16x8, b), c, 0, 0, 0);
}
template <int CTRL> __device__ __forceinline__ float dpp(float x) {
  return __builtin_bit_cast(float, __builtin_amdgcn_mov_dpp(__builtin_bit_cast(int, x), CTRL, 0xf, 0xf, true));
}
constexpr int XOR1 = 0xB1, XOR2 = 0x4E, HMIR = 0x141, ROR4 = 0x124, ROR8 = 0x128;
__device__ __forceinline__ float sw16_add(float a, float b) {
  auto s = __builtin_amdgcn_permlane16_swap(__float_as_uint(a), __float_as_uint(b), false, false);
  return __uint_as_float(s[0]) + __uint_as_float(s[1]);
}
__device__ __forceinline__ float sw32_add(float a, float b) {
  auto s = __builtin_amdgcn_permlane32_swap(__float_as_uint(a), __float_as_uint(b), false, false);
  return __uint_as_float(s[0]) + __uint_as_float(s[1]);
}
__device__ __forceinline__ float sw16_max(float a) {
  auto s = __builtin_amdgcn_permlane16_swap(__float_as_uint(a), __float_as_uint(a), false, false);
  return fmaxf(__uint_as_float(s[0]), __uint_as_float(s[1]));
}
__device__ __forceinline__ float sw32_max(float a) {
  auto s = __builtin_amdgcn_permlane32_swap(__float_as_uint(a), __float_as_uint(a), false, false);
  return fmaxf(__uint_as_float(s[0]), __uint_as_float(s[1]));
}
__device__ __forceinline__ v4u pack24(v4f v, unsigned tag) {
  const unsigned a = __float_as_uint(v.x), b = __float_as_uint(v.y), c = __float_as_uint(v.z), d = __float_as_uint(v.w);
  return v4u{__builtin_amdgcn_perm(b, a, 0x05030201u), __builtin_amdgcn_perm(c, b, 0x06050302u), __builtin_amdgcn_perm(d, c, 0x07060503u), tag};
}
__device__ __forceinline__ v4f unpack24(v4u g) {
  return v4f{__uint_as_float(g.x << 8), __uint_as_float(__builtin_amdgcn_perm(g.y, g.x, 0x0504030Cu)),
             __uint_as_float(__builtin_amdgcn_perm(g.z, g.y, 0x0403020Cu)), __uint_as_float(g.z & 0xFFFFFF00u)};
}
__device__ __forceinline__ v4u pack28(v4f v, unsigned tag) {
  return v4u{(__float_as_uint(v.x) & ~3u) | (tag & 3u), (__float_as_uint(v.y) & ~3u) | ((tag >> 2) & 3u),
             (__float_as_uint(v.z) & ~3u) | ((tag >> 4) & 3u), (__float_as_uint(v.w) & ~3u) | ((tag >> 6) & 3u)};
}
__device__ __forceinline__ v4f unpack28(v4u g) { return __builtin_bit_cast(v4f, g & ~3u); }
__device__ __forceinline__ bool bad28(v4u g, unsigned tag) {
  return (((g.x ^ tag) | (g.y ^ (tag >> 2)) | (g.z ^ (tag >> 4)) | (g.w ^ (tag >> 6))) & 3u) != 0u;
}
#define LGKM0() asm volatile("s_waitcnt lgkmcnt(0)" ::: "memory")
#define CLOB() asm volatile("" ::: "memory")
#define SB() __builtin_amdgcn_sched_barrier(0)
#define BAR() do { asm volatile("s_waitcnt lgkmcnt(0)" ::: "memory"); __builtin_amdgcn_s_barrier(); asm volatile("" ::: "memory"); __builtin_amdgcn_sched_barrier(0); } while (0)
#define LD128(rs, vo, so, aux) __builtin_amdgcn_raw_buffer_load_b128(rs, vo, so, aux)
#define ST128(v, rs, vo, so, aux) __builtin_amdgcn_raw_buffer_store_b128(v, rs, vo, so, aux)

#define STAMP(i) do {} while (0)

__device__ __forceinline__ void lds_wait_ge(volatile LAS unsigned* f, unsigned want, int slp) {
  Spin sp;
#pragma nounroll
  while (*f < want) {
    if (slp) __builtin_amdgcn_s_sleep(8);
    if (sp.expired()) break;
  }
  CLOB();
}
__device__ __forceinline__ void lds_signal(LAS unsigned char* L, int word, int lane, unsigned inc) {
  LGKM0();
  LAS unsigned* p = (LAS unsigned*)(L + (lane == 0 ? L_FLAG + word * 4 : L_SINK + lane * 4));
  __hip_atomic_fetch_add(p, inc, __ATOMIC_RELAXED, __HIP_MEMORY_SCOPE_WORKGROUP);
}
template <int N, int STR, int WIDE, int SLP>
__device__ __forceinline__ void gather(v4u (&v)[N], __amdgpu_buffer_rsrc_t rs, int off, unsigned tl, unsigned th, u64 last_mask) {
  Spin sp;
#pragma nounroll
  for (;;) {
    CLOB();
#pragma unroll
    for (int i = 0; i < N; ++i) v[i] = LD128(rs, off + i * STR, 0, AUX_SC1);
    u64 any = 0ull;
#pragma unroll
    for (int i = 0; i < N; ++i) {
      const u64 bad = __builtin_amdgcn_ballot_w64(WIDE == 2 ? bad28(v[i], tl) : WIDE ? ((v[i].z ^ tl) | (v[i].w ^ th)) != 0u : v[i].w != tl);
      any |= i == N - 1 ? bad & last_mask : bad;
    }
    if (any == 0ull || sp.expired()) break;
    if (SLP) __builtin_amdgcn_s_sleep(SLP);
  }
}
__device__ __forceinline__ void wait_until(unsigned until) {
  asm volatile("8:\n\ts_memrealtime vcc\n\ts_waitcnt lgkmcnt(0)\n\ts_sub_i32 vcc_lo, vcc_lo, %0\n\ts_cmp_lt_i32 vcc_lo, 0\n\ts_cbranch_scc1 8b\n" :: "s"(until) : "scc", "vcc", "memory");
}
__device__ __forceinline__ void doze_until(unsigned until) {
#pragma nounroll
  while ((int)((unsigned)__builtin_amdgcn_s_memrealtime() - until) < 0) __builtin_amdgcn_s_sleep(4);
}
constexpr int PN1 = 34;
constexpr int SCHED63 = (PN0 * TAU0 + (PN1 - PN0) * TAU1 + (63 - PN1) * TAU) >> 4;
__device__ __forceinline__ void pace(unsigned tb, int n) { wait_until(tb + (unsigned)((n < PN0 ? n * TAU0 : PN0 * TAU0 + (n - PN0) * TAU1) >> 4)); }
__device__ __forceinline__ void pace1(unsigned tb, int n) { wait_until(tb + (unsigned)((PN0 * TAU0 + (n - PN0) * TAU1) >> 4)); }
__device__ __forceinline__ void pace2(unsigned tb, int n) { wait_until(tb + (unsigned)((PN0 * TAU0 + (PN1 - PN0) * TAU1 + (n - PN1) * TAU) >> 4)); }
template <bool B> struct BoolC { static constexpr bool value = B; };
__device__ __forceinline__ void issue_turn(v4u* r, __amdgpu_buffer_rsrc_t rs, int voff, int soff) {
#pragma unroll
  for (int i = 0; i < 4; ++i) r[i] = LD128(rs, voff + i * 1024, soff, AUX_NT);
  SB();
}
__device__ __forceinline__ float k_turn(const v4u* k, v4u q0, v4u q1, bool b0, bool b1, bool b2) {
  float x[8];
#pragma unroll
  for (int i = 0; i < 4; ++i) {
    float a0 = dot2(k[i].x, q0.x, 0.f), a1 = dot2(k[i].x, q1.x, 0.f);
    a0 = dot2(k[i].y, q0.y, a0); a1 = dot2(k[i].y, q1.y, a1);
    a0 = dot2(k[i].z, q0.z, a0); a1 = dot2(k[i].z, q1.z, a1);
    a0 = dot2(k[i].w, q0.w, a0); a1 = dot2(k[i].w, q1.w, a1);
    x[2 * i] = a0; x[2 * i + 1] = a1;
  }
  float y[4], z[2];
#pragma unroll
  for (int v = 0; v < 4; ++v) y[v] = (b2 ? x[v + 4] : x[v]) + dpp<HMIR>(b2 ? x[v] : x[v + 4]);
#pragma unroll
  for (int v = 0; v < 2; ++v) z[v] = (b1 ? y[v + 2] : y[v]) + dpp<XOR2>(b1 ? y[v] : y[v + 2]);
  return (b0 ? z[1] : z[0]) + dpp<XOR1>(b0 ? z[0] : z[1]);
}
__device__ __forceinline__ float k_group(float r0, float r1, float r2, float r3, bool b3) {
  const float u0 = (b3 ? r1 : r0) + dpp<ROR8>(b3 ? r0 : r1);
  const float u1 = (b3 ? r3 : r2) + dpp<ROR8>(b3 ? r2 : r3);
  return rbf(sw16_add(u0, u1));
}
__device__ __forceinline__ void v_turn(const v4u* v, v4f pa, v4f pb, v2f (&a0)[4], v2f (&a1)[4]) {
#pragma unroll
  for (int i = 0; i < 4; ++i) {
    const float p0 = i == 0 ? pa.x : i == 1 ? pa.z : i == 2 ? pb.x : pb.z;
    const float p1 = i == 0 ? pa.y : i == 1 ? pa.w : i == 2 ? pb.y : pb.w;
#pragma unroll
    for (int j = 0; j < 4; ++j) {
      const v2f e = {bflo(v[i][j]), bfhi(v[i][j])};
      a0[j] = __builtin_elementwise_fma(e, v2f{p0, p0}, a0[j]);
      a1[j] = __builtin_elementwise_fma(e, v2f{p1, p1}, a1[j]);
    }
  }
}
template <bool FULL> __device__ __forceinline__ float row_sum(float v) {
  v += dpp<XOR1>(v); v += dpp<XOR2>(v); v += dpp<HMIR>(v); v += dpp<ROR8>(v);
  v = sw16_add(v, v);
  return FULL ? sw32_add(v, v) : v;
}
template <int NW> __device__ __forceinline__ void ln_row(unsigned (&x)[NW], bool act) {
  v2f v[NW];
#pragma unroll
  for (int k = 0; k < NW; ++k) v[k] = v2f{bflo(x[k]), bfhi(x[k])};
  float mean, var;
  if constexpr (NW == 4) {
    const float c = bflo(__builtin_amdgcn_readfirstlane(x[0]));
    v2f s1 = {0.f, 0.f}, s2 = {0.f, 0.f};
#pragma unroll
    for (int k = 0; k < NW; ++k) { const v2f d = v[k] - c; s1 += d; s2 = __builtin_elementwise_fma(d, d, s2); }
    float t = sw32_add(act ? s1.x + s1.y : 0.f, act ? s2.x + s2.y : 0.f);
    t = row_sum<false>(t) * (1.0f / D);
    const float m1 = __builtin_bit_cast(float, __builtin_amdgcn_readlane(__builtin_bit_cast(int, t), 0));
    const float m2 = __builtin_bit_cast(float, __builtin_amdgcn_readlane(__builtin_bit_cast(int, t), 32));
    mean = c + m1;
    var = fmaxf(fmaf(-m1, m1, m2), m2 - m2);
  } else {
    constexpr unsigned ONES = 0x3F803F80u;
    float sa = 0.f, sb = 0.f;
#pragma unroll
    for (int k = 0; k < NW; k += 2) { sa = dot2(x[k], ONES, sa); sb = dot2(x[k + 1], ONES, sb); }
    mean = row_sum<false>(sa + sb) * (1.0f / D);
    v2f q0 = {0.f, 0.f}, q1 = {0.f, 0.f};
#pragma unroll
    for (int k = 0; k < NW; k += 2) {
      const v2f d0 = v[k] - mean, d1 = v[k + 1] - mean;
      q0 = __builtin_elementwise_fma(d0, d0, q0); q1 = __builtin_elementwise_fma(d1, d1, q1);
    }
    q0 += q1;
    var = row_sum<false>(act ? q0.x + q0.y : 0.f) * (1.0f / D);
  }
  const float mb = rbf(mean);
#pragma unroll
  for (int k = 0; k < NW; ++k) { const v2f d = v[k] - mb; const unsigned p = pk2o(d.x, d.y); v[k] = v2f{bflo(p), bfhi(p)}; }
  const float den = rbf(__builtin_amdgcn_sqrtf(rbf(rbf(var) + 1e-5f)));
  const float ri = __builtin_amdgcn_rcpf(den);
#pragma unroll
  for (int k = 0; k < NW; ++k) { const v2f a = v[k] * ri; x[k] = pk2(a.x, a.y); }
}
__device__ __forceinline__ v4u frag_tr(const LAS unsigned char* p, int hi) {
#if defined(__HIP_DEVICE_COMPILE__)
  const v2u a = __builtin_bit_cast(v2u, __builtin_amdgcn_ds_read_tr16_b64_v4i16((LAS s16x4*)p));
  const v2u b = __builtin_bit_cast(v2u, __builtin_amdgcn_ds_read_tr16_b64_v4i16((LAS s16x4*)(p + hi)));
  return v4u{a.x, a.y, b.x, b.y};
#else
  return v4u{0u, 0u, 0u, 0u};
#endif
}
__device__ __forceinline__ v4f red16(v4f t) {
#pragma unroll
  for (int k = 0; k < 4; ++k) { float a = t[k]; a += dpp<ROR8>(a); a += dpp<ROR4>(a); a = sw16_add(a, a); a = sw32_add(a, a); t[k] = a; }
  return t;
}
__device__ __forceinline__ void mlp_ln(LAS unsigned char* L, __amdgpu_buffer_rsrc_t rt, int row, int lane, unsigned tl, unsigned t0) {
  const bool act = lane < 56;
  const int a = act ? L_LN + row * LN_STR + lane * 8 : L_SINK + lane * 8;
  v4u hg[2];
  int o0 = WS_H + (row * 112 + (act ? lane : 55)) * 16;
  asm volatile("" : "+v"(o0));
  wait_until(t0 + SCHED63 + MT);
  gather<2, 896, 0, SLPT>(hg, rt, o0, tl, 0u, ~0ull);
  { const int wv = row; (void)wv; STAMP(17); }
  unsigned x[4] = {hg[0].x, hg[0].y, hg[1].x, hg[1].y};
  ln_row<4>(x, act);
  *(LAS v2u*)(L + a) = v2u{x[0], x[1]}; *(LAS v2u*)(L + a + 448) = v2u{x[2], x[3]};
  BAR();
}

__global__ void __launch_bounds__(512, 2)
fused_decode(const u16* __restrict__ x, const u16* __restrict__ kc, const u16* __restrict__ vc, const u16* __restrict__ wq,
             const u16* __restrict__ wo, const u16* __restrict__ w1, const u16* __restrict__ b1, const u16* __restrict__ w2,
             const u16* __restrict__ b2, u16* __restrict__ out, unsigned char* __restrict__ ws) {
  extern __shared__ __attribute__((aligned(16))) unsigned char smem[];
  LAS unsigned char* L = (LAS unsigned char*)smem;
  const u64 pc0 = __builtin_amdgcn_s_getpc() & ~0xFFull;
  const int w = blockIdx.x, tid = threadIdx.x, lane = tid & 63, wv = __builtin_amdgcn_readfirstlane(tid >> 6);
  const int kvh = w & 7, r5 = w >> 3, b = r5 & 7, s = r5 >> 3, grp = b * 8 + kvh;
  const int m = (r5 - 28) * 8 + kvh;
  const bool isM = r5 >= 28 && m < 28;
  volatile LAS unsigned* FL = (volatile LAS unsigned*)(L + L_FLAG);
  const __amdgpu_buffer_rsrc_t rws = __builtin_amdgcn_make_buffer_rsrc((void*)ws, 0, WS_END, RSRC3);
  auto code_prefetch = [&]() {
    const __amdgpu_buffer_rsrc_t rc = __builtin_amdgcn_make_buffer_rsrc((void*)pc0, 0, CODE_BYTES, RSRC3);
    constexpr int CPL = (CODE_BYTES / 128 + 31) / 32;
    return __builtin_amdgcn_raw_buffer_load_b32(rc, lane < CPL ? (lane * 32 + r5) * 128 : CODE_BYTES, 0, CAUX);
  };
  unsigned cpf = 0;
  if (wv == 4) cpf = code_prefetch();
  SB();
  asm volatile("" :: "s"(x), "s"(kc), "s"(vc), "s"(wq), "s"(wo), "s"(w1), "s"(b1), "s"(w2), "s"(b2), "s"(out), "s"(ws));

  if (__builtin_expect(wv >= 4, 0)) {
    const int hw = wv - 4;
    STAMP(0);
    __builtin_amdgcn_s_setprio(3);
    const unsigned tstart = (unsigned)__builtin_amdgcn_s_memrealtime();
    const bool isW = r5 < 28;
    const int t = r5 >> 2, kq = r5 & 3;
    const int g = lane >> 4;
    const LAS unsigned char* ap = L + L_RAW + (g * 8 + ((lane & 15) >> 2)) * 128 + hw * 32 + (lane & 3) * 8;
    const LAS unsigned char* bp = L + L_LN + (lane & 7) * LN_STR + g * 16;
    const __amdgpu_buffer_rsrc_t rx = __builtin_amdgcn_make_buffer_rsrc((void*)x, 0, 8 * D * 2, RSRC3);
    if (HDELAY) { if (tid < 256 + 16) FL[tid - 256] = 0u; BAR(); doze_until(tstart + HDELAY + (HSP ? (r5 & 7) * HSP : 0)); }
    const v2u ep = __builtin_amdgcn_raw_buffer_load_b64(rws, WS_EPOCH, 0, AUX_SC1);
    unsigned tagl, tagh;
#define MAKE_TAGS() do { const unsigned el = __builtin_amdgcn_readfirstlane(ep.x), eh = __builtin_amdgcn_readfirstlane(ep.y); \
      u64 z = (((u64)eh << 32) | el) + 0x9E3779B97F4A7C15ull; \
      z = (z ^ (z >> 30)) * 0xBF58476D1CE4E5B9ull; z = (z ^ (z >> 27)) * 0x94D049BB133111EBull; z ^= z >> 31; \
      tagl = (((unsigned)z ^ el) & 3u) == 0u ? (unsigned)z ^ 1u : (unsigned)z; \
      tagl = ((tagl ^ (tagl >> 2)) & 3u) == 0u ? tagl ^ 4u : tagl;     \
      tagh = ((unsigned)(z >> 32) & ~31u) | ((eh + 1u) & 31u); } while (0)
    if (!HDELAY && tid < 256 + 16) FL[tid - 256] = 0u;
    {
      const int row = hw * 2 + (lane >> 5), c = lane & 31;
      const int ks0 = s < 3 ? s * 4 : 11, nks = s < 2 ? 4 : 3;
      const int xo = c < 28 ? row * (D * 2) + c * 32 : 8 * D * 2;
      const v4u xa = LD128(rx, xo, 0, XAUX), xb = LD128(rx, xo + 16, 0, XAUX);
      const __amdgpu_buffer_rsrc_t rq = __builtin_amdgcn_make_buffer_rsrc((void*)(wq + ks0 * 32 * NQ + (kvh * 8 + b) * 64), 0, (nks * 32 - 1) * (NQ * 2) + 128, RSRC3);
      v4u tt[4];
#pragma unroll
      for (int i = 0; i < 4; ++i) tt[i] = LD128(rq, (lane >> 3) * (NQ * 2) + (lane & 7) * 16, (i * 4 + hw) * 8 * (NQ * 2), QAUX);
      SB();
      if (!HDELAY) BAR();
      {
        unsigned xr[8] = {xa.x, xa.y, xa.z, xa.w, xb.x, xb.y, xb.z, xb.w};
        ln_row<8>(xr, c < 28);
        const int a = c < 28 ? L_LN + row * LN_STR + c * 32 : L_SINK + lane * 32;
        *(LAS v4u*)(L + a) = v4u{xr[0], xr[1], xr[2], xr[3]}; *(LAS v4u*)(L + a + 16) = v4u{xr[4], xr[5], xr[6], xr[7]};
      }
      MAKE_TAGS();
      STAMP(1);
#pragma unroll
      for (int i = 0; i < 4; ++i) *(LAS v4u*)(L + L_RAW + (i * 4 + hw) * 1024 + lane * 16) = tt[i];
      lds_signal(L, FL_LN, lane, 1u);
      lds_wait_ge(FL + FL_LN, 4u, 0);
      v4f d0 = {0.f, 0.f, 0.f, 0.f};
#pragma unroll
      for (int ks = 0; ks < 3; ++ks) d0 = mfma(frag_tr(ap + ks * 4096, 512), *(const LAS v4u*)(bp + (ks0 + ks) * 64), d0);
      if (s < 2) d0 = mfma(frag_tr(ap + 3 * 4096, 512), *(const LAS v4u*)(bp + (ks0 + 3) * 64), d0);
      ST128(pack28(d0, tagl), rws, (lane & 8) == 0 ? WS_G + (((kvh * 8 + (lane & 7)) * 4 + s) * 128 + b * 16 + hw * 4 + g) * 16 : WS_END, 0, AUX_SC1);
      lds_signal(L, FL_QP, lane, 1u);
      STAMP(2);
    }
    auto q_gather = [&]() {
      const int off = WS_G + (kvh * 8 + b) * 8192 + ((hw - 2) * 64 + lane) * 16;
      doze_until(tstart + QT);
      v4u gq[4];
      gather<4, 2048, 2, QSLEEP>(gq, rws, off, tagl, 0u, ~0ull);
      const v4f qv = (unpack28(gq[0]) + unpack28(gq[1])) + (unpack28(gq[2]) + unpack28(gq[3]));
      *(LAS v2u*)(L + L_Q + ((hw - 2) * 64 + lane) * 8) = v2u{pk2(qv.x, qv.y), pk2(qv.z, qv.w)};
      FL[FL_TAGL] = tagl; FL[FL_TAGH] = tagh;
      lds_signal(L, FL_Q, lane, 2u);
      STAMP(3);
    };
    if (hw >= 2) q_gather();
    if (hw == 0 && r5 == SENT_R5) {
      lds_wait_ge(FL + FL_ORG, 1u, 1);
      const v2u og = *(volatile LAS v2u*)(L + L_ML);
      ST128((v4u{og.x, og.y, tagl, tagh}), rws, lane == 0 ? WS_T + kvh * 16 : WS_END, 0, AUX_SC1);
    }
    if (hw == 1) {
      doze_until(tstart + TGT);
      v4u gt[1];
      gather<1, 0, 1, 32>(gt, rws, WS_T + (lane & 7) * 16, tagl, tagh, ~0ull);
      const unsigned t0 = __builtin_amdgcn_readfirstlane(gt[0].x);
      int o = (int)(gt[0].x - t0) * 8 + (int)gt[0].y * OGAIN;
      o += __builtin_amdgcn_mov_dpp(o, XOR1, 0xf, 0xf, true); o += __builtin_amdgcn_mov_dpp(o, XOR2, 0xf, 0xf, true); o += __builtin_amdgcn_mov_dpp(o, HMIR, 0xf, 0xf, true);
      o >>= 6;
      o = o < -1000 ? -1000 : o > 2000 ? 2000 : o;
      const unsigned tgv = t0 + (unsigned)o + (unsigned)(GOFF);
      FL[FL_TG] = tgv - tstart + 4000u < 8000u ? tgv : tstart;
      lds_signal(L, FL_TGOK, lane, 1u);
      if (VPRE == 1) {
        const unsigned z0 = *(const GAS unsigned*)(vc + ((size_t)grp * SK + (lane & 1) * (SK - 1)) * DH);
        asm volatile("" :: "v"(z0));
      }
    }
    if (VPRE == 2 && hw == 0) {
      doze_until(tstart + VPT);
#define CAS __attribute__((address_space(4)))
      const unsigned z0 = *(const CAS unsigned*)(vc + (size_t)grp * SK * DH), z1 = *(const CAS unsigned*)(vc + ((size_t)grp * SK + SK - 1) * DH);
      const unsigned z2 = *(const CAS unsigned*)(isM ? w2 + m * 64 * D : wo + (kvh * 512 + kq * 128) * D), z3 = *(const CAS unsigned*)(isM ? w2 + (m * 64 + 63) * D : wo + (kvh * 512 + kq * 128 + 127) * D);
      const unsigned z4 = *(const CAS unsigned*)(ws + (size_t)((tagh & 31u) + 1u) * WS_SPAN), z5 = *(const CAS unsigned*)(ws + (size_t)((tagh & 31u) + 2u) * WS_SPAN - 64);
      const unsigned z6 = *(const CAS unsigned*)(w1 + (r5 & 1) * (D - 1) * F);
      asm volatile("" :: "s"(z0), "s"(z1), "s"(z2), "s"(z3), "s"(z4), "s"(z5), "s"(z6));
      if (VOUT == 1 || (VOUT == 2 && r5 == 1)) { const unsigned z7 = *(const CAS unsigned*)(out + (VOUT == 2 ? kvh * D : 0)), z8 = *(const CAS unsigned*)(out + 8 * D - 2); asm volatile("" :: "s"(z7), "s"(z8)); }
      if (VDENSE) {
#pragma unroll 1
        for (int o = r5 * VDENSE; o < D * F * 2; o += 32 * VDENSE) {
          const unsigned y0 = *(const CAS unsigned*)((const unsigned char*)w1 + o), y1 = *(const CAS unsigned*)((const unsigned char*)w2 + o);
          const unsigned y2 = *(const CAS unsigned*)((const unsigned char*)wo + kvh * (512 * D * 2) + (o < 512 * D * 2 ? o : 0));
          asm volatile("" :: "s"(y0), "s"(y1), "s"(y2));
        }
      }
    }
    lds_wait_ge(FL + FL_Q, 4u, 1);
    lds_wait_ge(FL + FL_QP, 4u, 1);
    v4f cx, cb;
    {
      const v2u bv = *((const GAS v2u*)(isW ? b2 + r5 * 16 + (lane & 3) * 4 : b1 + (isM ? m : 0) * 64 + hw * 16 + g * 4));
      const v2u xv = __builtin_amdgcn_raw_buffer_load_b64(rx, (kvh * D + r5 * 16 + (lane & 3) * 4) * 2, 0, 0);
      cb = v4f{bflo(bv.x), bfhi(bv.x), bflo(bv.y), bfhi(bv.y)};
      cx = v4f{bflo(xv.x), bfhi(xv.x), bflo(xv.y), bfhi(xv.y)};
    }
    const __amdgpu_buffer_rsrc_t rw = __builtin_amdgcn_make_buffer_rsrc(
        isW ? (void*)(wo + (kvh * 512 + kq * 128) * D + t * 64) : (void*)(w1 + (isM ? m : 0) * 64), 0, isW ? 127 * (D * 2) + 128 : isM ? 447 * (F * 2) + 128 : 0, RSRC3);
    const int pitch = isW ? D * 2 : F * 2;
    int wvo = (lane >> 3) * pitch + (lane & 7) * 16;
    asm volatile("" : "+v"(wvo));
    if (W1M && isM) {
      doze_until(tstart + WOT);
      v4u t1[W1M + 1];
#pragma unroll
      for (int i = 0; i < W1M; ++i) t1[i] = LD128(rw, wvo, (i * 4 + hw) * 8 * (F * 2), LAUX);
#pragma unroll
      for (int i = 0; i < W1M; ++i) *(LAS v4u*)(L + L_RAW + (i * 4 + hw) * 1024 + lane * 16) = t1[i];
    }
    asm volatile("" :: "v"(cpf));
    STAMP(4);
    const __amdgpu_buffer_rsrc_t rt = __builtin_amdgcn_make_buffer_rsrc((void*)(ws + (size_t)(ROT ? (tagh & 31u) + 1u : 0u) * WS_SPAN), 0, WS_END, RSRC3);
    float fw[4] = {0.f, 0.f, 0.f, 0.f};
    if (hw < 2) {
      lds_wait_ge(FL + FL_ML, 4u, 1);
      const v2f ml = *(const LAS v2f*)(L + L_ML + ((lane & 3) * 2 + hw) * 8);
      float Ms = ml.x;
      Ms = fmaxf(Ms, dpp<XOR1>(Ms)); Ms = fmaxf(Ms, dpp<XOR2>(Ms));
      const float Msub = Ms == -INFINITY ? 0.f : Ms;
      float Lw = __builtin_amdgcn_exp2f((ml.x - Msub) * SC2) * ml.y;
      Lw += dpp<XOR1>(Lw); Lw += dpp<XOR2>(Lw);
      ST128((v4u{__float_as_uint(Ms), __float_as_uint(Lw), tagl, tagh}), rt, lane == 0 ? WS_ML + ((grp * 2 + hw) * 4 + s) * 16 : WS_END, 0, AUX_SC1);
      const int off = WS_ML + ((grp * 2 + hw) * 4 + (lane & 3)) * 16;
      v4u gm1[1];
      __builtin_amdgcn_s_sleep(96); __builtin_amdgcn_s_sleep(96);
      gather<1, 0, 1, 64>(gm1, rt, off, tagl, tagh, ~0ull);
      const v4u gm = gm1[0];
      float M = __uint_as_float(gm.x);
      M = fmaxf(M, dpp<XOR1>(M)); M = fmaxf(M, dpp<XOR2>(M));
      float Ls = __builtin_amdgcn_exp2f((__uint_as_float(gm.x) - M) * SC2) * __uint_as_float(gm.y);
      Ls += dpp<XOR1>(Ls); Ls += dpp<XOR2>(Ls);
      const float f = __builtin_amdgcn_exp2f((ml.x - M) * SC2) / Ls;
#pragma unroll
      for (int i = 0; i < 4; ++i) fw[i] = __builtin_bit_cast(float, __builtin_amdgcn_readlane(__builtin_bit_cast(int, f), i));
    }
    STAMP(5);
    lds_wait_ge(FL + FL_TGOK, 1u, 1);
    const unsigned tg = __builtin_amdgcn_readfirstlane(FL[FL_TG]);
    asm volatile("" : "+v"(cx), "+v"(cb));
    int p_p0 = WS_P0 + ((kvh * 8 + b) * 4 + s) * 2048 + (hw & 1) * 1024 + lane * 16;
    int p_st = isW ? ((lane & 8) == 0 ? WS_PW + (((kvh * 4 + kq) * 28 + t * 4 + hw) * 8 + (lane & 7)) * 64 + g * 16 : WS_END)
                   : WS_PF + ((m * 28 + hw * 7) * 8 + (lane & 15)) * 64 + g * 16;
    int p_h = lane < 4 ? WS_H + (kvh * 112 + r5 * 4 + lane) * 16 : WS_END;
    int g_p0 = WS_P0 + (kvh * 8 + (lane >> 3)) * 8192 + (kq * 32 + hw * 8 + (lane & 7)) * 16;
    int g_pw = WS_PW + (((lane >> 2) * 28 + r5) * 8 + kvh) * 64 + (lane & 3) * 16;
    int g_o = lane < 4 ? (kvh * D + r5 * 16 + lane * 4) * 2 : 8 * D * 2;
    int g_e = (w == 0 && lane == 0) ? WS_EPOCH : WS_END;
    int a_bw = isW ? L_BF + hw * 512 + (lane >> 3) * 64 + (lane & 3) * 16 + ((lane >> 2) & 1) * 8 : L_BF + (lane & 15) * 144 + (hw >> 1) * 64 + g * 16 + (hw & 1) * 8;
    int a_br = isW ? L_BF + (lane & 7) * 64 + g * 16 : L_BF + (lane & 15) * 144 + g * 16;
    int a_ow = L_RAW + (hw * 8 + (lane >> 3)) * WOP + (lane & 7) * 16, a_or = L_RAW + (g * 4 + ((lane & 15) >> 2)) * WOP + hw * 32 + (lane & 3) * 8;
    asm volatile("" : "+v"(a_ow), "+v"(a_or));
    asm volatile("" : "+v"(p_p0), "+v"(p_st), "+v"(p_h), "+v"(g_p0), "+v"(g_pw), "+v"(g_o), "+v"(g_e), "+v"(a_bw), "+v"(a_br));
    const __amdgpu_buffer_rsrc_t rout = __builtin_amdgcn_make_buffer_rsrc((void*)out, 0, 8 * D * 2, RSRC3);
    lds_wait_ge(FL + FL_PRE, 4u, 1);
    STAMP(6);
    if (hw == 3) { const unsigned z = code_prefetch(); asm volatile("" :: "v"(z)); }
    v4u to[14];
    if (PRI) { __builtin_amdgcn_s_setprio(0); doze_until(tg + SCHED63 + (isW ? WOL : W1L) - 6); } else wait_until(tg + SCHED63 + (isW ? WOL : W1L));
#pragma unroll
    for (int i = W1M; i < W1H; ++i) to[i] = LD128(rw, wvo, ((isW ? i - W1M : i) * 4 + hw) * 8 * pitch, LAUX);
    SB();
    if (PRI >= 2 && hw >= 2) doze_until(tg + SCHED63 + WT - 40);
    lds_wait_ge(FL + FL_DONE, 4u, 0);
    if (hw < 2) {
      STAMP(7);
      const LAS v4f* mp = (const LAS v4f*)(L + L_MRG + hw * 1024 + lane * 16);
      const v4f o = (mp[0] * fw[0] + mp[128] * fw[1]) + (mp[256] * fw[2] + mp[384] * fw[3]);
      ST128(pack24(o, tagl), rt, p_p0, 0, AUX_SC1);
      if (isM) lds_signal(L, FL_PUB, lane, 1u);
      STAMP(8);
    }
    if (isW) {
      __builtin_amdgcn_s_sleep(WDELAY);
      wait_until(tg + SCHED63 + WT);
      v4u pv[4];
      {
        CLOB();
#pragma unroll
        for (int i = 0; i < 4; ++i) pv[i] = LD128(rt, g_p0 + i * 2048, 0, AUX_SC1);
        SB();
#pragma unroll
        for (int i = 0; i < 4; ++i) *(LAS v4u*)(L + a_ow + i * 32 * WOP) = to[W1M + i];
        SB();
        const u64 bad = __builtin_amdgcn_ballot_w64((((pv[0].w ^ tagl) | (pv[1].w ^ tagl)) | ((pv[2].w ^ tagl) | (pv[3].w ^ tagl))) != 0u);
        if (__builtin_expect(bad != 0ull, 0)) gather<4, 2048, 0, SLPT>(pv, rt, g_p0, tagl, 0u, ~0ull);
      }
      STAMP(9);
      const v4f o = (unpack24(pv[0]) + unpack24(pv[1])) + (unpack24(pv[2]) + unpack24(pv[3]));
      *(LAS v2u*)(L + a_bw) = v2u{pk2(o.x, o.y), pk2(o.z, o.w)};
      BAR();
      v4u bf[4], Fr[4];
#pragma unroll
      for (int ks = 0; ks < 4; ++ks) { Fr[ks] = frag_tr(L + a_or + ks * 32 * WOP, 16 * WOP); bf[ks] = *(const LAS v4u*)(L + a_br + ks * 512); }
      SB();
      v4f d0 = {0.f, 0.f, 0.f, 0.f}, d1 = {0.f, 0.f, 0.f, 0.f};
      d0 = mfma(Fr[0], bf[0], d0); d1 = mfma(Fr[1], bf[1], d1); d0 = mfma(Fr[2], bf[2], d0); d1 = mfma(Fr[3], bf[3], d1);
      ST128(pack24(d0 + d1, tagl), rt, p_st, 0, AUX_SC1);
      STAMP(10);
      if (hw == 0) {
        v4u u[2];
        __builtin_amdgcn_s_sleep(RDELAY);
        wait_until(tg + SCHED63 + RT);
        gather<2, 16 * 28 * 512, 0, SLPT>(u, rt, g_pw, tagl, 0u, ~0ull);
        STAMP(11);
        const v4f hv = red16(unpack24(u[0]) + unpack24(u[1]));
        const unsigned h01 = pk2o(cx.x + rbf(hv.x), cx.y + rbf(hv.y)), h23 = pk2o(cx.z + rbf(hv.z), cx.w + rbf(hv.w));
        ST128((v4u{h01, h23, tagh, tagl}), rt, p_h, 0, AUX_SC1);
        STAMP(12);
        __builtin_amdgcn_s_sleep(FWAIT);
        wait_until(tg + SCHED63 + FT);
        gather<2, 16 * 28 * 512, 0, SLPT>(u, rt, g_pw + (WS_PF - WS_PW), tagl, 0u, (1ull << 48) - 1ull);
        STAMP(13);
        const v4f ov = red16(unpack24(u[0]) + unpack24(u[1]));
        const unsigned o01 = pk2(bflo(h01) + rbf(rbf(ov.x) + cb.x), bfhi(h01) + rbf(rbf(ov.y) + cb.y));
        const unsigned o23 = pk2(bflo(h23) + rbf(rbf(ov.z) + cb.z), bfhi(h23) + rbf(rbf(ov.w) + cb.w));
        __builtin_amdgcn_raw_buffer_store_b64(v2u{o01, o23}, rout, g_o, 0, OAUX);
        __builtin_amdgcn_raw_buffer_store_b64(v2u{tagl, tagh}, rws, g_e, 0, AUX_SC1);
        STAMP(14);
      }
    } else if (isM) {
      v4u Fr[14];
      lds_wait_ge(FL + FL_PUB, 2u, 0);
#pragma unroll
      for (int i = W1H; i < 14; ++i) to[i] = LD128(rw, wvo, (i * 4 + hw) * 8 * (F * 2), LAUX);
      SB();
#pragma unroll
      for (int i = W1M; i < 14; ++i) *(LAS v4u*)(L + L_RAW + (i * 4 + hw) * 1024 + lane * 16) = to[i];
      lds_signal(L, FL_RAW, lane, 1u);
      lds_wait_ge(FL + FL_RAW, 4u, 0);
#pragma unroll
      for (int ks = 0; ks < 14; ++ks) Fr[ks] = frag_tr(ap + ks * 4096, 512);
#pragma unroll
      for (int ks = 0; ks < 14; ++ks) asm volatile("" : "+v"(Fr[ks]));
      STAMP(19);
      mlp_ln(L, rt, wv, lane, tagl, tg);
      STAMP(15);
      v4u bf[14];
#pragma unroll
      for (int ks = 0; ks < 14; ++ks) bf[ks] = *(const LAS v4u*)(bp + ks * 64);
      SB();
      v4f d0 = {0.f, 0.f, 0.f, 0.f}, d1 = {0.f, 0.f, 0.f, 0.f};
#pragma unroll
      for (int ks = 0; ks < 14; ks += 2) { d0 = mfma(Fr[ks], bf[ks], d0); d1 = mfma(Fr[ks + 1], bf[ks + 1], d1); }
      d0 += d1;
      const unsigned g01 = pk2o(d0.x, d0.y), g23 = pk2o(d0.z, d0.w);
      const unsigned a01 = pk2o(bflo(g01) + cb.x, bfhi(g01) + cb.y), a23 = pk2o(bflo(g23) + cb.z, bfhi(g23) + cb.w);
      float u[4] = {bflo(a01), bfhi(a01), bflo(a23), bfhi(a23)};
#pragma unroll
      for (int k = 0; k < 4; ++k) { const float a = u[k] > 0.f ? u[k] : 0.f; u[k] = a * a; }
      *(LAS v2u*)(L + a_bw) = v2u{pk2(u[0], u[1]), pk2(u[2], u[3])};
      BAR();
      STAMP(18);
    }
  } else {
    const size_t kvoff = ((size_t)grp * SK + (size_t)s * 32) * DH;
    constexpr int TSTR = 65536, NREC = 31 * 65536 + 16384;
#define ROW(n) (((n) & 31) * TSTR)
    const __amdgpu_buffer_rsrc_t rk = __builtin_amdgcn_make_buffer_rsrc((void*)(kc + kvoff), 0, NREC, RSRC3);
    const int voff = lane * 16 + wv * 4096;
    v4u X[NEXTRA * 4][4], R[4][4];
    unsigned tb = (unsigned)__builtin_amdgcn_s_memrealtime();
#define LANDED() asm volatile("s_waitcnt vmcnt(%0)" :: "i"(4 * (LEAD - 1)) : "memory")
    STAMP(0);
    if (!EARLY) BAR();
#pragma unroll
    for (int n = 0; n < NEXTRA * 4 + LEAD; ++n) {
      if (EARLY && n == EARLY) BAR();
      if (n >= LEAD) LANDED();
      if (n == 2) STAMP(20);
      if (n == 3) STAMP(21);
      if (n == 6) STAMP(22);
      if (n >= LEAD) pace(tb, n);
      if (n == NSTAR && wv == 0) {
        const unsigned late = (unsigned)__builtin_amdgcn_s_memrealtime() - tb - (unsigned)((NSTAR * TAU0) >> 4);
        *(volatile LAS v2u*)(L + L_ML) = v2u{tb, late};
        lds_signal(L, FL_ORG, lane, 1u);
      }
      issue_turn(n < NEXTRA * 4 ? X[n] : R[n - NEXTRA * 4], rk, voff, ROW(n));
    }
    const bool b0 = lane & 1, b1_ = lane & 2, b2_ = lane & 4, b3 = lane & 8;
    const int hf = lane >> 5, c = lane & 31;
    LAS float* P = (LAS float*)(L + L_P) + wv * 512;
    LAS float* Pl = P + ((lane >> 3) & 3) * 16 + hf * 8 + (lane & 7);
    STAMP(1);
    lds_wait_ge(FL + FL_Q, 4u, 0);
    STAMP(2);
#define NEW_TB() do { const unsigned ok_ = FL[FL_TGOK], t_ = FL[FL_TG]; tb = __builtin_amdgcn_readfirstlane(ok_ ? t_ : tb); } while (0)
    const v4u q0 = *(const LAS v4u*)(L + L_Q + c * 16), q1 = *(const LAS v4u*)(L + L_Q + 512 + c * 16);
    float mx = -INFINITY;
#pragma unroll
    for (int g = 0; g < NEXTRA; ++g) {
      float r[4], rx[4];
#pragma unroll
      for (int d = 0; d < 4; ++d) {
        rx[d] = k_turn(X[4 * g + d], q0, q1, b0, b1_, b2_);
        if ((d & 1) == 0) NEW_TB();
        SB();
        LANDED();
        pace(tb, (NEXTRA + g) * 4 + d + LEAD);
        issue_turn(R[(d + LEAD) & 3], rk, voff, ROW((NEXTRA + g) * 4 + d + LEAD));
        r[d] = k_turn(R[d], q0, q1, b0, b1_, b2_);
        SB();
      }
      const float sx = k_group(rx[0], rx[1], rx[2], rx[3], b3), sc = k_group(r[0], r[1], r[2], r[3], b3);
      mx = fmaxf(mx, fmaxf(sx, sc));
      Pl[g * 64] = sx; Pl[(NEXTRA + g) * 64] = sc;
      SB();
    }
    STAMP(3);
    lds_wait_ge(FL + FL_TGOK, 1u, 0);
    NEW_TB();
    const __amdgpu_buffer_rsrc_t rv = __builtin_amdgcn_make_buffer_rsrc((void*)(vc + kvoff), 0, NREC, RSRC3);
#pragma unroll 1
    for (int g = 2 * NEXTRA; g < 8; ++g) {
      float r[4];
#pragma unroll
      for (int d = 0; d < 4; ++d) {
        const int nx = g * 4 + d + LEAD;
        const bool tov = (d + LEAD >= 4) && g == 7;
        const __amdgpu_buffer_rsrc_t rn = __builtin_amdgcn_make_buffer_rsrc((void*)((tov ? vc : kc) + kvoff), 0, NREC, RSRC3);
        LANDED();
        pace1(tb, nx);
        issue_turn(R[(d + LEAD) & 3], rn, voff, ROW(nx));
        r[d] = k_turn(R[d], q0, q1, b0, b1_, b2_);
        SB();
      }
      const float sc = k_group(r[0], r[1], r[2], r[3], b3);
      mx = fmaxf(mx, sc);
      Pl[g * 64] = sc;
    }
    STAMP(4);
    mx = fmaxf(mx, dpp<XOR2>(mx)); mx = fmaxf(mx, dpp<ROR4>(mx)); mx = fmaxf(mx, dpp<ROR8>(mx));
    mx = sw16_max(mx); mx = sw32_max(mx);
    const float msub = mx == -INFINITY ? 0.f : mx;
    float ls = 0.f;
    {
      float sv[8];
#pragma unroll
      for (int g = 0; g < 8; ++g) sv[g] = Pl[g * 64];
#pragma unroll
      for (int g = 0; g < 8; ++g) { const float p = __builtin_amdgcn_exp2f((sv[g] - msub) * SC2); ls += p; Pl[g * 64] = p; }
    }
    ls += dpp<XOR2>(ls); ls += dpp<ROR8>(ls); ls += dpp<ROR4>(ls);
    ls = sw16_add(ls, ls); ls = sw32_add(ls, ls);
    *(LAS v2f*)(L + (lane < 2 ? L_ML + (wv * 2 + lane) * 8 : L_SINK + 1024 + lane * 8)) = v2f{mx, ls};
    lds_signal(L, FL_ML, lane, 1u);
    v2f a0[4], a1[4];
#pragma unroll
    for (int k = 0; k < 4; ++k) { a0[k] = v2f{0.f, 0.f}; a1[k] = v2f{0.f, 0.f}; }
    const LAS v4f* Pr = (const LAS v4f*)(P + hf * 8);
    LAS v4f* mo = (LAS v4f*)(L + L_MRG + wv * 2048 + lane * 32);
    auto v_group = [&](int g, auto last) {
      v4f pa[4], pb[4];
#pragma unroll
      for (int d = 0; d < 4; ++d) { pa[d] = Pr[(g * 4 + d) * 4]; pb[d] = Pr[(g * 4 + d) * 4 + 1]; }
#pragma unroll
      for (int d = 0; d < 4; ++d) {
        if (!last.value || d + LEAD < 4) {
          LANDED();
          pace2(tb, 32 + g * 4 + d + LEAD);
          issue_turn(R[(d + LEAD) & 3], rv, voff, ROW(g * 4 + d + LEAD));
        }
        v_turn(R[d], pa[d], pb[d], a0, a1);
        SB();
        if (!last.value && d == 1) lds_signal(L, FL_PRE, lane, g == PREG ? 1u : 0u);
      }
    };
#pragma unroll 1
    for (int g = 0; g < 7; ++g) v_group(g, BoolC<false>{});
    if (PRI) __builtin_amdgcn_s_setprio(2);
    v_group(7, BoolC<true>{});
    mo[0] = v4f{sw32_add(a0[0].x, a1[0].x), sw32_add(a0[0].y, a1[0].y), sw32_add(a0[1].x, a1[1].x), sw32_add(a0[1].y, a1[1].y)};
    mo[1] = v4f{sw32_add(a0[2].x, a1[2].x), sw32_add(a0[2].y, a1[2].y), sw32_add(a0[3].x, a1[3].x), sw32_add(a0[3].y, a1[3].y)};
    lds_signal(L, FL_DONE, lane, 1u);
    STAMP(5);
    if (isM) {
      const __amdgpu_buffer_rsrc_t r2 = __builtin_amdgcn_make_buffer_rsrc((void*)(w2 + m * 64 * D), 0, 64 * D * 2, RSRC3);
      lds_wait_ge(FL + FL_PUB, 2u, 0);
      __builtin_amdgcn_s_sleep(2);
      v4u uu[16];
#pragma unroll
      for (int i = 0; i < 16; ++i) uu[i] = LD128(r2, lane < 56 ? lane * 16 : 64 * D * 2, (i * 4 + wv) * (D * 2), LAUX);
      const int aw = lane < 56 ? L_RAW2 + wv * W2P + lane * 16 : L_SINK + lane * 16, aws = lane < 56 ? 4 * W2P : 0;
#pragma unroll
      for (int i = 0; i < 16; ++i) *(LAS v4u*)(L + aw + i * aws) = uu[i];
      STAMP(19);
      const unsigned tagl = __builtin_amdgcn_readfirstlane(FL[FL_TAGL]), th = __builtin_amdgcn_readfirstlane(FL[FL_TAGH]);
      const __amdgpu_buffer_rsrc_t rt = __builtin_amdgcn_make_buffer_rsrc((void*)(ws + (size_t)(ROT ? (th & 31u) + 1u : 0u) * WS_SPAN), 0, WS_END, RSRC3);
      const int g = lane >> 4;
      int p_st = WS_PF + ((m * 28 + wv * 7) * 8 + (lane & 15)) * 64 + g * 16;
      int a_br = L_BF + (lane & 15) * 144 + g * 16;
      int a2 = L_RAW2 + (g * 4 + ((lane & 15) >> 2)) * W2P + wv * 224 + (lane & 3) * 8;
      asm volatile("" : "+v"(p_st), "+v"(a_br), "+v"(a2));
      mlp_ln(L, rt, wv, lane, tagl, tb);
      __builtin_amdgcn_s_sleep(W2D);
      v4u Fr[28];
#pragma unroll
      for (int i = 0; i < 14; ++i) Fr[14 + i] = frag_tr(L + a2 + (i & 1) * 32 * W2P + (i >> 1) * 32, 16 * W2P);
      BAR();
      const v4u h0 = *(const LAS v4u*)(L + a_br), h1 = *(const LAS v4u*)(L + a_br + 64);
      const bool up = lane & 8;
      v4u ev[4];
#pragma unroll
      for (int i = 0; i < 4; ++i) {
        v4f ea = mfma(Fr[14 + 4 * i], h0, v4f{0.f, 0.f, 0.f, 0.f}), eb = ea;
        if (i < 3) eb = mfma(Fr[14 + 4 * i + 2], h0, v4f{0.f, 0.f, 0.f, 0.f});
        ea = mfma(Fr[14 + 4 * i + 1], h1, ea);
        if (i < 3) eb = mfma(Fr[14 + 4 * i + 3], h1, eb);
        ev[i] = pack24(v4f{up ? eb.x : ea.x, up ? eb.y : ea.y, up ? eb.z : ea.z, up ? eb.w : ea.w}, tagl);
        ST128(ev[i], rt, (i == 3 && up) ? WS_END : p_st + i * 1024, 0, AUX_SC1);
      }
      asm volatile("" :: "v"(ev[0]), "v"(ev[1]), "v"(ev[2]), "v"(ev[3]));
      STAMP(16);
    }
  }
}
}

extern "C" void kernel_launch(void* const* d_in, const int* in_sizes, int n_in, void* d_out, int out_size, void* d_ws,
                              size_t ws_size, hipStream_t stream) {
  using namespace fk;
  (void)hipFuncSetAttribute((const void*)fused_decode, hipFuncAttributeMaxDynamicSharedMemorySize, L_END + DBG_STAMPS * 2048);
  hipLaunchKernelGGL(fused_decode, dim3(256), dim3(512), L_END + DBG_STAMPS * 2048, stream, (const u16*)d_in[0], (const u16*)d_in[1],
                     (const u16*)d_in[2], (const u16*)d_in[3], (const u16*)d_in[4], (const u16*)d_in[6], (const u16*)d_in[7],
                     (const u16*)d_in[8], (const u16*)d_in[9], (u16*)d_out, (unsigned char*)d_ws);
}
```

```cpp
#include <hip/hip_runtime.h>
#include <hip/hip_bf16.h>
#include <stdint.h>

namespace fk {
typedef unsigned short u16;
typedef unsigned long long u64;
typedef unsigned v4u __attribute__((ext_vector_type(4)));
typedef unsigned v2u __attribute__((ext_vector_type(2)));
typedef float v4f __attribute__((ext_vector_type(4)));
typedef float v2f __attribute__((ext_vector_type(2)));
typedef __bf16 bf16x2 __attribute__((ext_vector_type(2)));
typedef __bf16 bf16x8 __attribute__((ext_vector_type(8)));
typedef short s16x4 __attribute__((ext_vector_type(4)));
#define LAS __attribute__((address_space(3)))
#define GAS __attribute__((address_space(1)))

constexpr int D = 448, F = 1792, SK = 4096, DH = 256, NQ = 4096;
constexpr int AUX_NT = 2, AUX_SC1 = 16;
constexpr int TAU0 = 950;
constexpr int TAU1 = 896;
constexpr int TAU = 886;
constexpr int NSTAR = 10;
constexpr int TGT = 860;
constexpr int OGAIN = 7;
constexpr int OKNEE = 30;
constexpr int OGAIN2 = 8;
constexpr int SENT_R5 = 5;
constexpr int HDELAY = 50;
constexpr int HSP = 20;
constexpr int QT = 600;
constexpr int QSLEEP = 24;
constexpr int VPT = 500;
constexpr int VDENSE = 262144;
constexpr int WOT = 1100;
constexpr int W1M = 4;
constexpr int W1H = 11;
constexpr int WOL = 85;
constexpr int W1L = 20;
constexpr int WT = 188;
constexpr int G1 = 87;
constexpr int G2 = 75;
constexpr int G3 = 156;
constexpr int WDELAY = 6;
constexpr int RDELAY = 8;
constexpr int FWAIT = 56;
constexpr int W2D = 2;
constexpr int PREG = 6;
constexpr int CODE_BYTES = 36200;
static_assert(W1H - W1M >= 4 && W1H <= 14, "the wo tile takes four loads per wave");
constexpr int NEXTRA = 3;
constexpr int LEAD = 2;
constexpr int PN0 = 16;
constexpr int RT = WT + G1, MT = RT + G2, FT = MT + G3;
constexpr int RSRC3 = 0x00020000;
constexpr float SC2 = 0.0625f * 1.4426950408889634f;
struct Spin {
  unsigned n = 0, t0 = 0;
  __device__ __forceinline__ bool expired() {
    if ((++n & 4095u) != 0u) return false;
    const unsigned t = (unsigned)__builtin_amdgcn_s_memrealtime();
    if (n == 4096u) t0 = t;
    return t - t0 > (1u << 27);
  }
};

constexpr int WS_EPOCH = 0;
constexpr int WS_T = 2048;
constexpr int WS_G = 4096;
constexpr int WS_ML = WS_G + 8 * 8 * 4 * 128 * 16;
constexpr int WS_H = WS_ML + 64 * 8 * 16;
constexpr int WS_P0 = WS_H + 16384;
constexpr int WS_PW = WS_P0 + 64 * 4 * 2048;
constexpr int WS_PF = WS_PW + 32 * 28 * 512;
constexpr int WS_END = WS_PF + 28 * 28 * 512;
constexpr int WS_SPAN = 2 << 20;
static_assert(WS_END <= WS_SPAN, "span");

constexpr int LN_STR = 912;
constexpr int L_P = 0;
constexpr int L_MRG = 8192;
constexpr int L_Q = 16384;
constexpr int L_FLAG = 17408;
constexpr int L_ML = L_FLAG + 64;
constexpr int L_LN = L_ML + 64;
constexpr int L_BF = L_LN + 8 * LN_STR;
constexpr int L_SINK = L_BF + 4096;
constexpr int L_RAW = L_SINK + 2048;
constexpr int L_RAW2 = L_RAW + 57344;
constexpr int W2P = 928, WOP = 160;
constexpr int L_END = L_RAW2 + 64 * W2P;
static_assert(L_END + 2048 <= 160 * 1024 && L_END > 80 * 1024, "lds");
static_assert(L_LN % 16 == 0 && L_BF % 16 == 0 && L_RAW % 16 == 0, "align");
enum { FL_LN = 0, FL_QP = 1, FL_Q = 2, FL_PRE = 3, FL_DONE = 4, FL_RAW = 5, FL_ML = 9, FL_TAGL = 10, FL_TAGH = 11, FL_TG = 12, FL_ORG = 13, FL_TGOK = 14, FL_PUB = 15 };

__device__ __forceinline__ float bflo(unsigned v) { return __builtin_bit_cast(float, v << 16); }
__device__ __forceinline__ float bfhi(unsigned v) { return __builtin_bit_cast(float, v & 0xffff0000u); }
__device__ __forceinline__ float rbf(float f) { return (float)(__bf16)f; }
__device__ __forceinline__ unsigned pk2(float lo, float hi) {
  bf16x2 v = {(__bf16)lo, (__bf16)hi};
  return __builtin_bit_cast(unsigned, v);
}
__device__ __forceinline__ unsigned pk2o(float lo, float hi) {
  unsigned p = pk2(lo, hi);
  asm("" : "+v"(p));
  return p;
}
__device__ __forceinline__ float dot2(unsigned a, unsigned b, float c) {
  return __builtin_amdgcn_fdot2_f32_bf16(__builtin_bit_cast(bf16x2, a), __builtin_bit_cast(bf16x2, b), c, false);
}
__device__ __forceinline__ v4f mfma(v4u a, v4u b, v4f c) {
  return __builtin_amdgcn_mfma_f32_16x16x32_bf16(__builtin_bit_cast(bf16x8, a), __builtin_bit_cast(bf16x8, b), c, 0, 0, 0);
}
template <int CTRL> __device__ __forceinline__ float dpp(float x) {
  return __builtin_bit_cast(float, __builtin_amdgcn_mov_dpp(__builtin_bit_cast(int, x), CTRL, 0xf, 0xf, true));
}
constexpr int XOR1 = 0xB1, XOR2 = 0x4E, HMIR = 0x141, ROR4 = 0x124, ROR8 = 0x128;
__device__ __forceinline__ float sw16_add(float a, float b) {
  auto s = __builtin_amdgcn_permlane16_swap(__float_as_uint(a), __float_as_uint(b), false, false);
  return __uint_as_float(s[0]) + __uint_as_float(s[1]);
}
__device__ __forceinline__ float sw32_add(float a, float b) {
  auto s = __builtin_amdgcn_permlane32_swap(__float_as_uint(a), __float_as_uint(b), false, false);
  return __uint_as_float(s[0]) + __uint_as_float(s[1]);
}
__device__ __forceinline__ float sw16_max(float a) {
  auto s = __builtin_amdgcn_permlane16_swap(__float_as_uint(a), __float_as_uint(a), false, false);
  return fmaxf(__uint_as_float(s[0]), __uint_as_float(s[1]));
}
__device__ __forceinline__ float sw32_max(float a) {
  auto s = __builtin_amdgcn_permlane32_swap(__float_as_uint(a), __float_as_uint(a), false, false);
  return fmaxf(__uint_as_float(s[0]), __uint_as_float(s[1]));
}
__device__ __forceinline__ v4u pack24(v4f v, unsigned tag) {
  const unsigned a = __float_as_uint(v.x), b = __float_as_uint(v.y), c = __float_as_uint(v.z), d = __float_as_uint(v.w);
  return v4u{__builtin_amdgcn_perm(b, a, 0x05030201u), __builtin_amdgcn_perm(c, b, 0x06050302u), __builtin_amdgcn_perm(d, c, 0x07060503u), tag};
}
__device__ __forceinline__ v4f unpack24(v4u g) {
  return v4f{__uint_as_float(g.x << 8), __uint_as_float(__builtin_amdgcn_perm(g.y, g.x, 0x0504030Cu)),
             __uint_as_float(__builtin_amdgcn_perm(g.z, g.y, 0x0403020Cu)), __uint_as_float(g.z & 0xFFFFFF00u)};
}
__device__ __forceinline__ v4u pack28(v4f v, unsigned tag) {
  return v4u{(__float_as_uint(v.x) & ~3u) | (tag & 3u), (__float_as_uint(v.y) & ~3u) | ((tag >> 2) & 3u),
             (__float_as_uint(v.z) & ~3u) | ((tag >> 4) & 3u), (__float_as_uint(v.w) & ~3u) | ((tag >> 6) & 3u)};
}
__device__ __forceinline__ v4f unpack28(v4u g) { return __builtin_bit_cast(v4f, g & ~3u); }
__device__ __forceinline__ bool bad28(v4u g, unsigned tag) {
  return (((g.x ^ tag) | (g.y ^ (tag >> 2)) | (g.z ^ (tag >> 4)) | (g.w ^ (tag >> 6))) & 3u) != 0u;
}
#define LGKM0() asm volatile("s_waitcnt lgkmcnt(0)" ::: "memory")
#define CLOB() asm volatile("" ::: "memory")
#define SB() __builtin_amdgcn_sched_barrier(0)
#define BAR() do { asm volatile("s_waitcnt lgkmcnt(0)" ::: "memory"); __builtin_amdgcn_s_barrier(); asm volatile("" ::: "memory"); __builtin_amdgcn_sched_barrier(0); } while (0)
#define LD128(rs, vo, so, aux) __builtin_amdgcn_raw_buffer_load_b128(rs, vo, so, aux)
#define ST128(v, rs, vo, so, aux) __builtin_amdgcn_raw_buffer_store_b128(v, rs, vo, so, aux)

__device__ __forceinline__ void lds_wait_ge(volatile LAS unsigned* f, unsigned want, int slp) {
  Spin sp;
#pragma nounroll
  while (*f < want) {
    if (slp) __builtin_amdgcn_s_sleep(8);
    if (sp.expired()) break;
  }
  CLOB();
}
__device__ __forceinline__ void lds_signal(LAS unsigned char* L, int word, int lane, unsigned inc) {
  LGKM0();
  LAS unsigned* p = (LAS unsigned*)(L + (lane == 0 ? L_FLAG + word * 4 : L_SINK + lane * 4));
  __hip_atomic_fetch_add(p, inc, __ATOMIC_RELAXED, __HIP_MEMORY_SCOPE_WORKGROUP);
}
template <int N, int STR, int WIDE, int SLP>
__device__ __forceinline__ void gather(v4u (&v)[N], __amdgpu_buffer_rsrc_t rs, int off, unsigned tl, unsigned th, u64 last_mask) {
  Spin sp;
#pragma nounroll
  for (;;) {
    CLOB();
#pragma unroll
    for (int i = 0; i < N; ++i) v[i] = LD128(rs, off + i * STR, 0, AUX_SC1);
    u64 any = 0ull;
#pragma unroll
    for (int i = 0; i < N; ++i) {
      const u64 bad = __builtin_amdgcn_ballot_w64(WIDE == 2 ? bad28(v[i], tl) : WIDE ? ((v[i].z ^ tl) | (v[i].w ^ th)) != 0u : v[i].w != tl);
      any |= i == N - 1 ? bad & last_mask : bad;
    }
    if (any == 0ull || sp.expired()) break;
    if (SLP) __builtin_amdgcn_s_sleep(SLP);
  }
}
__device__ __forceinline__ void wait_until(unsigned until) {
  asm volatile("8:\n\ts_memrealtime vcc\n\ts_waitcnt lgkmcnt(0)\n\ts_sub_i32 vcc_lo, vcc_lo, %0\n\ts_cmp_lt_i32 vcc_lo, 0\n\ts_cbranch_scc1 8b\n" :: "s"(until) : "scc", "vcc", "memory");
}
__device__ __forceinline__ void doze_until(unsigned until) {
#pragma nounroll
  while ((int)((unsigned)__builtin_amdgcn_s_memrealtime() - until) < 0) __builtin_amdgcn_s_sleep(4);
}
constexpr int PN1 = 34;
constexpr int SCHED63 = (PN0 * TAU0 + (PN1 - PN0) * TAU1 + (63 - PN1) * TAU) >> 4;
__device__ __forceinline__ void pace(unsigned tb, int n) { wait_until(tb + (unsigned)((n < PN0 ? n * TAU0 : PN0 * TAU0 + (n - PN0) * TAU1) >> 4)); }
__device__ __forceinline__ void pace1(unsigned tb, int n) { wait_until(tb + (unsigned)((PN0 * TAU0 + (n - PN0) * TAU1) >> 4)); }
__device__ __forceinline__ void pace2(unsigned tb, int n) { wait_until(tb + (unsigned)((PN0 * TAU0 + (PN1 - PN0) * TAU1 + (n - PN1) * TAU) >> 4)); }
template <bool B> struct BoolC { static constexpr bool value = B; };
__device__ __forceinline__ void issue_turn(v4u* r, __amdgpu_buffer_rsrc_t rs, int voff, int soff) {
#pragma unroll
  for (int i = 0; i < 4; ++i) r[i] = LD128(rs, voff + i * 1024, soff, AUX_NT);
  SB();
}
__device__ __forceinline__ float k_turn(const v4u* k, v4u q0, v4u q1, bool b0, bool b1, bool b2) {
  float x[8];
#pragma unroll
  for (int i = 0; i < 4; ++i) {
    float a0 = dot2(k[i].x, q0.x, 0.f), a1 = dot2(k[i].x, q1.x, 0.f);
    a0 = dot2(k[i].y, q0.y, a0); a1 = dot2(k[i].y, q1.y, a1);
    a0 = dot2(k[i].z, q0.z, a0); a1 = dot2(k[i].z, q1.z, a1);
    a0 = dot2(k[i].w, q0.w, a0); a1 = dot2(k[i].w, q1.w, a1);
    x[2 * i] = a0; x[2 * i + 1] = a1;
  }
  float y[4], z[2];
#pragma unroll
  for (int v = 0; v < 4; ++v) y[v] = (b2 ? x[v + 4] : x[v]) + dpp<HMIR>(b2 ? x[v] : x[v + 4]);
#pragma unroll
  for (int v = 0; v < 2; ++v) z[v] = (b1 ? y[v + 2] : y[v]) + dpp<XOR2>(b1 ? y[v] : y[v + 2]);
  return (b0 ? z[1] : z[0]) + dpp<XOR1>(b0 ? z[0] : z[1]);
}
__device__ __forceinline__ float k_group(float r0, float r1, float r2, float r3, bool b3) {
  const float u0 = (b3 ? r1 : r0) + dpp<ROR8>(b3 ? r0 : r1);
  const float u1 = (b3 ? r3 : r2) + dpp<ROR8>(b3 ? r2 : r3);
  return rbf(sw16_add(u0, u1));
}
__device__ __forceinline__ void v_turn(const v4u* v, v4f pa, v4f pb, v2f (&a0)[4], v2f (&a1)[4]) {
#pragma unroll
  for (int i = 0; i < 4; ++i) {
    const float p0 = i == 0 ? pa.x : i == 1 ? pa.z : i == 2 ? pb.x : pb.z;
    const float p1 = i == 0 ? pa.y : i == 1 ? pa.w : i == 2 ? pb.y : pb.w;
#pragma unroll
    for (int j = 0; j < 4; ++j) {
      const v2f e = {bflo(v[i][j]), bfhi(v[i][j])};
      a0[j] = __builtin_elementwise_fma(e, v2f{p0, p0}, a0[j]);
      a1[j] = __builtin_elementwise_fma(e, v2f{p1, p1}, a1[j]);
    }
  }
}
template <bool FULL> __device__ __forceinline__ float row_sum(float v) {
  v += dpp<XOR1>(v); v += dpp<XOR2>(v); v += dpp<HMIR>(v); v += dpp<ROR8>(v);
  v = sw16_add(v, v);
  return FULL ? sw32_add(v, v) : v;
}
template <int NW> __device__ __forceinline__ void ln_row(unsigned (&x)[NW], bool act) {
  v2f v[NW];
#pragma unroll
  for (int k = 0; k < NW; ++k) v[k] = v2f{bflo(x[k]), bfhi(x[k])};
  float mean, var;
  if constexpr (NW == 4) {
    const float c = bflo(__builtin_amdgcn_readfirstlane(x[0]));
    v2f s1 = {0.f, 0.f}, s2 = {0.f, 0.f};
#pragma unroll
    for (int k = 0; k < NW; ++k) { const v2f d = v[k] - c; s1 += d; s2 = __builtin_elementwise_fma(d, d, s2); }
    float t = sw32_add(act ? s1.x + s1.y : 0.f, act ? s2.x + s2.y : 0.f);
    t = row_sum<false>(t) * (1.0f / D);
    const float m1 = __builtin_bit_cast(float, __builtin_amdgcn_readlane(__builtin_bit_cast(int, t), 0));
    const float m2 = __builtin_bit_cast(float, __builtin_amdgcn_readlane(__builtin_bit_cast(int, t), 32));
    mean = c + m1;
    var = fmaxf(fmaf(-m1, m1, m2), m2 - m2);
  } else {
    constexpr unsigned ONES = 0x3F803F80u;
    float sa = 0.f, sb = 0.f;
#pragma unroll
    for (int k = 0; k < NW; k += 2) { sa = dot2(x[k], ONES, sa); sb = dot2(x[k + 1], ONES, sb); }
    mean = row_sum<false>(sa + sb) * (1.0f / D);
    v2f q0 = {0.f, 0.f}, q1 = {0.f, 0.f};
#pragma unroll
    for (int k = 0; k < NW; k += 2) {
      const v2f d0 = v[k] - mean, d1 = v[k + 1] - mean;
      q0 = __builtin_elementwise_fma(d0, d0, q0); q1 = __builtin_elementwise_fma(d1, d1, q1);
    }
    q0 += q1;
    var = row_sum<false>(act ? q0.x + q0.y : 0.f) * (1.0f / D);
  }
  const float mb = rbf(mean);
#pragma unroll
  for (int k = 0; k < NW; ++k) { const v2f d = v[k] - mb; const unsigned p = pk2o(d.x, d.y); v[k] = v2f{bflo(p), bfhi(p)}; }
  const float den = rbf(__builtin_amdgcn_sqrtf(rbf(rbf(var) + 1e-5f)));
  const float ri = __builtin_amdgcn_rcpf(den);
#pragma unroll
  for (int k = 0; k < NW; ++k) { const v2f a = v[k] * ri; x[k] = pk2(a.x, a.y); }
}
__device__ __forceinline__ v4u frag_tr(const LAS unsigned char* p, int hi) {
#if defined(__HIP_DEVICE_COMPILE__)
  const v2u a = __builtin_bit_cast(v2u, __builtin_amdgcn_ds_read_tr16_b64_v4i16((LAS s16x4*)p));
  const v2u b = __builtin_bit_cast(v2u, __builtin_amdgcn_ds_read_tr16_b64_v4i16((LAS s16x4*)(p + hi)));
  return v4u{a.x, a.y, b.x, b.y};
#else
  return v4u{0u, 0u, 0u, 0u};
#endif
}
__device__ __forceinline__ float red16t(v4f t) {
  float a = sw16_add(sw32_add(t.x, t.y), sw32_add(t.z, t.w));
  a += dpp<ROR8>(a); a += dpp<ROR4>(a);
  return a;
}
__device__ __forceinline__ v2u quad_pack(float v) {
  auto s = __builtin_amdgcn_permlane32_swap(__float_as_uint(v), __float_as_uint(v), false, false);
  const unsigned p = __builtin_amdgcn_perm(s[1], s[0], 0x07060302u);
  auto q = __builtin_amdgcn_permlane16_swap(p, p, false, false);
  return v2u{q[0], q[1]};
}
__device__ __forceinline__ void mlp_ln(LAS unsigned char* L, __amdgpu_buffer_rsrc_t rt, int row, int lane, unsigned tl, unsigned t0) {
  const bool act = lane < 56;
  const int a = act ? L_LN + row * LN_STR + lane * 8 : L_SINK + lane * 8;
  v4u hg[2];
  int o0 = WS_H + (row * 112 + (act ? lane : 55)) * 16;
  asm volatile("" : "+v"(o0));
  wait_until(t0 + SCHED63 + MT);
  gather<2, 896, 0, 0>(hg, rt, o0, tl, 0u, ~0ull);
  unsigned x[4] = {hg[0].x, hg[0].y, hg[1].x, hg[1].y};
  ln_row<4>(x, act);
  *(LAS v2u*)(L + a) = v2u{x[0], x[1]}; *(LAS v2u*)(L + a + 448) = v2u{x[2], x[3]};
  BAR();
}

__global__ void __launch_bounds__(512, 2)
fused_decode(const u16* __restrict__ x, const u16* __restrict__ kc, const u16* __restrict__ vc, const u16* __restrict__ wq,
             const u16* __restrict__ wo, const u16* __restrict__ w1, const u16* __restrict__ b1, const u16* __restrict__ w2,
             const u16* __restrict__ b2, u16* __restrict__ out, unsigned char* __restrict__ ws) {
  extern __shared__ __attribute__((aligned(16))) unsigned char smem[];
  LAS unsigned char* L = (LAS unsigned char*)smem;
  const u64 pc0 = __builtin_amdgcn_s_getpc() & ~0xFFull;
  const int w = blockIdx.x, tid = threadIdx.x, lane = tid & 63, wv = __builtin_amdgcn_readfirstlane(tid >> 6);
  const int kvh = w & 7, r5 = w >> 3, b = r5 & 7, s = r5 >> 3, grp = b * 8 + kvh;
  const int m = (r5 - 28) * 8 + kvh;
  const bool isM = r5 >= 28 && m < 28;
  volatile LAS unsigned* FL = (volatile LAS unsigned*)(L + L_FLAG);
  const __amdgpu_buffer_rsrc_t rws = __builtin_amdgcn_make_buffer_rsrc((void*)ws, 0, WS_END, RSRC3);
  auto code_prefetch = [&]() {
    const __amdgpu_buffer_rsrc_t rc = __builtin_amdgcn_make_buffer_rsrc((void*)pc0, 0, CODE_BYTES, RSRC3);
    constexpr int CPL = (CODE_BYTES / 128 + 31) / 32;
    return __builtin_amdgcn_raw_buffer_load_b32(rc, lane < CPL ? (lane * 32 + r5) * 128 : CODE_BYTES, 0, 0);
  };
  unsigned cpf = 0;
  if (wv == 4) cpf = code_prefetch();
  SB();
  asm volatile("" :: "s"(x), "s"(kc), "s"(vc), "s"(wq), "s"(wo), "s"(w1), "s"(b1), "s"(w2), "s"(b2), "s"(out), "s"(ws));

  if (__builtin_expect(wv >= 4, 0)) {
    const int hw = wv - 4;
    __builtin_amdgcn_s_setprio(3);
    const unsigned tstart = (unsigned)__builtin_amdgcn_s_memrealtime();
    const bool isW = r5 < 28;
    const int t = r5 >> 2, kq = r5 & 3;
    const int g = lane >> 4;
    const LAS unsigned char* ap = L + L_RAW + (g * 8 + ((lane & 15) >> 2)) * 128 + hw * 32 + (lane & 3) * 8;
    const LAS unsigned char* bp = L + L_LN + (lane & 7) * LN_STR + g * 16;
    const __amdgpu_buffer_rsrc_t rx = __builtin_amdgcn_make_buffer_rsrc((void*)x, 0, 8 * D * 2, RSRC3);
    if (tid < 256 + 16) FL[tid - 256] = 0u;
    BAR();
    doze_until(tstart + HDELAY + (r5 & 7) * HSP);
    const v2u ep = __builtin_amdgcn_raw_buffer_load_b64(rws, WS_EPOCH, 0, AUX_SC1);
    unsigned tagl, tagh;
#define MAKE_TAGS() do { const unsigned el = __builtin_amdgcn_readfirstlane(ep.x), eh = __builtin_amdgcn_readfirstlane(ep.y); \
      u64 z = (((u64)eh << 32) | el) + 0x9E3779B97F4A7C15ull; \
      z = (z ^ (z >> 30)) * 0xBF58476D1CE4E5B9ull; z = (z ^ (z >> 27)) * 0x94D049BB133111EBull; z ^= z >> 31; \
      tagl = (((unsigned)z ^ el) & 3u) == 0u ? (unsigned)z ^ 1u : (unsigned)z; \
      tagl = ((tagl ^ (tagl >> 2)) & 3u) == 0u ? tagl ^ 4u : tagl;     \
      tagh = ((unsigned)(z >> 32) & ~31u) | ((eh + 1u) & 31u); } while (0)
    {
      const int row = hw * 2 + (lane >> 5), c = lane & 31;
      const int ks0 = s < 3 ? s * 4 : 11, nks = s < 2 ? 4 : 3;
      const int xo = c < 28 ? row * (D * 2) + c * 32 : 8 * D * 2;
      const v4u xa = LD128(rx, xo, 0, 0), xb = LD128(rx, xo + 16, 0, 0);
      const __amdgpu_buffer_rsrc_t rq = __builtin_amdgcn_make_buffer_rsrc((void*)(wq + ks0 * 32 * NQ + (kvh * 8 + b) * 64), 0, (nks * 32 - 1) * (NQ * 2) + 128, RSRC3);
      v4u tt[4];
#pragma unroll
      for (int i = 0; i < 4; ++i) tt[i] = LD128(rq, (lane >> 3) * (NQ * 2) + (lane & 7) * 16, (i * 4 + hw) * 8 * (NQ * 2), AUX_NT);
      SB();
      {
        unsigned xr[8] = {xa.x, xa.y, xa.z, xa.w, xb.x, xb.y, xb.z, xb.w};
        ln_row<8>(xr, c < 28);
        const int a = c < 28 ? L_LN + row * LN_STR + c * 32 : L_SINK + lane * 32;
        *(LAS v4u*)(L + a) = v4u{xr[0], xr[1], xr[2], xr[3]}; *(LAS v4u*)(L + a + 16) = v4u{xr[4], xr[5], xr[6], xr[7]};
      }
      MAKE_TAGS();
#pragma unroll
      for (int i = 0; i < 4; ++i) *(LAS v4u*)(L + L_RAW + (i * 4 + hw) * 1024 + lane * 16) = tt[i];
      lds_signal(L, FL_LN, lane, 1u);
      lds_wait_ge(FL + FL_LN, 4u, 0);
      v4f d0 = {0.f, 0.f, 0.f, 0.f};
#pragma unroll
      for (int ks = 0; ks < 3; ++ks) d0 = mfma(frag_tr(ap + ks * 4096, 512), *(const LAS v4u*)(bp + (ks0 + ks) * 64), d0);
      if (s < 2) d0 = mfma(frag_tr(ap + 3 * 4096, 512), *(const LAS v4u*)(bp + (ks0 + 3) * 64), d0);
      ST128(pack28(d0, tagl >> (8 * g)), rws, (lane & 8) == 0 ? WS_G + (((kvh * 8 + (lane & 7)) * 4 + s) * 128 + b * 16 + hw * 4 + g) * 16 : WS_END, 0, AUX_SC1);
      lds_signal(L, FL_QP, lane, 1u);
    }
    auto q_gather = [&]() {
      const int off = WS_G + (kvh * 8 + b) * 8192 + ((hw - 2) * 64 + lane) * 16;
      doze_until(tstart + QT);
      v4u gq[4];
      gather<4, 2048, 2, QSLEEP>(gq, rws, off, tagl >> (8 * (lane & 3)), 0u, ~0ull);
      const v4f qv = (unpack28(gq[0]) + unpack28(gq[1])) + (unpack28(gq[2]) + unpack28(gq[3]));
      *(LAS v2u*)(L + L_Q + ((hw - 2) * 64 + lane) * 8) = v2u{pk2(qv.x, qv.y), pk2(qv.z, qv.w)};
      FL[FL_TAGL] = tagl; FL[FL_TAGH] = tagh;
      lds_signal(L, FL_Q, lane, 2u);
    };
    if (hw >= 2) q_gather();
    if (hw == 0 && r5 == SENT_R5) {
      lds_wait_ge(FL + FL_ORG, 1u, 1);
      const v2u og = *(volatile LAS v2u*)(L + L_ML);
      ST128((v4u{og.x, og.y, tagl, tagh}), rws, lane == 0 ? WS_T + kvh * 16 : WS_END, 0, AUX_SC1);
    }
    if (hw == 1) {
      doze_until(tstart + TGT);
      v4u gt[1];
      gather<1, 0, 1, 32>(gt, rws, WS_T + (lane & 7) * 16, tagl, tagh, ~0ull);
      const unsigned t0 = __builtin_amdgcn_readfirstlane(gt[0].x);
      int o = (int)(gt[0].x - t0), lt = (int)gt[0].y;
      o += __builtin_amdgcn_mov_dpp(o, XOR1, 0xf, 0xf, true); o += __builtin_amdgcn_mov_dpp(o, XOR2, 0xf, 0xf, true); o += __builtin_amdgcn_mov_dpp(o, HMIR, 0xf, 0xf, true);
      lt += __builtin_amdgcn_mov_dpp(lt, XOR1, 0xf, 0xf, true); lt += __builtin_amdgcn_mov_dpp(lt, XOR2, 0xf, 0xf, true); lt += __builtin_amdgcn_mov_dpp(lt, HMIR, 0xf, 0xf, true);
      lt >>= 3;
      o = (o + lt * OGAIN + (lt > OKNEE ? (lt - OKNEE) * OGAIN2 : 0)) >> 3;
      o = o < -1000 ? -1000 : o > 2000 ? 2000 : o;
      const unsigned tgv = t0 + (unsigned)o;
      FL[FL_TG] = tgv - tstart + 4000u < 8000u ? tgv : tstart;
      lds_signal(L, FL_TGOK, lane, 1u);
    }
#define CAS __attribute__((address_space(4)))
    if (hw == 0 && (s == 1 || r5 == 2)) {
      doze_until(tstart + VPT);
      if (s == 1) {
        const unsigned z0 = *(const CAS unsigned*)(vc + (size_t)grp * SK * DH), z1 = *(const CAS unsigned*)(vc + ((size_t)grp * SK + SK - 1) * DH);
        asm volatile("" :: "s"(z0), "s"(z1));
      } else {
        const unsigned z4 = *(const CAS unsigned*)(ws + (size_t)((tagh & 31u) + 1u) * WS_SPAN), z5 = *(const CAS unsigned*)(ws + (size_t)((tagh & 31u) + 2u) * WS_SPAN - 64);
        const unsigned z7 = *(const CAS unsigned*)(out + kvh * D), z8 = *(const CAS unsigned*)(out + 8 * D - 2);
        asm volatile("" :: "s"(z4), "s"(z5), "s"(z7), "s"(z8));
#pragma unroll 1
        for (int o = 0; o < D * F * 2; o += VDENSE) {
          const unsigned y0 = *(const CAS unsigned*)((const unsigned char*)w1 + o), y1 = *(const CAS unsigned*)((const unsigned char*)w2 + o);
          const unsigned y2 = *(const CAS unsigned*)((const unsigned char*)wo + kvh * (512 * D * 2) + (o >> 2));
          asm volatile("" :: "s"(y0), "s"(y1), "s"(y2));
        }
      }
    }
    lds_wait_ge(FL + FL_Q, 4u, 1);
    lds_wait_ge(FL + FL_QP, 4u, 1);
    v4f cx, cb;
    {
      const v2u bv = *((const GAS v2u*)(isW ? b2 + r5 * 16 + (lane & 3) * 4 : b1 + (isM ? m : 0) * 64 + hw * 16 + g * 4));
      const v2u xv = __builtin_amdgcn_raw_buffer_load_b64(rx, (kvh * D + r5 * 16 + (lane & 3) * 4) * 2, 0, 0);
      cb = v4f{bflo(bv.x), bfhi(bv.x), bflo(bv.y), bfhi(bv.y)};
      cx = v4f{bflo(xv.x), bfhi(xv.x), bflo(xv.y), bfhi(xv.y)};
    }
    const __amdgpu_buffer_rsrc_t rw = __builtin_amdgcn_make_buffer_rsrc(
        isW ? (void*)(wo + (kvh * 512 + kq * 128) * D + t * 64) : (void*)(w1 + (isM ? m : 0) * 64), 0, isW ? 127 * (D * 2) + 128 : isM ? 447 * (F * 2) + 128 : 0, RSRC3);
    const int pitch = isW ? D * 2 : F * 2;
    int wvo = (lane >> 3) * pitch + (lane & 7) * 16;
    asm volatile("" : "+v"(wvo));
    if (isM) {
      doze_until(tstart + WOT);
      v4u t1[W1M];
#pragma unroll
      for (int i = 0; i < W1M; ++i) t1[i] = LD128(rw, wvo, (i * 4 + hw) * 8 * (F * 2), AUX_NT);
#pragma unroll
      for (int i = 0; i < W1M; ++i) *(LAS v4u*)(L + L_RAW + (i * 4 + hw) * 1024 + lane * 16) = t1[i];
    }
    asm volatile("" :: "v"(cpf));
    const __amdgpu_buffer_rsrc_t rt = __builtin_amdgcn_make_buffer_rsrc((void*)(ws + (size_t)((tagh & 31u) + 1u) * WS_SPAN), 0, WS_END, RSRC3);
    float fw[4] = {0.f, 0.f, 0.f, 0.f};
    if (hw < 2) {
      lds_wait_ge(FL + FL_ML, 4u, 1);
      const v2f ml = *(const LAS v2f*)(L + L_ML + ((lane & 3) * 2 + hw) * 8);
      float Ms = ml.x;
      Ms = fmaxf(Ms, dpp<XOR1>(Ms)); Ms = fmaxf(Ms, dpp<XOR2>(Ms));
      const float Msub = Ms == -INFINITY ? 0.f : Ms;
      float Lw = __builtin_amdgcn_exp2f((ml.x - Msub) * SC2) * ml.y;
      Lw += dpp<XOR1>(Lw); Lw += dpp<XOR2>(Lw);
      ST128((v4u{__float_as_uint(Ms), __float_as_uint(Lw), tagl, tagh}), rt, lane == 0 ? WS_ML + ((grp * 2 + hw) * 4 + s) * 16 : WS_END, 0, AUX_SC1);
      const int off = WS_ML + ((grp * 2 + hw) * 4 + (lane & 3)) * 16;
      v4u gm1[1];
      __builtin_amdgcn_s_sleep(96); __builtin_amdgcn_s_sleep(96);
      gather<1, 0, 1, 64>(gm1, rt, off, tagl, tagh, ~0ull);
      const v4u gm = gm1[0];
      float M = __uint_as_float(gm.x);
      M = fmaxf(M, dpp<XOR1>(M)); M = fmaxf(M, dpp<XOR2>(M));
      float Ls = __builtin_amdgcn_exp2f((__uint_as_float(gm.x) - M) * SC2) * __uint_as_float(gm.y);
      Ls += dpp<XOR1>(Ls); Ls += dpp<XOR2>(Ls);
      const float f = __builtin_amdgcn_exp2f((ml.x - M) * SC2) / Ls;
#pragma unroll
      for (int i = 0; i < 4; ++i) fw[i] = __builtin_bit_cast(float, __builtin_amdgcn_readlane(__builtin_bit_cast(int, f), i));
    }
    lds_wait_ge(FL + FL_TGOK, 1u, 1);
    const unsigned tg = __builtin_amdgcn_readfirstlane(FL[FL_TG]);
    float cx1 = g == 0 ? cx.x : g == 1 ? cx.z : g == 2 ? cx.y : cx.w, cb1 = g == 0 ? cb.x : g == 1 ? cb.z : g == 2 ? cb.y : cb.w;
    asm volatile("" : "+v"(cx1), "+v"(cb1), "+v"(cb));
    int p_p0 = WS_P0 + ((kvh * 8 + b) * 4 + s) * 2048 + (hw & 1) * 1024 + lane * 16;
    int p_st = (lane & 8) == 0 ? WS_PW + (((kvh * 4 + kq) * 28 + t * 4 + hw) * 8 + (lane & 7)) * 64 + g * 16 : WS_END;
    int p_h = lane < 4 ? WS_H + (kvh * 112 + r5 * 4 + lane) * 16 : WS_END;
    int g_p0 = WS_P0 + (kvh * 8 + (lane >> 3)) * 8192 + (kq * 32 + hw * 8 + (lane & 7)) * 16;
    int g_pw = WS_PW + (((lane >> 2) * 28 + r5) * 8 + kvh) * 64 + (lane & 3) * 16;
    int g_o = lane < 4 ? (kvh * D + r5 * 16 + lane * 4) * 2 : 8 * D * 2;
    int g_e = (w == 0 && lane == 0) ? WS_EPOCH : WS_END;
    int a_bw = isW ? L_BF + hw * 512 + (lane >> 3) * 64 + (lane & 3) * 16 + ((lane >> 2) & 1) * 8 : L_BF + (lane & 15) * 144 + (hw >> 1) * 64 + g * 16 + (hw & 1) * 8;
    int a_br = isW ? L_BF + (lane & 7) * 64 + g * 16 : L_LN + (lane & 7) * LN_STR + ((lane >> 3) & 1) * 64 + g * 16;
    int a_ow = L_RAW + (hw * 8 + (lane >> 3)) * WOP + (lane & 7) * 16, a_or = L_RAW + (g * 4 + ((lane & 15) >> 2)) * WOP + hw * 32 + (lane & 3) * 8;
    asm volatile("" : "+v"(a_ow), "+v"(a_or));
    asm volatile("" : "+v"(p_p0), "+v"(p_st), "+v"(p_h), "+v"(g_p0), "+v"(g_pw), "+v"(g_o), "+v"(g_e), "+v"(a_bw), "+v"(a_br));
    const __amdgpu_buffer_rsrc_t rout = __builtin_amdgcn_make_buffer_rsrc((void*)out, 0, 8 * D * 2, RSRC3);
    lds_wait_ge(FL + FL_PRE, 4u, 1);
    if (hw == 3) { const unsigned z = code_prefetch(); asm volatile("" :: "v"(z)); }
    v4u to[14];
    wait_until(tg + SCHED63 + (isW ? WOL : W1L));
    if (isW) {
#pragma unroll
      for (int i = 0; i < 4; ++i) to[W1M + i] = LD128(rw, wvo, (i * 4 + hw) * 8 * (D * 2), AUX_NT);
    } else {
#pragma unroll
      for (int i = W1M; i < W1H; ++i) to[i] = LD128(rw, wvo, (i * 4 + hw) * 8 * (F * 2), AUX_NT);
    }
    SB();
    lds_wait_ge(FL + FL_DONE, 4u, 0);
    if (hw < 2) {
      const LAS v4f* mp = (const LAS v4f*)(L + L_MRG + hw * 1024 + lane * 16);
      const v4f o = (mp[0] * fw[0] + mp[128] * fw[1]) + (mp[256] * fw[2] + mp[384] * fw[3]);
      ST128(pack24(o, tagl), rt, p_p0, 0, AUX_SC1);
      if (isM) lds_signal(L, FL_PUB, lane, 1u);
    }
    if (isW) {
      __builtin_amdgcn_s_sleep(WDELAY);
      wait_until(tg + SCHED63 + WT);
      v4u pv[4];
      {
        CLOB();
#pragma unroll
        for (int i = 0; i < 4; ++i) pv[i] = LD128(rt, g_p0 + i * 2048, 0, AUX_SC1);
        SB();
#pragma unroll
        for (int i = 0; i < 4; ++i) *(LAS v4u*)(L + a_ow + i * 32 * WOP) = to[W1M + i];
        SB();
        const u64 bad = __builtin_amdgcn_ballot_w64((((pv[0].w ^ tagl) | (pv[1].w ^ tagl)) | ((pv[2].w ^ tagl) | (pv[3].w ^ tagl))) != 0u);
        if (__builtin_expect(bad != 0ull, 0)) gather<4, 2048, 0, 0>(pv, rt, g_p0, tagl, 0u, ~0ull);
      }
      const v4f o = (unpack24(pv[0]) + unpack24(pv[1])) + (unpack24(pv[2]) + unpack24(pv[3]));
      *(LAS v2u*)(L + a_bw) = v2u{pk2(o.x, o.y), pk2(o.z, o.w)};
      BAR();
      v4u bf[4], Fr[4];
#pragma unroll
      for (int ks = 0; ks < 4; ++ks) { Fr[ks] = frag_tr(L + a_or + ks * 32 * WOP, 16 * WOP); bf[ks] = *(const LAS v4u*)(L + a_br + ks * 512); }
      SB();
      v4f d0 = {0.f, 0.f, 0.f, 0.f}, d1 = {0.f, 0.f, 0.f, 0.f};
      d0 = mfma(Fr[0], bf[0], d0); d1 = mfma(Fr[1], bf[1], d1); d0 = mfma(Fr[2], bf[2], d0); d1 = mfma(Fr[3], bf[3], d1);
      ST128(pack24(d0 + d1, tagl), rt, p_st, 0, AUX_SC1);
      if (hw == 0) {
        v4u u[2];
        __builtin_amdgcn_s_sleep(RDELAY);
        wait_until(tg + SCHED63 + RT);
        gather<2, 16 * 28 * 512, 0, 0>(u, rt, g_pw, tagl, 0u, ~0ull);
        const float hv = rbf(cx1 + rbf(red16t(unpack24(u[0]) + unpack24(u[1]))));
        const v2u hq = quad_pack(hv);
        ST128((v4u{hq.x, hq.y, tagh, tagl}), rt, p_h, 0, AUX_SC1);
        __builtin_amdgcn_s_sleep(FWAIT);
        wait_until(tg + SCHED63 + FT);
        gather<2, 16 * 28 * 512, 0, 0>(u, rt, g_pw + (WS_PF - WS_PW), tagl, 0u, (1ull << 48) - 1ull);
        const float ov = rbf(hv + rbf(rbf(red16t(unpack24(u[0]) + unpack24(u[1]))) + cb1));
        __builtin_amdgcn_raw_buffer_store_b64(quad_pack(ov), rout, g_o, 0, 0);
        __builtin_amdgcn_raw_buffer_store_b64(v2u{tagl, tagh}, rws, g_e, 0, AUX_SC1);
      }
    } else if (isM) {
      v4u Fr[14];
      lds_wait_ge(FL + FL_PUB, 2u, 0);
#pragma unroll
      for (int i = W1H; i < 14; ++i) to[i] = LD128(rw, wvo, (i * 4 + hw) * 8 * (F * 2), AUX_NT);
      SB();
#pragma unroll
      for (int i = W1M; i < 14; ++i) *(LAS v4u*)(L + L_RAW + (i * 4 + hw) * 1024 + lane * 16) = to[i];
      lds_signal(L, FL_RAW, lane, 1u);
      lds_wait_ge(FL + FL_RAW, 4u, 0);
#pragma unroll
      for (int ks = 0; ks < 14; ++ks) Fr[ks] = frag_tr(ap + ks * 4096, 512);
#pragma unroll
      for (int ks = 0; ks < 14; ++ks) asm volatile("" : "+v"(Fr[ks]));
      mlp_ln(L, rt, wv, lane, tagl, tg);
      v4u bf[7];
#pragma unroll
      for (int ks = 0; ks < 7; ++ks) bf[ks] = *(const LAS v4u*)(L + a_br + ks * 128);
      SB();
      v4f d0 = {0.f, 0.f, 0.f, 0.f}, d1 = {0.f, 0.f, 0.f, 0.f};
#pragma unroll
      for (int ks = 0; ks < 7; ++ks) { d0 = mfma(Fr[2 * ks], bf[ks], d0); d1 = mfma(Fr[2 * ks + 1], bf[ks], d1); }
#pragma unroll
      for (int k = 0; k < 4; ++k) { const float tq = (lane & 8) ? d1[k] : d0[k]; d0[k] = tq + dpp<ROR8>(tq); }
      const unsigned g01 = pk2o(d0.x, d0.y), g23 = pk2o(d0.z, d0.w);
      const unsigned a01 = pk2o(bflo(g01) + cb.x, bfhi(g01) + cb.y), a23 = pk2o(bflo(g23) + cb.z, bfhi(g23) + cb.w);
      float u[4] = {bflo(a01), bfhi(a01), bflo(a23), bfhi(a23)};
#pragma unroll
      for (int k = 0; k < 4; ++k) { const float a = u[k] > 0.f ? u[k] : 0.f; u[k] = a * a; }
      *(LAS v2u*)(L + a_bw) = v2u{pk2(u[0], u[1]), pk2(u[2], u[3])};
      BAR();
    }
  } else {
    const size_t kvoff = ((size_t)grp * SK + (size_t)s * 32) * DH;
    constexpr int TSTR = 65536, NREC = 31 * 65536 + 16384;
#define ROW(n) (((n) & 31) * TSTR)
    const __amdgpu_buffer_rsrc_t rk = __builtin_amdgcn_make_buffer_rsrc((void*)(kc + kvoff), 0, NREC, RSRC3);
    const int voff = lane * 16 + wv * 4096;
    v4u X[NEXTRA * 4][4], R[4][4];
    unsigned tb = (unsigned)__builtin_amdgcn_s_memrealtime();
#define LANDED() asm volatile("s_waitcnt vmcnt(%0)" :: "i"(4 * (LEAD - 1)) : "memory")
#pragma unroll
    for (int n = 0; n < NEXTRA * 4 + LEAD; ++n) {
      if (n == LEAD) BAR();
      if (n >= LEAD) LANDED();
      if (n >= LEAD) pace(tb, n);
      if (n == NSTAR && wv == 0) {
        const unsigned late = (unsigned)__builtin_amdgcn_s_memrealtime() - tb - (unsigned)((NSTAR * TAU0) >> 4);
        *(volatile LAS v2u*)(L + L_ML) = v2u{tb, late};
        lds_signal(L, FL_ORG, lane, 1u);
      }
      issue_turn(n < NEXTRA * 4 ? X[n] : R[n - NEXTRA * 4], rk, voff, ROW(n));
    }
    const bool b0 = lane & 1, b1_ = lane & 2, b2_ = lane & 4, b3 = lane & 8;
    const int hf = lane >> 5, c = lane & 31;
    LAS float* P = (LAS float*)(L + L_P) + wv * 512;
    LAS float* Pl = P + ((lane >> 3) & 3) * 16 + hf * 8 + (lane & 7);
    lds_wait_ge(FL + FL_Q, 4u, 0);
#define NEW_TB() do { const unsigned ok_ = FL[FL_TGOK], t_ = FL[FL_TG]; tb = __builtin_amdgcn_readfirstlane(ok_ ? t_ : tb); } while (0)
    const v4u q0 = *(const LAS v4u*)(L + L_Q + c * 16), q1 = *(const LAS v4u*)(L + L_Q + 512 + c * 16);
    float mx = -INFINITY;
#pragma unroll
    for (int g = 0; g < NEXTRA; ++g) {
      float r[4], rx[4];
#pragma unroll
      for (int d = 0; d < 4; ++d) {
        rx[d] = k_turn(X[4 * g + d], q0, q1, b0, b1_, b2_);
        if ((d & 1) == 0) NEW_TB();
        SB();
        LANDED();
        pace(tb, (NEXTRA + g) * 4 + d + LEAD);
        issue_turn(R[(d + LEAD) & 3], rk, voff, ROW((NEXTRA + g) * 4 + d + LEAD));
        r[d] = k_turn(R[d], q0, q1, b0, b1_, b2_);
        SB();
      }
      const float sx = k_group(rx[0], rx[1], rx[2], rx[3], b3), sc = k_group(r[0], r[1], r[2], r[3], b3);
      mx = fmaxf(mx, fmaxf(sx, sc));
      Pl[g * 64] = sx; Pl[(NEXTRA + g) * 64] = sc;
      SB();
    }
    lds_wait_ge(FL + FL_TGOK, 1u, 0);
    NEW_TB();
    const __amdgpu_buffer_rsrc_t rv = __builtin_amdgcn_make_buffer_rsrc((void*)(vc + kvoff), 0, NREC, RSRC3);
#pragma unroll 1
    for (int g = 2 * NEXTRA; g < 8; ++g) {
      float r[4];
#pragma unroll
      for (int d = 0; d < 4; ++d) {
        const int nx = g * 4 + d + LEAD;
        const bool tov = (d + LEAD >= 4) && g == 7;
        const __amdgpu_buffer_rsrc_t rn = __builtin_amdgcn_make_buffer_rsrc((void*)((tov ? vc : kc) + kvoff), 0, NREC, RSRC3);
        LANDED();
        pace1(tb, nx);
        issue_turn(R[(d + LEAD) & 3], rn, voff, ROW(nx));
        r[d] = k_turn(R[d], q0, q1, b0, b1_, b2_);
        SB();
      }
      const float sc = k_group(r[0], r[1], r[2], r[3], b3);
      mx = fmaxf(mx, sc);
      Pl[g * 64] = sc;
    }
    mx = fmaxf(mx, dpp<XOR2>(mx)); mx = fmaxf(mx, dpp<ROR4>(mx)); mx = fmaxf(mx, dpp<ROR8>(mx));
    mx = sw16_max(mx); mx = sw32_max(mx);
    const float msub = mx == -INFINITY ? 0.f : mx;
    float ls = 0.f;
    {
      float sv[8];
#pragma unroll
      for (int g = 0; g < 8; ++g) sv[g] = Pl[g * 64];
#pragma unroll
      for (int g = 0; g < 8; ++g) { const float p = __builtin_amdgcn_exp2f((sv[g] - msub) * SC2); ls += p; Pl[g * 64] = p; }
    }
    ls += dpp<XOR2>(ls); ls += dpp<ROR8>(ls); ls += dpp<ROR4>(ls);
    ls = sw16_add(ls, ls); ls = sw32_add(ls, ls);
    *(LAS v2f*)(L + (lane < 2 ? L_ML + (wv * 2 + lane) * 8 : L_SINK + 1024 + lane * 8)) = v2f{mx, ls};
    lds_signal(L, FL_ML, lane, 1u);
    v2f a0[4], a1[4];
#pragma unroll
    for (int k = 0; k < 4; ++k) { a0[k] = v2f{0.f, 0.f}; a1[k] = v2f{0.f, 0.f}; }
    const LAS v4f* Pr = (const LAS v4f*)(P + hf * 8);
    LAS v4f* mo = (LAS v4f*)(L + L_MRG + wv * 2048 + lane * 32);
    auto v_group = [&](int g, auto last) {
      v4f pa[4], pb[4];
#pragma unroll
      for (int d = 0; d < 4; ++d) { pa[d] = Pr[(g * 4 + d) * 4]; pb[d] = Pr[(g * 4 + d) * 4 + 1]; }
#pragma unroll
      for (int d = 0; d < 4; ++d) {
        if (!last.value || d + LEAD < 4) {
          LANDED();
          pace2(tb, 32 + g * 4 + d + LEAD);
          issue_turn(R[(d + LEAD) & 3], rv, voff, ROW(g * 4 + d + LEAD));
        }
        v_turn(R[d], pa[d], pb[d], a0, a1);
        SB();
        if (!last.value && d == 1) lds_signal(L, FL_PRE, lane, g == PREG ? 1u : 0u);
      }
    };
#pragma unroll 1
    for (int g = 0; g < 7; ++g) v_group(g, BoolC<false>{});
    v_group(7, BoolC<true>{});
    mo[0] = v4f{sw32_add(a0[0].x, a1[0].x), sw32_add(a0[0].y, a1[0].y), sw32_add(a0[1].x, a1[1].x), sw32_add(a0[1].y, a1[1].y)};
    mo[1] = v4f{sw32_add(a0[2].x, a1[2].x), sw32_add(a0[2].y, a1[2].y), sw32_add(a0[3].x, a1[3].x), sw32_add(a0[3].y, a1[3].y)};
    lds_signal(L, FL_DONE, lane, 1u);
    if (isM) {
      const __amdgpu_buffer_rsrc_t r2 = __builtin_amdgcn_make_buffer_rsrc((void*)(w2 + m * 64 * D), 0, 64 * D * 2, RSRC3);
      lds_wait_ge(FL + FL_PUB, 2u, 0);
      __builtin_amdgcn_s_sleep(2);
      v4u uu[16];
#pragma unroll
      for (int i = 0; i < 16; ++i) uu[i] = LD128(r2, lane < 56 ? lane * 16 : 64 * D * 2, (i * 4 + wv) * (D * 2), AUX_NT);
      const int aw = lane < 56 ? L_RAW2 + wv * W2P + lane * 16 : L_SINK + lane * 16, aws = lane < 56 ? 4 * W2P : 0;
#pragma unroll
      for (int i = 0; i < 16; ++i) *(LAS v4u*)(L + aw + i * aws) = uu[i];
      const unsigned tagl = __builtin_amdgcn_readfirstlane(FL[FL_TAGL]), th = __builtin_amdgcn_readfirstlane(FL[FL_TAGH]);
      const __amdgpu_buffer_rsrc_t rt = __builtin_amdgcn_make_buffer_rsrc((void*)(ws + (size_t)((th & 31u) + 1u) * WS_SPAN), 0, WS_END, RSRC3);
      const int g = lane >> 4;
      int p_st = WS_PF + ((m * 28 + wv * 7) * 8 + (lane & 15)) * 64 + g * 16;
      int a_br = L_BF + (lane & 15) * 144 + g * 16;
      int a2 = L_RAW2 + (g * 4 + ((lane & 15) >> 2)) * W2P + wv * 224 + (lane & 3) * 8;
      asm volatile("" : "+v"(p_st), "+v"(a_br), "+v"(a2));
      mlp_ln(L, rt, wv, lane, tagl, tb);
      __builtin_amdgcn_s_sleep(W2D);
      v4u Fr[28];
#pragma unroll
      for (int i = 0; i < 14; ++i) Fr[14 + i] = frag_tr(L + a2 + (i & 1) * 32 * W2P + (i >> 1) * 32, 16 * W2P);
      BAR();
      const v4u h0 = *(const LAS v4u*)(L + a_br), h1 = *(const LAS v4u*)(L + a_br + 64);
      const bool up = lane & 8;
      v4u ev[4];
#pragma unroll
      for (int i = 0; i < 4; ++i) {
        v4f ea = mfma(Fr[14 + 4 * i], h0, v4f{0.f, 0.f, 0.f, 0.f}), eb = ea;
        if (i < 3) eb = mfma(Fr[14 + 4 * i + 2], h0, v4f{0.f, 0.f, 0.f, 0.f});
        ea = mfma(Fr[14 + 4 * i + 1], h1, ea);
        if (i < 3) eb = mfma(Fr[14 + 4 * i + 3], h1, eb);
        ev[i] = pack24(v4f{up ? eb.x : ea.x, up ? eb.y : ea.y, up ? eb.z : ea.z, up ? eb.w : ea.w}, tagl);
        ST128(ev[i], rt, (i == 3 && up) ? WS_END : p_st + i * 1024, 0, AUX_SC1);
      }
      asm volatile("" :: "v"(ev[0]), "v"(ev[1]), "v"(ev[2]), "v"(ev[3]));
    }
  }
}
}

extern "C" void kernel_launch(void* const* d_in, const int* in_sizes, int n_in, void* d_out, int out_size, void* d_ws,
                              size_t ws_size, hipStream_t stream) {
  using namespace fk;
  (void)hipFuncSetAttribute((const void*)fused_decode, hipFuncAttributeMaxDynamicSharedMemorySize, L_END);
  hipLaunchKernelGGL(fused_decode, dim3(256), dim3(512), L_END, stream, (const u16*)d_in[0], (const u16*)d_in[1],
                     (const u16*)d_in[2], (const u16*)d_in[3], (const u16*)d_in[4], (const u16*)d_in[6], (const u16*)d_in[7],
                     (const u16*)d_in[8], (const u16*)d_in[9], (u16*)d_out, (unsigned char*)d_ws);
}
```
